```python
import torch

def _softmax(x, axis=-1):
    return torch.softmax(x, dim=axis)
def _gelu(x):
    return torch.nn.functional.gelu(x, approximate="tanh")
def _silu(x):
    return torch.nn.functional.silu(x)
def _sigmoid(x):
    return torch.sigmoid(x)
def _layernorm(x, axis=-1, eps=1e-5):
    m = x.mean(dim=axis, keepdim=True)
    v = x.var(dim=axis, keepdim=True, unbiased=False)
    return (x - m) / torch.sqrt(v + eps)
def _scatter_add(tgt, idx, src, axis=0):
    return tgt.clone().index_add_(axis, idx, src)
def _segment_sum(src, seg, n):
    out = torch.zeros((n,) + src.shape[1:], dtype=src.dtype)
    return out.index_add_(0, seg, src)
def _pad(x, pw):
    flat = tuple(v for pair in reversed(pw) for v in pair)
    return torch.nn.functional.pad(x, flat)

kernel_name = 'gated_mix__sliding_block_cached__dense_activation_silu__decode'

def setup_inputs():

    _w = (lambda a: (lambda t: t.to(torch.bfloat16) if t.is_floating_point() else t)(torch.as_tensor(a)))
    def _A_setup():
        B, Sq, D = 8, 1, 448
        Hq, Hkv, Sk, Dh = 16, 8, 4096, 256
        assert Hq % Hkv == 0, (Hq, Hkv)
        return {
            "x": _w((lambda *s: torch.randn(*s, dtype=torch.bfloat16) * 0.2)(B, Sq, D)),
            "k_cache": _w((lambda *s: torch.randn(*s, dtype=torch.bfloat16) * 3.2)(B, Hkv, Sk, Dh)),
            "v_cache": _w((lambda *s: torch.randn(*s, dtype=torch.bfloat16) * 3.2)(B, Hkv, Sk, Dh)),
            "wq": _w((lambda *s: torch.randn(*s, dtype=torch.bfloat16) * 3.2)(D, Hq * Dh) * (D ** -0.5)),
            "wo": _w((lambda *s: torch.randn(*s, dtype=torch.bfloat16) * 3.2)(Hq * Dh, D) * ((Hq * Dh) ** -0.5)),
        }
    _A_in = _A_setup()

    def _B_setup():
        B, S, D, F = 8, 1, 448, 1792
        return {
            "x": _w((lambda *s: torch.randn(*s, dtype=torch.bfloat16) * 0.2)(B, S, D)),
            "w1": _w((lambda *s: torch.randn(*s, dtype=torch.bfloat16) * 3.2)(D, F) * (D ** -0.5)),
            "w2": _w((lambda *s: torch.randn(*s, dtype=torch.bfloat16) * 3.2)(F, D) * (F ** -0.5)),
        }
    _B_in = _B_setup()

    return {
        "A_x": _A_in["x"],
        "A_k_cache": _A_in["k_cache"],
        "A_v_cache": _A_in["v_cache"],
        "A_wq": _A_in["wq"],
        "A_wo": _A_in["wo"],
        "B_x": _B_in["x"],
        "B_w1": _B_in["w1"],
        "B_w2": _B_in["w2"],
        "gate_w": _w((lambda *s: torch.randn(*s, dtype=torch.bfloat16) * 0.2)(448) * 16.0),
    }

def reference(A_x, A_k_cache, A_v_cache, A_wq, A_wo, B_x, B_w1, B_w2, gate_w):
    def _A_ref(x, k_cache, v_cache, wq, wo):
        B, Sq, D = x.shape
        Hq, Dh = 16, 256
        Hkv, Sk = k_cache.shape[1], k_cache.shape[2]
        W = min(4096, max(256, Sk // 4))
        rep = Hq // Hkv
        q = (lambda x, *d: x.permute(*d))((x @ wq).reshape(B, Sq, Hq, Dh), 0, 2, 1, 3)
        k = (lambda x, n, axis: torch.repeat_interleave(x, n, dim=axis))(k_cache[:, :, Sk - W:, :], rep, axis=1)
        v = (lambda x, n, axis: torch.repeat_interleave(x, n, dim=axis))(v_cache[:, :, Sk - W:, :], rep, axis=1)
        scores = torch.einsum("bhqd,bhkd->bhqk", q, k) * (Dh ** -0.5)
        p = _softmax(scores, axis=-1)
        o = torch.einsum("bhqk,bhkd->bhqd", p, v)
        return (lambda x, *d: x.permute(*d))(o, 0, 2, 1, 3).reshape(B, Sq, Hq * Dh) @ wo

    def _B_ref(x, w1, w2):
        h = torch.einsum("bsd,df->bsf", x, w1)
        h = _silu(h)
        out = torch.einsum("bsf,fd->bsd", h, w2)
        return out

    x = A_x
    g = _sigmoid((x * gate_w).sum(-1, keepdims=True))
    a = _A_ref(x, A_k_cache, A_v_cache, A_wq, A_wo)
    b = _B_ref(x, B_w1, B_w2)
    return g * a + (1 - g) * b
```

```cpp
#include <hip/hip_runtime.h>
#include <hip/hip_bf16.h>
#include <stdint.h>

typedef unsigned u32x4 __attribute__((ext_vector_type(4)));
typedef float f32x4 __attribute__((ext_vector_type(4)));
typedef unsigned short u16;

constexpr int B = 8, D = 448, HQ = 16, HKV = 8, SK = 4096, DH = 256, W = 1024, F = 1792;
constexpr int NSPLIT = 4;

__device__ __forceinline__ float bf2f(u16 v) { return __uint_as_float((unsigned)v << 16); }
__device__ __forceinline__ float rbf(float f) { return __bfloat162float(__float2bfloat16(f)); }

__global__ void __launch_bounds__(256) k_pre(const u16* x, const u16* wq, const u16* w1, float* q, float* h) {
  __shared__ float xs[B * D];
  for (int i = threadIdx.x; i < B * D; i += 256) xs[i] = bf2f(x[i]);
  __syncthreads();
  int col = blockIdx.x * 64 + (threadIdx.x & 63);
  int kq = threadIdx.x >> 6;
  __shared__ float red[4][B][64];
  const u16* wp; int ld; int c;
  bool isq = col < HQ * DH;
  if (isq) { wp = wq; ld = HQ * DH; c = col; } else { wp = w1; ld = F; c = col - HQ * DH; }
  float acc[B] = {0};
  for (int k = kq * 112; k < kq * 112 + 112; ++k) {
    float w = bf2f(wp[(size_t)k * ld + c]);
#pragma unroll
    for (int b = 0; b < B; ++b) acc[b] += xs[b * D + k] * w;
  }
#pragma unroll
  for (int b = 0; b < B; ++b) red[kq][b][threadIdx.x & 63] = acc[b];
  __syncthreads();
  if (kq == 0) {
#pragma unroll
    for (int b = 0; b < B; ++b) {
      int l = threadIdx.x & 63;
      float s = red[0][b][l] + red[1][b][l] + red[2][b][l] + red[3][b][l];
      s = rbf(s);
      if (isq) q[b * HQ * DH + c] = s;
      else { float y = s / (1.f + __expf(-s)); h[b * F + c] = rbf(y); }
    }
  }
}

__global__ void __launch_bounds__(256) k_attn(const u16* kc, const u16* vc, const float* q, float* po, float* pml) {
  int s = blockIdx.x, kvh = blockIdx.y, b = blockIdx.z;
  __shared__ float qs[2][DH];
  __shared__ float sc[2][256];
  __shared__ float redm[2][4], redl[2][4];
  int tid = threadIdx.x, lane = tid & 63, wave = tid >> 6;
  for (int i = tid; i < 2 * DH; i += 256) qs[i / DH][i % DH] = q[b * HQ * DH + kvh * 2 * DH + i];
  __syncthreads();
  const u16* kb = kc + ((size_t)(b * HKV + kvh) * SK + (SK - W) + s * 256) * DH;
  const u16* vb = vc + ((size_t)(b * HKV + kvh) * SK + (SK - W) + s * 256) * DH;
  int half = lane >> 5, c = lane & 31;
  for (int i = 0; i < 32; ++i) {
    int key = wave * 64 + i * 2 + half;
    u32x4 kv = *(const u32x4*)(kb + (size_t)key * DH + c * 8);
    float d0 = 0, d1 = 0;
#pragma unroll
    for (int j = 0; j < 4; ++j) {
      float lo = __uint_as_float(kv[j] << 16), hi = __uint_as_float(kv[j] & 0xffff0000u);
      d0 += lo * qs[0][c * 8 + 2 * j] + hi * qs[0][c * 8 + 2 * j + 1];
      d1 += lo * qs[1][c * 8 + 2 * j] + hi * qs[1][c * 8 + 2 * j + 1];
    }
#pragma unroll
    for (int o = 16; o > 0; o >>= 1) { d0 += __shfl_xor(d0, o); d1 += __shfl_xor(d1, o); }
    if (c == 0) { sc[0][key] = rbf(d0) * 0.0625f; sc[1][key] = rbf(d1) * 0.0625f; }
  }
  __syncthreads();
  float s0 = sc[0][tid], s1 = sc[1][tid];
  float m0 = s0, m1 = s1;
#pragma unroll
  for (int o = 32; o > 0; o >>= 1) { m0 = fmaxf(m0, __shfl_xor(m0, o)); m1 = fmaxf(m1, __shfl_xor(m1, o)); }
  if (lane == 0) { redm[0][wave] = m0; redm[1][wave] = m1; }
  __syncthreads();
  m0 = fmaxf(fmaxf(redm[0][0], redm[0][1]), fmaxf(redm[0][2], redm[0][3]));
  m1 = fmaxf(fmaxf(redm[1][0], redm[1][1]), fmaxf(redm[1][2], redm[1][3]));
  float p0 = __expf(s0 - m0), p1 = __expf(s1 - m1);
  float l0 = p0, l1 = p1;
#pragma unroll
  for (int o = 32; o > 0; o >>= 1) { l0 += __shfl_xor(l0, o); l1 += __shfl_xor(l1, o); }
  if (lane == 0) { redl[0][wave] = l0; redl[1][wave] = l1; }
  sc[0][tid] = p0; sc[1][tid] = p1;
  __syncthreads();
  l0 = redl[0][0] + redl[0][1] + redl[0][2] + redl[0][3];
  l1 = redl[1][0] + redl[1][1] + redl[1][2] + redl[1][3];
  float a0[8] = {0}, a1[8] = {0};
  for (int i = 0; i < 32; ++i) {
    int key = wave * 64 + i * 2 + half;
    u32x4 vv = *(const u32x4*)(vb + (size_t)key * DH + c * 8);
    float q0 = sc[0][key], q1 = sc[1][key];
#pragma unroll
    for (int j = 0; j < 4; ++j) {
      float lo = __uint_as_float(vv[j] << 16), hi = __uint_as_float(vv[j] & 0xffff0000u);
      a0[2 * j] += q0 * lo; a0[2 * j + 1] += q0 * hi;
      a1[2 * j] += q1 * lo; a1[2 * j + 1] += q1 * hi;
    }
  }
  __shared__ float ored[8][2][DH];
#pragma unroll
  for (int j = 0; j < 8; ++j) { ored[wave * 2 + half][0][c * 8 + j] = a0[j]; ored[wave * 2 + half][1][c * 8 + j] = a1[j]; }
  __syncthreads();
  for (int i = tid; i < 2 * DH; i += 256) {
    int hh = i / DH, d = i % DH;
    float t = 0;
#pragma unroll
    for (int w = 0; w < 8; ++w) t += ored[w][hh][d];
    po[(((size_t)b * HQ + kvh * 2 + hh) * NSPLIT + s) * DH + d] = t;
  }
  if (tid == 0) {
    size_t r0 = (((size_t)b * HQ + kvh * 2) * NSPLIT + s) * 2;
    size_t r1 = (((size_t)b * HQ + kvh * 2 + 1) * NSPLIT + s) * 2;
    pml[r0] = m0; pml[r0 + 1] = l0; pml[r1] = m1; pml[r1 + 1] = l1;
  }
}

__global__ void __launch_bounds__(1024) k_post(const u16* x, const u16* gw, const u16* wo, const u16* w2, const float* h,
                                               const float* po, const float* pml, __hip_bfloat16* out) {
  int b = blockIdx.y, n0 = blockIdx.x * 64;
  __shared__ float os[HQ * DH];
  __shared__ float hs[F];
  __shared__ float red[16][2][64];
  __shared__ float gs;
  int tid = threadIdx.x;
  for (int i = tid; i < HQ * DH; i += 1024) {
    int hh = i / DH, d = i % DH;
    const float* ml = pml + (size_t)(b * HQ + hh) * NSPLIT * 2;
    float M = fmaxf(fmaxf(ml[0], ml[2]), fmaxf(ml[4], ml[6]));
    float L = 0, o = 0;
#pragma unroll
    for (int s = 0; s < NSPLIT; ++s) { float w = __expf(ml[2 * s] - M); L += w * ml[2 * s + 1]; o += w * po[((size_t)(b * HQ + hh) * NSPLIT + s) * DH + d]; }
    os[i] = rbf(o / L);
  }
  for (int i = tid; i < F; i += 1024) hs[i] = h[b * F + i];
  if (tid < 64) {
    float t = 0;
    for (int i = tid; i < D; i += 64) t += rbf(bf2f(x[b * D + i]) * bf2f(gw[i]));
#pragma unroll
    for (int o = 32; o > 0; o >>= 1) t += __shfl_xor(t, o);
    if (tid == 0) { t = rbf(t); gs = rbf(1.f / (1.f + __expf(-t))); }
  }
  __syncthreads();
  int l = tid & 63, g = tid >> 6;
  float a = 0, bb = 0;
  for (int k = g * 256; k < g * 256 + 256; ++k) a += os[k] * bf2f(wo[(size_t)k * D + n0 + l]);
  for (int k = g * 112; k < g * 112 + 112; ++k) bb += hs[k] * bf2f(w2[(size_t)k * D + n0 + l]);
  red[g][0][l] = a; red[g][1][l] = bb;
  __syncthreads();
  if (tid < 64) {
    float A = 0, Bv = 0;
    for (int i = 0; i < 16; ++i) { A += red[i][0][l]; Bv += red[i][1][l]; }
    A = rbf(A); Bv = rbf(Bv);
    float gg = gs;
    float r = rbf(gg * A) + rbf(rbf(1.f - gg) * Bv);
    out[b * D + n0 + l] = __float2bfloat16(r);
  }
}

extern "C" void kernel_launch(void* const* d_in, const int* in_sizes, int n_in, void* d_out, int out_size, void* d_ws, size_t ws_size,
                              hipStream_t stream) {
  const u16* x = (const u16*)d_in[0];
  const u16* kc = (const u16*)d_in[1];
  const u16* vc = (const u16*)d_in[2];
  const u16* wq = (const u16*)d_in[3];
  const u16* wo = (const u16*)d_in[4];
  const u16* w1 = (const u16*)d_in[6];
  const u16* w2 = (const u16*)d_in[7];
  const u16* gw = (const u16*)d_in[8];
  float* ws = (float*)d_ws;
  float* q = ws;
  float* h = q + B * HQ * DH;
  float* po = h + B * F;
  float* pml = po + B * HQ * NSPLIT * DH;
  k_pre<<<(HQ * DH + F) / 64, 256, 0, stream>>>(x, wq, w1, q, h);
  k_attn<<<dim3(NSPLIT, HKV, B), 256, 0, stream>>>(kc, vc, q, po, pml);
  k_post<<<dim3(7, B), 1024, 0, stream>>>(x, gw, wo, w2, h, po, pml, (__hip_bfloat16*)d_out);
}
```

```cpp
#include <hip/hip_runtime.h>
#include <hip/hip_bf16.h>
#include <stdint.h>

#define STAMPS 0
#define QHINT 0
#define QSLEEP 20
#define WK 3
#define WV 2
#define WA 1
#define WW 2
#define NLDS 8

typedef unsigned u32x4 __attribute__((ext_vector_type(4)));
typedef unsigned u32x2 __attribute__((ext_vector_type(2)));
typedef float f32x4 __attribute__((ext_vector_type(4)));
typedef float f32x2 __attribute__((ext_vector_type(2)));
typedef short s16x4 __attribute__((ext_vector_type(4)));
typedef short s16x8 __attribute__((ext_vector_type(8)));
typedef __bf16 bf16x2 __attribute__((ext_vector_type(2)));
typedef __bf16 bf16x8 __attribute__((ext_vector_type(8)));
typedef unsigned short u16;
#define LAS __attribute__((address_space(3)))
#define GAS __attribute__((address_space(1)))

constexpr int NB = 8, DM = 448, HKV = 8, SK = 4096, DH = 256, WIN = 1024, FF = 1792, NQ = 4096;
constexpr int WS_EPOCH = 0;
constexpr int WS_QF = 4096;
constexpr int WS_HF = WS_QF + 2048;
constexpr int QSTR = 16384 + 512, HSTR = 4096 + 512, PSTR = 4096 + 128;
constexpr int WS_Q = 16384;
constexpr int WS_H = WS_Q + 64 * QSTR;
constexpr int WS_S = WS_H + 224 * HSTR;
constexpr int WS_PO = WS_S + 524288;
constexpr int WS_END = WS_PO + 256 * PSTR;
constexpr int WS_STAMP = 4 << 20;
constexpr int L_FLAG = 0, L_Q = 256, L_H = L_Q + 1024, L_P = L_H + 256, L_OP = L_P + 4096, L_OX = L_OP + 4096, L_WQ = L_OX + 2048,
              L_W1 = L_WQ + 14336, L_WO = L_W1 + 8192, L_END = L_WO + 7 * NLDS * 1024 + 16;
enum { F_TAGV = 0, F_TAG = 1, F_QRDY = 2, F_PRDY = 3, F_KCNT = 4, F_G = 5, F_HRDY = 6, F_KISS = 7, F_WOC = 8 };
constexpr int AUX_NT = 2, AUX_SC1 = 16;
constexpr unsigned SPIN_CAP = 1u << 18;

#define RSRC(p, n) __builtin_amdgcn_make_buffer_rsrc((void*)(p), 0, (n), 0x00020000)
#define LD128(r, off, aux) __builtin_amdgcn_raw_buffer_load_b128((r), (off), 0, (aux))
#define ST128(v, r, off, aux) __builtin_amdgcn_raw_buffer_store_b128((v), (r), (off), 0, (aux))
#define SCHED_FENCE() __builtin_amdgcn_sched_barrier(0)
#define MEM_FENCE() asm volatile("" ::: "memory")
template <int N> __device__ __forceinline__ void vmwait() { asm volatile("s_waitcnt vmcnt(%0)" ::"n"(N) : "memory"); }
#define LGKM0() asm volatile("s_waitcnt lgkmcnt(0)" ::: "memory")

__device__ __forceinline__ f32x4 mfma(u32x4 a, u32x4 b, f32x4 c) {
  return __builtin_amdgcn_mfma_f32_16x16x32_bf16(__builtin_bit_cast(bf16x8, a), __builtin_bit_cast(bf16x8, b), c, 0, 0, 0);
}
__device__ __forceinline__ u32x2 trd(LAS unsigned char* p) {
  return __builtin_bit_cast(u32x2, __builtin_amdgcn_ds_read_tr16_b64_v4i16((LAS s16x4*)p));
}
__device__ __forceinline__ unsigned pk_bf16(float lo, float hi) {
  f32x2 v = {lo, hi};
  return __builtin_bit_cast(unsigned, __builtin_convertvector(v, bf16x2));
}
__device__ __forceinline__ float bf_lo(unsigned u) { return __uint_as_float(u << 16); }
__device__ __forceinline__ float bf_hi(unsigned u) { return __uint_as_float(u & 0xffff0000u); }
__device__ __forceinline__ float rbf(float f) { return bf_lo(pk_bf16(f, f)); }
template <int CTRL> __device__ __forceinline__ float dpp(float x) {
  return __builtin_bit_cast(float, __builtin_amdgcn_mov_dpp(__builtin_bit_cast(int, x), CTRL, 0xf, 0xf, true));
}
constexpr int XOR1 = 0xB1, XOR2 = 0x4E, HMIRROR = 0x141, ROR8 = 0x128;
__device__ __forceinline__ float x16_a(float x, float& o) {
  auto s = __builtin_amdgcn_permlane16_swap(__float_as_uint(x), __float_as_uint(x), false, false);
  o = __uint_as_float(s[1]);
  return __uint_as_float(s[0]);
}
__device__ __forceinline__ float x32_a(float x, float& o) {
  auto s = __builtin_amdgcn_permlane32_swap(__float_as_uint(x), __float_as_uint(x), false, false);
  o = __uint_as_float(s[1]);
  return __uint_as_float(s[0]);
}
__device__ __forceinline__ float wave_max(float x) {
  x = fmaxf(x, dpp<XOR1>(x)); x = fmaxf(x, dpp<XOR2>(x)); x = fmaxf(x, dpp<HMIRROR>(x)); x = fmaxf(x, dpp<ROR8>(x));
  float o, a = x16_a(x, o); x = fmaxf(a, o);
  a = x32_a(x, o); return fmaxf(a, o);
}
__device__ __forceinline__ float wave_sum(float x) {
  x += dpp<XOR1>(x); x += dpp<XOR2>(x); x += dpp<HMIRROR>(x); x += dpp<ROR8>(x);
  float o, a = x16_a(x, o); x = a + o;
  a = x32_a(x, o); return a + o;
}
__device__ __forceinline__ void lds_wait_ge(volatile LAS unsigned* f, unsigned want) {
  for (unsigned n = 0; n < (1u << 22); ++n) {
    if (__builtin_amdgcn_readfirstlane(*f) >= want) break;
    __builtin_amdgcn_s_sleep(1);
  }
  MEM_FENCE();
}
__device__ __forceinline__ void lds_inc(LAS unsigned char* L, int f, int lane) {
  if (lane == 0) __hip_atomic_fetch_add((LAS unsigned*)L + f, 1u, __ATOMIC_RELAXED, __HIP_MEMORY_SCOPE_WORKGROUP);
}
__device__ __forceinline__ void dma16(__amdgpu_buffer_rsrc_t r, int voff, unsigned ldsdst) {
  unsigned keep;
  asm volatile("s_mov_b32 %0, m0\n\ts_mov_b32 m0, %3\n\ts_nop 0\n\tbuffer_load_dwordx4 %1, %2, 0 offen nt lds\n\ts_mov_b32 m0, %0"
               : "=&s"(keep) : "v"(voff), "s"(r), "s"(ldsdst) : "memory");
}
struct Sel { unsigned lo[4], hi[4]; };
__device__ __forceinline__ Sel make_sel(int lane) {
  Sel s;
  const int j = lane & 7;
#pragma unroll
  for (int t = 0; t < 4; ++t) {
    const bool on = (j >> 1) == t;
    s.lo[t] = on ? ((j & 1) ? 0x01000c0cu : 0x0c0c0100u) : 0x0c0c0c0cu;
    s.hi[t] = on ? ((j & 1) ? 0x03020c0cu : 0x0c0c0302u) : 0x0c0c0c0cu;
  }
  return s;
}
__device__ __forceinline__ u32x4 place(unsigned src, const unsigned (&sel)[4]) {
  u32x4 r;
#pragma unroll
  for (int t = 0; t < 4; ++t) r[t] = __builtin_amdgcn_perm(src, src, sel[t]);
  return r;
}

#define STAMP(i) do {} while (0)

__global__ void __launch_bounds__(1024) fused_kernel(const u16* __restrict__ x, const u16* __restrict__ kc, const u16* __restrict__ vc,
                                                     const u16* __restrict__ wq, const u16* __restrict__ wo, const u16* __restrict__ w1,
                                                     const u16* __restrict__ w2, const u16* __restrict__ gw, u16* __restrict__ out,
                                                     unsigned char* __restrict__ ws) {
  __shared__ __attribute__((aligned(16))) unsigned char smem[L_END];
  LAS unsigned char* L = (LAS unsigned char*)smem;
  volatile LAS unsigned* flag = (volatile LAS unsigned*)(L + L_FLAG);
  STAMP(0);
  const int bid = blockIdx.x, kvh = bid & 7, b = (bid >> 3) & 7, s = bid >> 6;
  const int tid = threadIdx.x, lane = tid & 63, wave = __builtin_amdgcn_readfirstlane(tid >> 6);
  const int grp = kvh * 4 + s;
  const int fgrp = s < 3 ? kvh * 3 + s : (kvh < 4 ? 24 + kvh : -1);
  const size_t kvbase = ((size_t)(b * HKV + kvh) * SK + (SK - WIN)) * DH;
  const auto rws = RSRC(ws, WS_END);
  if (tid < 16) flag[tid] = 0u;
  asm volatile("s_waitcnt lgkmcnt(0)" ::"s"(x), "s"(kc), "s"(vc), "s"(wq), "s"(wo), "s"(w1), "s"(w2), "s"(gw), "s"(out), "s"(ws) : "memory");
  if (wave >= 2 || (wave == 1 && (fgrp < 0 || b == 7))) asm volatile("s_barrier" ::: "memory");

  if (wave == 0) {
    __builtin_amdgcn_s_setprio(3);
    unsigned epoch, tag;
    asm volatile("s_load_dword %0, %1, 0x0" : "=s"(epoch) : "s"(ws) : "memory");
    {
      const int cb = ((bid >> 3) & 15) + 16 * (kvh >> 1), rb = (bid >> 7) + 2 * (kvh & 1);
      const auto rq = RSRC(wq, DM * NQ * 2);
      u32x4 wr[14], xa[3];
      const int qoffs = ((rb * 112 + (lane >> 3)) * NQ + cb * 64 + (lane & 7) * 8) * 2;
#pragma unroll
      for (int i = 0; i < 14; ++i) wr[i] = LD128(rq, qoffs + i * 8 * NQ * 2, AUX_NT);
      const u16* xp = x + (lane & 7) * DM + rb * 112;
#pragma unroll
      for (int i = 0; i < 3; ++i) xa[i] = *(const u32x4*)(xp + i * 32 + (lane >> 4) * 8);
      const u32x2 xb = *(const u32x2*)(xp + 96 + (lane >> 4) * 4);
      SCHED_FENCE();
      asm volatile("s_barrier\n\ts_waitcnt lgkmcnt(0)" : "+s"(epoch)::"memory");
      tag = epoch * 1664525u + 1013904223u;
      if (lane == 0) flag[F_TAG] = tag;
      LGKM0();
      if (lane == 0) flag[F_TAGV] = 1u;
#pragma unroll
      for (int i = 0; i < 14; ++i) *(LAS u32x4*)(L + L_WQ + i * 1024 + lane * 16) = wr[i];
      STAMP(1);
      LGKM0();
      LAS unsigned char* tb = L + L_WQ + ((lane >> 4) * 8 + ((lane >> 2) & 3)) * 128 + (lane & 3) * 8;
      LAS unsigned char* tb2 = L + L_WQ + (96 + (lane >> 4) * 4 + ((lane >> 2) & 3)) * 128 + (lane & 3) * 8;
      f32x4 acc[4];
#pragma unroll
      for (int ct = 0; ct < 4; ++ct) {
        const u32x2 pt = trd(tb2 + ct * 32);
        acc[ct] = mfma(u32x4{xb[0], xb[1], 0u, 0u}, u32x4{pt[0], pt[1], 0u, 0u}, f32x4{0, 0, 0, 0});
#pragma unroll
        for (int ks = 0; ks < 3; ++ks) {
          const u32x2 p0 = trd(tb + ks * 4096 + ct * 32), p1 = trd(tb + ks * 4096 + 512 + ct * 32);
          acc[ct] = mfma(xa[ks], u32x4{p0[0], p0[1], p1[0], p1[1]}, acc[ct]);
        }
      }
      const int qo = WS_Q + ((cb >> 3) * 8 + ((lane >> 4) & 1) * 4 + (lane >> 5) * 2) * QSTR + rb * 4096 + ((cb & 7) * 32 + (lane & 15)) * 16;
#pragma unroll
      for (int cp = 0; cp < 2; ++cp)
#pragma unroll
        for (int rr = 0; rr < 2; ++rr) {
          const u32x4 qv = {__float_as_uint(lane >= 32 ? acc[2 * cp][rr + 2] : acc[2 * cp][rr]), tag,
                            __float_as_uint(lane >= 32 ? acc[2 * cp + 1][rr + 2] : acc[2 * cp + 1][rr]), tag};
          ST128(qv, rws, qo + rr * QSTR + cp * 256, AUX_SC1);
        }
    }
    STAMP(2);
    u32x4 xg = {0, 0, 0, 0}, gg = {0, 0, 0, 0};
    if (lane < 56) {
      xg = *(const u32x4*)(x + b * DM + lane * 8);
      gg = *(const u32x4*)(gw + lane * 8);
    }
    {
      float z = 0.f;
#pragma unroll
      for (int t = 0; t < 4; ++t) z += rbf(bf_lo(xg[t]) * bf_lo(gg[t])) + rbf(bf_hi(xg[t]) * bf_hi(gg[t]));
      z = rbf(wave_sum(z));
      const float g = rbf(1.f / (1.f + __expf(-z)));
      if (lane == 0) flag[F_G] = __float_as_uint(g);
    }
    __builtin_amdgcn_s_sleep(QSLEEP);
    STAMP(6);
    {
      const int qoff = WS_Q + (kvh * 8 + b) * QSTR + lane * 16;
      u32x4 qg[16];
      for (unsigned n = 0; n < SPIN_CAP; ++n) {
        MEM_FENCE();
        bool ok = true;
#pragma unroll
        for (int i = 0; i < 16; ++i) qg[i] = LD128(rws, qoff + i * 1024, AUX_SC1);
#pragma unroll
        for (int i = 0; i < 16; ++i) ok = ok && qg[i][1] == tag && qg[i][3] == tag;
        if (__all(ok)) break;
      }
#pragma unroll
      for (int i = 0; i < 4; ++i) {
        const float a0 = (__uint_as_float(qg[i][0]) + __uint_as_float(qg[4 + i][0])) + (__uint_as_float(qg[8 + i][0]) + __uint_as_float(qg[12 + i][0]));
        const float a1 = (__uint_as_float(qg[i][2]) + __uint_as_float(qg[4 + i][2])) + (__uint_as_float(qg[8 + i][2]) + __uint_as_float(qg[12 + i][2]));
        const unsigned pk = pk_bf16(a0, a1);
        LAS u16* qd = (LAS u16*)(L + L_Q) + i * 128 + (lane >> 4) * 32 + (lane & 15);
        qd[0] = (u16)pk;
        qd[16] = (u16)(pk >> 16);
      }
      LGKM0();
      if (lane == 0) flag[F_QRDY] = 1u;
    }
    STAMP(3);
    lds_wait_ge(flag + F_KCNT, 6u);
    __builtin_amdgcn_s_sleep(10);
    const int soff = WS_S + (b * 8 + kvh) * 8192 + lane * 16;
    u32x4 sg[8];
    for (unsigned n = 0; n < SPIN_CAP; ++n) {
      MEM_FENCE();
      bool ok = true;
#pragma unroll
      for (int i = 0; i < 8; ++i) sg[i] = LD128(rws, soff + i * 1024, AUX_SC1);
#pragma unroll
      for (int i = 0; i < 8; ++i) ok = ok && sg[i][1] == tag && sg[i][3] == tag;
      if (__all(ok)) break;
    }
    STAMP(4);
    float m0 = -INFINITY, m1 = -INFINITY;
#pragma unroll
    for (int i = 0; i < 8; ++i) {
      m0 = fmaxf(m0, fmaxf(bf_lo(sg[i][0]), bf_lo(sg[i][2])));
      m1 = fmaxf(m1, fmaxf(bf_hi(sg[i][0]), bf_hi(sg[i][2])));
    }
    m0 = wave_max(m0); m1 = wave_max(m1);
    constexpr float C2 = 0.0625f * 1.4426950408889634f;
    float e0[16], e1[16], l0 = 0.f, l1 = 0.f;
#pragma unroll
    for (int i = 0; i < 8; ++i) {
      e0[2 * i] = __builtin_amdgcn_exp2f((bf_lo(sg[i][0]) - m0) * C2);
      e0[2 * i + 1] = __builtin_amdgcn_exp2f((bf_lo(sg[i][2]) - m0) * C2);
      e1[2 * i] = __builtin_amdgcn_exp2f((bf_hi(sg[i][0]) - m1) * C2);
      e1[2 * i + 1] = __builtin_amdgcn_exp2f((bf_hi(sg[i][2]) - m1) * C2);
      l0 += e0[2 * i] + e0[2 * i + 1];
      l1 += e1[2 * i] + e1[2 * i + 1];
    }
    l0 = __builtin_amdgcn_rcpf(wave_sum(l0)); l1 = __builtin_amdgcn_rcpf(wave_sum(l1));
#pragma unroll
    for (int i = 0; i < 8; ++i) {
      *(LAS unsigned*)(L + L_P + i * 256 + lane * 4) = pk_bf16(e0[2 * i] * l0, e0[2 * i + 1] * l0);
      *(LAS unsigned*)(L + L_P + 2048 + i * 256 + lane * 4) = pk_bf16(e1[2 * i] * l1, e1[2 * i + 1] * l1);
    }
    LGKM0();
    if (lane == 0) flag[F_PRDY] = 1u;
    STAMP(5);
    asm volatile("s_waitcnt lgkmcnt(0)\n\ts_barrier" ::: "memory");
    const int poff = WS_PO + (b * 32 + grp) * PSTR + lane * 16;
    u32x4 pg[4];
    const bool pad = (lane & 7) == 7;
    for (unsigned n = 0; n < SPIN_CAP; ++n) {
      MEM_FENCE();
      bool ok = true;
#pragma unroll
      for (int i = 0; i < 4; ++i) pg[i] = LD128(rws, poff + i * 1024, AUX_SC1);
#pragma unroll
      for (int i = 0; i < 4; ++i) ok = ok && pg[i][1] == tag && pg[i][3] == tag;
      if (__all(ok || pad)) break;
    }
    float a0 = (__uint_as_float(pg[0][0]) + __uint_as_float(pg[1][0])) + (__uint_as_float(pg[2][0]) + __uint_as_float(pg[3][0]));
    float a1 = (__uint_as_float(pg[0][2]) + __uint_as_float(pg[1][2])) + (__uint_as_float(pg[2][2]) + __uint_as_float(pg[3][2]));
    a0 += dpp<ROR8>(a0); a1 += dpp<ROR8>(a1);
    float o, t;
    t = x16_a(a0, o); a0 = t + o; t = x16_a(a1, o); a1 = t + o;
    t = x32_a(a0, o); a0 = t + o; t = x32_a(a1, o); a1 = t + o;
    if (lane < 7) *(unsigned*)(out + b * DM + grp * 14 + lane * 2) = pk_bf16(a0, a1);
    if (bid == 0 && lane == 0) __hip_atomic_store((GAS unsigned*)(ws + WS_EPOCH), tag, __ATOMIC_RELAXED, __HIP_MEMORY_SCOPE_AGENT);
  } else if (wave < 8) {
    unsigned tag;
    __builtin_amdgcn_s_setprio(2);
    if (wave == 1) {
      if (fgrp < 0 || b == 7) {
        lds_wait_ge(flag + F_TAGV, 1u);
        tag = __builtin_amdgcn_readfirstlane(flag[F_TAG]);
      }
      if (fgrp >= 0) {
        if (b < 7) {
          const auto r1 = RSRC(w1, DM * FF * 2);
          u32x4 wr[8], xa[2];
          const int o1 = ((b * 64 + (lane >> 3)) * FF + fgrp * 64 + (lane & 7) * 8) * 2;
#pragma unroll
          for (int i = 0; i < 8; ++i) wr[i] = LD128(r1, o1 + i * 8 * FF * 2, AUX_NT);
#pragma unroll
          for (int i = 0; i < 2; ++i) xa[i] = *(const u32x4*)(x + (lane & 7) * DM + b * 64 + i * 32 + (lane >> 4) * 8);
          SCHED_FENCE();
          asm volatile("s_barrier" ::: "memory");
          lds_wait_ge(flag + F_TAGV, 1u);
          tag = __builtin_amdgcn_readfirstlane(flag[F_TAG]);
#pragma unroll
          for (int i = 0; i < 8; ++i) *(LAS u32x4*)(L + L_W1 + i * 1024 + lane * 16) = wr[i];
          STAMP(1);
          LGKM0();
          LAS unsigned char* tb = L + L_W1 + ((lane >> 4) * 8 + ((lane >> 2) & 3)) * 128 + (lane & 3) * 8;
          f32x4 acc[4];
#pragma unroll
          for (int ct = 0; ct < 4; ++ct) {
            acc[ct] = f32x4{0, 0, 0, 0};
#pragma unroll
            for (int ks = 0; ks < 2; ++ks) {
              const u32x2 p0 = trd(tb + ks * 4096 + ct * 32), p1 = trd(tb + ks * 4096 + 512 + ct * 32);
              acc[ct] = mfma(xa[ks], u32x4{p0[0], p0[1], p1[0], p1[1]}, acc[ct]);
            }
          }
          const int ho = WS_H + (fgrp * 8 + ((lane >> 4) & 1) * 4 + (lane >> 5) * 2) * HSTR + b * 512 + (lane & 15) * 16;
#pragma unroll
          for (int cp = 0; cp < 2; ++cp)
#pragma unroll
            for (int rr = 0; rr < 2; ++rr) {
              const u32x4 qv = {__float_as_uint(lane >= 32 ? acc[2 * cp][rr + 2] : acc[2 * cp][rr]), tag,
                                __float_as_uint(lane >= 32 ? acc[2 * cp + 1][rr + 2] : acc[2 * cp + 1][rr]), tag};
              ST128(qv, rws, ho + rr * HSTR + cp * 256, AUX_SC1);
            }
        }
        STAMP(2);
        lds_wait_ge(flag + F_QRDY, 1u);
        const int hoff = WS_H + (fgrp * 8 + b) * HSTR + lane * 16;
        u32x4 hg[4];
        for (unsigned n = 0; n < SPIN_CAP; ++n) {
          MEM_FENCE();
          bool ok = true;
#pragma unroll
          for (int i = 0; i < 4; ++i) hg[i] = LD128(rws, hoff + i * 1024, AUX_SC1);
#pragma unroll
          for (int i = 0; i < 3; ++i) ok = ok && hg[i][1] == tag && hg[i][3] == tag;
          ok = ok && (lane >= 32 || (hg[3][1] == tag && hg[3][3] == tag));
          if (__all(ok)) break;
          __builtin_amdgcn_s_sleep(4);
        }
        float a0 = (__uint_as_float(hg[0][0]) + __uint_as_float(hg[1][0])) + (__uint_as_float(hg[2][0]) + (lane < 32 ? __uint_as_float(hg[3][0]) : 0.f));
        float a1 = (__uint_as_float(hg[0][2]) + __uint_as_float(hg[1][2])) + (__uint_as_float(hg[2][2]) + (lane < 32 ? __uint_as_float(hg[3][2]) : 0.f));
        float o, t;
        t = x32_a(a0, o); a0 = rbf(t + o);
        t = x32_a(a1, o); a1 = rbf(t + o);
        a0 = a0 / (1.f + __expf(-a0));
        a1 = a1 / (1.f + __expf(-a1));
        if (lane < 32) {
          const unsigned pk = pk_bf16(a0, a1);
          LAS u16* hd = (LAS u16*)(L + L_H) + (lane >> 4) * 32 + (lane & 15);
          hd[0] = (u16)pk;
          hd[16] = (u16)(pk >> 16);
        }
        LGKM0();
        if (lane == 0) flag[F_HRDY] = 1u;
      }
      STAMP(3);
    } else {
      const int kw = wave - 2, u0 = kw < 2 ? kw * 6 : 2 + kw * 5, nu = kw < 2 ? 6 : 5;
      const int urot = u0 + (bid >> 3) * 3, brot = wave + (bid >> 3);
      const auto rk = RSRC(kc + kvbase + s * 256 * DH, 256 * DH * 2);
      u32x4 r[24];
      const int lo = (lane & 7) * 512 + ((lane >> 3) & 1) * 64 + (lane >> 4) * 16;
      const int lo6 = kw < 2 ? lo : 0x7f000000;
#pragma unroll
      for (int i = 0; i < 24; ++i) {
        if (i >= WK) vmwait<WK - 1>();
        const int so = ((urot + (i >> 2)) & 31) * 4096 + ((brot + i) & 3) * 128;
        r[i] = __builtin_amdgcn_raw_buffer_load_b128(rk, i < 20 ? lo : lo6, so, AUX_NT);
        SCHED_FENCE();
      }
      lds_inc(L, F_KISS, lane);
      STAMP(1);
      lds_wait_ge(flag + F_QRDY, 1u);
      tag = __builtin_amdgcn_readfirstlane(flag[F_TAG]);
      STAMP(2);
      u32x4 qa[4];
#pragma unroll
      for (int t = 0; t < 4; ++t) qa[t] = *(const LAS u32x4*)(L + L_Q + (lane & 1) * 512 + ((brot + t) & 3) * 128 + ((lane >> 1) & 1) * 64 + (lane >> 4) * 16);
      float s0 = 0.f, s1 = 0.f;
#pragma unroll
      for (int u = 0; u < 6; ++u) {
        f32x4 c = {0, 0, 0, 0};
#pragma unroll
        for (int t = 0; t < 4; ++t) c = mfma(qa[t], r[4 * u + t], c);
        float v0 = (lane & 8) ? c[2] : c[0], v1 = (lane & 8) ? c[3] : c[1];
        v0 += dpp<ROR8>(v0); v1 += dpp<ROR8>(v1);
        if ((lane >> 3) == u) { s0 = v0; s1 = v1; }
      }
      if ((lane >> 3) < nu)
        __builtin_amdgcn_raw_buffer_store_b64(u32x2{pk_bf16(s0, s1), tag}, rws,
                                              WS_S + (b * 8 + kvh) * 8192 + (s * 256 + ((urot + (lane >> 3)) & 31) * 8 + (lane & 7)) * 8, 0, AUX_SC1);
      lds_inc(L, F_KCNT, lane);
      STAMP(3);
    }
    SCHED_FENCE();
    __builtin_amdgcn_s_setprio(0);
    const int c = wave - 1, u = lane >> 3;
    u32x4 w2r[8], r[16];
    {
      const auto r2 = RSRC(w2, FF * DM * 2);
      const int off2 = fgrp >= 0 ? (fgrp * 64 + u * 8) * 896 + c * 128 + (lane & 7) * 16 : 0x7f000000;
#pragma unroll
      for (int i = 0; i < 8; ++i) {
        if (i >= WW) vmwait<WW - 1>();
        w2r[i] = LD128(r2, off2 + i * 896, AUX_NT);
        SCHED_FENCE();
      }
    }
    const auto ro = RSRC(wo, NQ * DM * 2);
    const int offo = ((kvh * 2 + (u >> 2)) * 256 + s * 64 + (u & 3) * 16) * 896 + c * 128 + (lane & 7) * 16;
#pragma unroll
    for (int i = NLDS; i < 16; ++i) {
      vmwait<WW - 1>();
      r[i] = LD128(ro, offo + i * 896, AUX_NT);
      SCHED_FENCE();
    }
    STAMP(4);
    if (NLDS) {
      lds_wait_ge(flag + F_WOC, 8u);
#pragma unroll
      for (int i = 0; i < NLDS; ++i) r[i] = *(const LAS u32x4*)(L + L_WO + (c * NLDS + i) * 1024 + lane * 16);
    }
    const Sel sel = make_sel(lane);
    f32x4 accb = {0, 0, 0, 0};
    if (fgrp >= 0) {
      lds_wait_ge(flag + F_HRDY, 1u);
      const u32x4 hv = *(const LAS u32x4*)(L + L_H + u * 16);
#pragma unroll
      for (int i = 0; i < 8; ++i) accb = mfma(place(hv[i >> 1], (i & 1) ? sel.hi : sel.lo), w2r[i], accb);
    }
    lds_wait_ge(flag + F_QRDY, 1u);
    const float g = __uint_as_float(flag[F_G]);
    const float g1 = rbf(1.f - g);
    accb *= g1;
    const int col = c * 64 + (lane & 7) * 8 + ((lane >> 4) & 1) * 4 + (lane >> 5) * 2;
    const int cons = col / 14, pc = (col - cons * 14) >> 1;
    int pooff = WS_PO + (b * 32 + cons) * PSTR + grp * 128 + pc * 16;
    asm volatile("" : "+v"(pooff), "+v"(accb));
#pragma unroll
    for (int i = 0; i < 16; ++i) asm volatile("" : "+v"(r[i]));
    STAMP(5);
    asm volatile("s_waitcnt lgkmcnt(0)\n\ts_barrier" ::: "memory");
    __builtin_amdgcn_s_setprio(3);
    f32x2 ov = {0.f, 0.f};
#pragma unroll
    for (int w = 0; w < 8; ++w) ov += *(const LAS f32x2*)(L + L_OP + w * 512 + lane * 8);
    *(LAS unsigned*)(L + L_OX + c * 256 + lane * 4) = pk_bf16(ov[0], ov[1]);
    LGKM0();
    const u32x4 oa = *(const LAS u32x4*)(L + L_OX + c * 256 + u * 32);
    const u32x4 ob = *(const LAS u32x4*)(L + L_OX + c * 256 + u * 32 + 16);
    f32x4 acca = {0, 0, 0, 0}, accc = {0, 0, 0, 0};
#pragma unroll
    for (int i = 0; i < 8; ++i) {
      acca = mfma(place(oa[i >> 1], (i & 1) ? sel.hi : sel.lo), r[i], acca);
      accc = mfma(place(ob[i >> 1], (i & 1) ? sel.hi : sel.lo), r[8 + i], accc);
    }
    f32x4 t = (acca + accc) * g + accb;
    f32x4 y;
#pragma unroll
    for (int k = 0; k < 4; ++k) {
      const float rr = dpp<ROR8>(t[k]);
      float hi, lo = x32_a(rr, hi);
      y[k] = t[k] + (lane < 32 ? hi : lo);
    }
    const bool valid = ((lane >> 3) & 1) == (lane >> 5);
    if (valid) {
      const u32x4 pv = {__float_as_uint(lane < 32 ? y[0] : y[2]), tag, __float_as_uint(lane < 32 ? y[1] : y[3]), tag};
      ST128(pv, rws, pooff, AUX_SC1);
    }
    STAMP(6);
  } else {
    const int w = wave - 8;
    __builtin_amdgcn_s_setprio(1);
    if (NLDS) {
      const auto ro = RSRC(wo, NQ * DM * 2);
      const int u = lane >> 3;
      const int offo = ((kvh * 2 + (u >> 2)) * 256 + s * 64 + (u & 3) * 16 + w) * 896 + (lane & 7) * 16;
      const unsigned lb = (unsigned)(uintptr_t)(L + L_WO) + w * 1024;
#pragma unroll
      for (int k = 0; k < NLDS / 8; ++k)
#pragma unroll
        for (int c = 0; c < 7; ++c) {
          if (k * 7 + c >= WA) vmwait<WA - 1>();
          dma16(ro, offo + k * 8 * 896 + c * 128, lb + (c * NLDS + k * 8) * 1024);
        }
      vmwait<0>();
      lds_inc(L, F_WOC, lane);
    }
    STAMP(1);
    lds_wait_ge(flag + F_KISS, w < 4 ? 3u : 6u);
    STAMP(2);
    const auto rv = RSRC(vc + kvbase, WIN * DH * 2);
    u32x4 r[16];
    const int trot = w + (bid >> 3);
    const int voff = (w * 128 + (lane >> 4) * 8 + ((lane >> 3) & 1)) * 512 + s * 128 + (lane & 7) * 16;
#pragma unroll
    for (int i = 0; i < 16; ++i) {
      if (i >= WV) vmwait<WV - 1>();
      r[i] = __builtin_amdgcn_raw_buffer_load_b128(rv, voff + (i & 3) * 1024, (((i >> 2) + trot) & 3) * 16384, AUX_NT);
      SCHED_FENCE();
    }
    STAMP(3);
    const Sel sel = make_sel(lane);
    lds_wait_ge(flag + F_PRDY, 1u);
    STAMP(4);
    u32x4 pp[4];
#pragma unroll
    for (int t = 0; t < 4; ++t) pp[t] = *(const LAS u32x4*)(L + L_P + ((lane >> 3) & 1) * 2048 + (w * 128 + ((t + trot) & 3) * 32 + (lane >> 4) * 8) * 2);
    f32x4 a0 = {0, 0, 0, 0}, a1 = {0, 0, 0, 0};
#pragma unroll
    for (int i = 0; i < 16; ++i) {
      a0 = mfma(place(pp[i >> 2][i & 3], sel.lo), r[i], a0);
      a1 = mfma(place(pp[i >> 2][i & 3], sel.hi), r[i], a1);
    }
    f32x4 t = (lane & 8) ? a1 : a0;
#pragma unroll
    for (int k = 0; k < 4; ++k) t[k] += dpp<ROR8>(t[k]);
    if (!(lane & 8)) *(LAS f32x4*)(L + L_OP + w * 512 + ((lane >> 5) * 64 + (lane & 7) * 8 + ((lane >> 4) & 1) * 4) * 4) = t;
    STAMP(5);
    asm volatile("s_waitcnt lgkmcnt(0)\n\ts_barrier" ::: "memory");
  }
}

extern "C" void kernel_launch(void* const* d_in, const int* in_sizes, int n_in, void* d_out, int out_size, void* d_ws, size_t ws_size,
                              hipStream_t stream) {
  fused_kernel<<<256, 1024, 0, stream>>>((const u16*)d_in[0], (const u16*)d_in[1], (const u16*)d_in[2], (const u16*)d_in[3], (const u16*)d_in[4],
                                         (const u16*)d_in[6], (const u16*)d_in[7], (const u16*)d_in[8], (u16*)d_out, (unsigned char*)d_ws);
}
```

```cpp
#include <hip/hip_runtime.h>
#include <hip/hip_bf16.h>
#include <stdint.h>

#define STAMPS 0
#define QSLEEP 40
#define WK 3
#define WV 2
#define WW 3
#define VSPLIT 12

typedef unsigned u32x4 __attribute__((ext_vector_type(4)));
typedef unsigned u32x2 __attribute__((ext_vector_type(2)));
typedef float f32x4 __attribute__((ext_vector_type(4)));
typedef float f32x2 __attribute__((ext_vector_type(2)));
typedef short s16x4 __attribute__((ext_vector_type(4)));
typedef short s16x8 __attribute__((ext_vector_type(8)));
typedef __bf16 bf16x2 __attribute__((ext_vector_type(2)));
typedef __bf16 bf16x8 __attribute__((ext_vector_type(8)));
typedef unsigned short u16;
#define LAS __attribute__((address_space(3)))
#define GAS __attribute__((address_space(1)))

constexpr int NB = 8, DM = 448, HKV = 8, SK = 4096, DH = 256, WIN = 1024, FF = 1792, NQ = 4096;
constexpr int QSTR = 16384 + 512, HSTR = 4096 + 512, PSTR = 4096 + 128;
constexpr int WS_EPOCH = 0;
constexpr int WS_Q = 16384;
constexpr int WS_H = WS_Q + 64 * QSTR;
constexpr int WS_S = WS_H + 224 * HSTR;
constexpr int WS_HB = WS_S + 524288;
constexpr int WS_OB = WS_HB + 65536;
constexpr int WS_PO = WS_OB + 131072;
constexpr int WS_END = WS_PO + 224 * PSTR;
constexpr int WS_STAMP = 4 << 20;
constexpr int L_FLAG = 0, L_Q = 256, L_P = L_Q + 1024, L_OP = L_P + 4096, L_WQ = L_OP + 4096, L_W1 = L_WQ + 14336, L_WO = L_W1 + 8192,
              L_W2 = L_WO + 16384, L_END = L_W2 + 8192;
enum { F_TAGV = 0, F_TAG = 1, F_QRDY = 2, F_PRDY = 3, F_KCNT = 4, F_KISS = 5, F_OCNT = 6 };
#define LDAUX 16
#define STAUX 16
#define WS_AT_END 0
constexpr int AUX_NT = 2, AUX_SC1 = 16;
constexpr unsigned SPIN_CAP = 1u << 18;

#define RSRC(p, n) __builtin_amdgcn_make_buffer_rsrc((void*)(p), 0, (n), 0x00020000)
#define LD128(r, off, aux) __builtin_amdgcn_raw_buffer_load_b128((r), (off), 0, (aux))
#define ST128(v, r, off, aux) __builtin_amdgcn_raw_buffer_store_b128((v), (r), (off), 0, (aux))
#define SCHED_FENCE() __builtin_amdgcn_sched_barrier(0)
#define MEM_FENCE() asm volatile("" ::: "memory")
template <int N> __device__ __forceinline__ void vmwait() { asm volatile("s_waitcnt vmcnt(%0)" ::"n"(N) : "memory"); }
#define LGKM0() asm volatile("s_waitcnt lgkmcnt(0)" ::: "memory")

__device__ __forceinline__ f32x4 mfma(u32x4 a, u32x4 b, f32x4 c) {
  return __builtin_amdgcn_mfma_f32_16x16x32_bf16(__builtin_bit_cast(bf16x8, a), __builtin_bit_cast(bf16x8, b), c, 0, 0, 0);
}
__device__ __forceinline__ u32x2 trd(LAS unsigned char* p) {
  return __builtin_bit_cast(u32x2, __builtin_amdgcn_ds_read_tr16_b64_v4i16((LAS s16x4*)p));
}
__device__ __forceinline__ unsigned pk_bf16(float lo, float hi) {
  f32x2 v = {lo, hi};
  return __builtin_bit_cast(unsigned, __builtin_convertvector(v, bf16x2));
}
__device__ __forceinline__ float bf_lo(unsigned u) { return __uint_as_float(u << 16); }
__device__ __forceinline__ float bf_hi(unsigned u) { return __uint_as_float(u & 0xffff0000u); }
__device__ __forceinline__ float rbf(float f) { return bf_lo(pk_bf16(f, f)); }
template <int CTRL> __device__ __forceinline__ float dpp(float x) {
  return __builtin_bit_cast(float, __builtin_amdgcn_mov_dpp(__builtin_bit_cast(int, x), CTRL, 0xf, 0xf, true));
}
constexpr int XOR1 = 0xB1, XOR2 = 0x4E, HMIRROR = 0x141, ROR8 = 0x128;
__device__ __forceinline__ float x16_a(float x, float& o) {
  auto s = __builtin_amdgcn_permlane16_swap(__float_as_uint(x), __float_as_uint(x), false, false);
  o = __uint_as_float(s[1]);
  return __uint_as_float(s[0]);
}
__device__ __forceinline__ float x32_a(float x, float& o) {
  auto s = __builtin_amdgcn_permlane32_swap(__float_as_uint(x), __float_as_uint(x), false, false);
  o = __uint_as_float(s[1]);
  return __uint_as_float(s[0]);
}
__device__ __forceinline__ float wave_max(float x) {
  x = fmaxf(x, dpp<XOR1>(x)); x = fmaxf(x, dpp<XOR2>(x)); x = fmaxf(x, dpp<HMIRROR>(x)); x = fmaxf(x, dpp<ROR8>(x));
  float o, a = x16_a(x, o); x = fmaxf(a, o);
  a = x32_a(x, o); return fmaxf(a, o);
}
__device__ __forceinline__ float wave_sum(float x) {
  x += dpp<XOR1>(x); x += dpp<XOR2>(x); x += dpp<HMIRROR>(x); x += dpp<ROR8>(x);
  float o, a = x16_a(x, o); x = a + o;
  a = x32_a(x, o); return a + o;
}
__device__ __forceinline__ void lds_wait_ge(volatile LAS unsigned* f, unsigned want) {
  for (unsigned n = 0; n < (1u << 22); ++n) {
    if (__builtin_amdgcn_readfirstlane(*f) >= want) break;
    __builtin_amdgcn_s_sleep(1);
  }
  MEM_FENCE();
}
__device__ __forceinline__ void lds_inc(LAS unsigned char* L, int f, int lane) {
  if (lane == 0) __hip_atomic_fetch_add((LAS unsigned*)L + f, 1u, __ATOMIC_RELAXED, __HIP_MEMORY_SCOPE_WORKGROUP);
}
__device__ __forceinline__ void dma16(__amdgpu_buffer_rsrc_t r, int voff, unsigned ldsdst) {
  unsigned keep;
  asm volatile("s_mov_b32 %0, m0\n\ts_mov_b32 m0, %3\n\ts_nop 0\n\tbuffer_load_dwordx4 %1, %2, 0 offen nt lds\n\ts_mov_b32 m0, %0"
               : "=&s"(keep) : "v"(voff), "s"(r), "s"(ldsdst) : "memory");
}
struct Sel { unsigned lo[4], hi[4]; };
__device__ __forceinline__ Sel make_sel(int lane) {
  Sel s;
  const int j = lane & 7;
#pragma unroll
  for (int t = 0; t < 4; ++t) {
    const bool on = (j >> 1) == t;
    s.lo[t] = on ? ((j & 1) ? 0x01000c0cu : 0x0c0c0100u) : 0x0c0c0c0cu;
    s.hi[t] = on ? ((j & 1) ? 0x03020c0cu : 0x0c0c0302u) : 0x0c0c0c0cu;
  }
  return s;
}
__device__ __forceinline__ u32x4 place(unsigned src, const unsigned (&sel)[4]) {
  u32x4 r;
#pragma unroll
  for (int t = 0; t < 4; ++t) r[t] = __builtin_amdgcn_perm(src, src, sel[t]);
  return r;
}

#define STAMP(i) do {} while (0)

__global__ void __launch_bounds__(1024) fused_kernel(const u16* __restrict__ x, const u16* __restrict__ kc, const u16* __restrict__ vc,
                                                     const u16* __restrict__ wq, const u16* __restrict__ wo, const u16* __restrict__ w1,
                                                     const u16* __restrict__ w2, const u16* __restrict__ gw, u16* __restrict__ out,
                                                     unsigned char* __restrict__ ws) {
  __shared__ __attribute__((aligned(16))) unsigned char smem[L_END];
  LAS unsigned char* L = (LAS unsigned char*)smem;
  volatile LAS unsigned* flag = (volatile LAS unsigned*)(L + L_FLAG);
  STAMP(0);
  const int bid = blockIdx.x, kvh = bid & 7, b = (bid >> 3) & 7, s = bid >> 6;
  const int tid = threadIdx.x, lane = tid & 63, wave = __builtin_amdgcn_readfirstlane(tid >> 6);
  const int grp = kvh * 4 + s;
  const int fgrp = s < 3 ? kvh * 3 + s : (kvh < 4 ? 24 + kvh : -1);
  const size_t kvbase = ((size_t)(b * HKV + kvh) * SK + (SK - WIN)) * DH;
  const auto rws = RSRC(ws, WS_END);
  if (tid < 16) flag[tid] = 0u;
  asm volatile("s_waitcnt lgkmcnt(0)" ::"s"(x), "s"(kc), "s"(vc), "s"(wq), "s"(wo), "s"(w1), "s"(w2), "s"(gw), "s"(out), "s"(ws) : "memory");
  if (wave >= 2 || (wave == 1 && (fgrp < 0 || b == 7))) asm volatile("s_barrier" ::: "memory");

  if (wave == 0) {
    __builtin_amdgcn_s_setprio(3);
    unsigned epoch, tag;
    asm volatile("s_load_dword %0, %1, 0x0" : "=s"(epoch) : "s"(ws) : "memory");
    {
      const int cb = ((bid >> 3) & 15) + 16 * (kvh >> 1), rb = (bid >> 7) + 2 * (kvh & 1);
      const auto rq = RSRC(wq, DM * NQ * 2);
      u32x4 wr[14], xa[3];
      const int qoffs = ((rb * 112 + (lane >> 3)) * NQ + cb * 64 + (lane & 7) * 8) * 2;
#pragma unroll
      for (int i = 0; i < 14; ++i) wr[i] = LD128(rq, qoffs + i * 8 * NQ * 2, AUX_NT);
      const u16* xp = x + (lane & 7) * DM + rb * 112;
#pragma unroll
      for (int i = 0; i < 3; ++i) xa[i] = *(const u32x4*)(xp + i * 32 + (lane >> 4) * 8);
      const u32x2 xb = *(const u32x2*)(xp + 96 + (lane >> 4) * 4);
      SCHED_FENCE();
      asm volatile("s_barrier\n\ts_waitcnt lgkmcnt(0)" : "+s"(epoch)::"memory");
      tag = epoch * 1664525u + 1013904223u;
      if (lane == 0) flag[F_TAG] = tag;
      LGKM0();
      if (lane == 0) flag[F_TAGV] = 1u;
#pragma unroll
      for (int i = 0; i < 14; ++i) *(LAS u32x4*)(L + L_WQ + i * 1024 + lane * 16) = wr[i];
      STAMP(1);
      LGKM0();
      LAS unsigned char* tb = L + L_WQ + ((lane >> 4) * 8 + ((lane >> 2) & 3)) * 128 + (lane & 3) * 8;
      LAS unsigned char* tb2 = L + L_WQ + (96 + (lane >> 4) * 4 + ((lane >> 2) & 3)) * 128 + (lane & 3) * 8;
      f32x4 acc[4];
#pragma unroll
      for (int ct = 0; ct < 4; ++ct) {
        const u32x2 pt = trd(tb2 + ct * 32);
        acc[ct] = mfma(u32x4{xb[0], xb[1], 0u, 0u}, u32x4{pt[0], pt[1], 0u, 0u}, f32x4{0, 0, 0, 0});
#pragma unroll
        for (int ks = 0; ks < 3; ++ks) {
          const u32x2 p0 = trd(tb + ks * 4096 + ct * 32), p1 = trd(tb + ks * 4096 + 512 + ct * 32);
          acc[ct] = mfma(xa[ks], u32x4{p0[0], p0[1], p1[0], p1[1]}, acc[ct]);
        }
      }
      const int qo = WS_Q + ((cb >> 3) * 8 + ((lane >> 4) & 1) * 4 + (lane >> 5) * 2) * QSTR + rb * 4096 + ((cb & 7) * 32 + (lane & 15)) * 16;
#pragma unroll
      for (int cp = 0; cp < 2; ++cp)
#pragma unroll
        for (int rr = 0; rr < 2; ++rr) {
          const u32x4 qv = {__float_as_uint(lane >= 32 ? acc[2 * cp][rr + 2] : acc[2 * cp][rr]), tag,
                            __float_as_uint(lane >= 32 ? acc[2 * cp + 1][rr + 2] : acc[2 * cp + 1][rr]), tag};
          ST128(qv, rws, qo + rr * QSTR + cp * 256, STAUX);
        }
    }
    STAMP(2);
    __builtin_amdgcn_s_sleep(QSLEEP);
    STAMP(6);
    {
      const int qoff = WS_Q + (kvh * 8 + b) * QSTR + lane * 16;
      u32x4 qg[16];
      for (unsigned n = 0; n < SPIN_CAP; ++n) {
        MEM_FENCE();
        bool ok = true;
#pragma unroll
        for (int i = 0; i < 16; ++i) qg[i] = LD128(rws, qoff + i * 1024, LDAUX);
#pragma unroll
        for (int i = 0; i < 16; ++i) ok = ok && qg[i][1] == tag && qg[i][3] == tag;
        if (__all(ok)) break;
      }
#pragma unroll
      for (int i = 0; i < 4; ++i) {
        const float a0 = (__uint_as_float(qg[i][0]) + __uint_as_float(qg[4 + i][0])) + (__uint_as_float(qg[8 + i][0]) + __uint_as_float(qg[12 + i][0]));
        const float a1 = (__uint_as_float(qg[i][2]) + __uint_as_float(qg[4 + i][2])) + (__uint_as_float(qg[8 + i][2]) + __uint_as_float(qg[12 + i][2]));
        const unsigned pk = pk_bf16(a0, a1);
        LAS u16* qd = (LAS u16*)(L + L_Q) + i * 128 + (lane >> 4) * 32 + (lane & 15);
        qd[0] = (u16)pk;
        qd[16] = (u16)(pk >> 16);
      }
      LGKM0();
      if (lane == 0) flag[F_QRDY] = 1u;
    }
    STAMP(3);
    lds_wait_ge(flag + F_KCNT, 6u);
    __builtin_amdgcn_s_sleep(10);
    const int soff = WS_S + (b * 8 + kvh) * 8192 + lane * 16;
    u32x4 sg[8];
    for (unsigned n = 0; n < SPIN_CAP; ++n) {
      MEM_FENCE();
      bool ok = true;
#pragma unroll
      for (int i = 0; i < 8; ++i) sg[i] = LD128(rws, soff + i * 1024, LDAUX);
#pragma unroll
      for (int i = 0; i < 8; ++i) ok = ok && sg[i][1] == tag && sg[i][3] == tag;
      if (__all(ok)) break;
    }
    STAMP(4);
    float m0 = -INFINITY, m1 = -INFINITY;
#pragma unroll
    for (int i = 0; i < 8; ++i) {
      m0 = fmaxf(m0, fmaxf(bf_lo(sg[i][0]), bf_lo(sg[i][2])));
      m1 = fmaxf(m1, fmaxf(bf_hi(sg[i][0]), bf_hi(sg[i][2])));
    }
    m0 = wave_max(m0); m1 = wave_max(m1);
    constexpr float C2 = 0.0625f * 1.4426950408889634f;
    float e0[16], e1[16], l0 = 0.f, l1 = 0.f;
#pragma unroll
    for (int i = 0; i < 8; ++i) {
      e0[2 * i] = __builtin_amdgcn_exp2f((bf_lo(sg[i][0]) - m0) * C2);
      e0[2 * i + 1] = __builtin_amdgcn_exp2f((bf_lo(sg[i][2]) - m0) * C2);
      e1[2 * i] = __builtin_amdgcn_exp2f((bf_hi(sg[i][0]) - m1) * C2);
      e1[2 * i + 1] = __builtin_amdgcn_exp2f((bf_hi(sg[i][2]) - m1) * C2);
      l0 += e0[2 * i] + e0[2 * i + 1];
      l1 += e1[2 * i] + e1[2 * i + 1];
    }
    l0 = __builtin_amdgcn_rcpf(wave_sum(l0)); l1 = __builtin_amdgcn_rcpf(wave_sum(l1));
#pragma unroll
    for (int i = 0; i < 8; ++i) {
      *(LAS unsigned*)(L + L_P + i * 256 + lane * 4) = pk_bf16(e0[2 * i] * l0, e0[2 * i + 1] * l0);
      *(LAS unsigned*)(L + L_P + 2048 + i * 256 + lane * 4) = pk_bf16(e1[2 * i] * l1, e1[2 * i + 1] * l1);
    }
    LGKM0();
    if (lane == 0) flag[F_PRDY] = 1u;
    STAMP(5);
    if (bid < 224) {
      lds_wait_ge(flag + F_OCNT, 8u);
      __builtin_amdgcn_s_sleep(24);
      const int poff = WS_PO + bid * PSTR + lane * 16;
      u32x4 pg[4];
      for (unsigned n = 0; n < SPIN_CAP; ++n) {
        MEM_FENCE();
        bool ok = true;
#pragma unroll
        for (int i = 0; i < 4; ++i) pg[i] = LD128(rws, poff + i * 1024, LDAUX);
#pragma unroll
        for (int i = 0; i < 4; ++i) ok = ok && pg[i][1] == tag && pg[i][3] == tag;
        if (__all(ok)) break;
      }
      float a0 = (__uint_as_float(pg[0][0]) + __uint_as_float(pg[1][0])) + (__uint_as_float(pg[2][0]) + __uint_as_float(pg[3][0]));
      float a1 = (__uint_as_float(pg[0][2]) + __uint_as_float(pg[1][2])) + (__uint_as_float(pg[2][2]) + __uint_as_float(pg[3][2]));
      a0 += dpp<ROR8>(a0); a1 += dpp<ROR8>(a1);
      float o, t;
      t = x16_a(a0, o); a0 = t + o; t = x16_a(a1, o); a1 = t + o;
      t = x32_a(a0, o); a0 = t + o; t = x32_a(a1, o); a1 = t + o;
      if (lane < 8) *(unsigned*)(out + lane * DM + bid * 2) = pk_bf16(a0, a1);
      if (bid == 0 && lane == 0) __hip_atomic_store((GAS unsigned*)(ws + WS_EPOCH), tag, __ATOMIC_RELAXED, __HIP_MEMORY_SCOPE_AGENT);
    }
  } else if (wave == 1) {
    unsigned tag;
    __builtin_amdgcn_s_setprio(3);
    if (fgrp < 0 || b == 7) {
      lds_wait_ge(flag + F_TAGV, 1u);
      tag = __builtin_amdgcn_readfirstlane(flag[F_TAG]);
    } else {
      const auto r1 = RSRC(w1, DM * FF * 2);
      u32x4 wr[8], xa[2];
      const int o1 = ((b * 64 + (lane >> 3)) * FF + fgrp * 64 + (lane & 7) * 8) * 2;
#pragma unroll
      for (int i = 0; i < 8; ++i) wr[i] = LD128(r1, o1 + i * 8 * FF * 2, AUX_NT);
#pragma unroll
      for (int i = 0; i < 2; ++i) xa[i] = *(const u32x4*)(x + (lane & 7) * DM + b * 64 + i * 32 + (lane >> 4) * 8);
      SCHED_FENCE();
      asm volatile("s_barrier" ::: "memory");
      lds_wait_ge(flag + F_TAGV, 1u);
      tag = __builtin_amdgcn_readfirstlane(flag[F_TAG]);
#pragma unroll
      for (int i = 0; i < 8; ++i) *(LAS u32x4*)(L + L_W1 + i * 1024 + lane * 16) = wr[i];
      LGKM0();
      LAS unsigned char* tb = L + L_W1 + ((lane >> 4) * 8 + ((lane >> 2) & 3)) * 128 + (lane & 3) * 8;
      f32x4 acc[4];
#pragma unroll
      for (int ct = 0; ct < 4; ++ct) {
        acc[ct] = f32x4{0, 0, 0, 0};
#pragma unroll
        for (int ks = 0; ks < 2; ++ks) {
          const u32x2 p0 = trd(tb + ks * 4096 + ct * 32), p1 = trd(tb + ks * 4096 + 512 + ct * 32);
          acc[ct] = mfma(xa[ks], u32x4{p0[0], p0[1], p1[0], p1[1]}, acc[ct]);
        }
      }
      const int ho = WS_H + (fgrp * 8 + ((lane >> 4) & 1) * 4 + (lane >> 5) * 2) * HSTR + b * 512 + (lane & 15) * 16;
#pragma unroll
      for (int cp = 0; cp < 2; ++cp)
#pragma unroll
        for (int rr = 0; rr < 2; ++rr) {
          const u32x4 qv = {__float_as_uint(lane >= 32 ? acc[2 * cp][rr + 2] : acc[2 * cp][rr]), tag,
                            __float_as_uint(lane >= 32 ? acc[2 * cp + 1][rr + 2] : acc[2 * cp + 1][rr]), tag};
          ST128(qv, rws, ho + rr * HSTR + cp * 256, STAUX);
        }
    }
    STAMP(1);
    float g;
    {
      u32x4 xg = {0, 0, 0, 0}, gg = {0, 0, 0, 0};
      if (lane < 56) {
        xg = *(const u32x4*)(x + b * DM + lane * 8);
        gg = *(const u32x4*)(gw + lane * 8);
      }
      float z = 0.f;
#pragma unroll
      for (int t = 0; t < 4; ++t) z += rbf(bf_lo(xg[t]) * bf_lo(gg[t])) + rbf(bf_hi(xg[t]) * bf_hi(gg[t]));
      z = rbf(wave_sum(z));
      g = rbf(1.f / (1.f + __expf(-z)));
    }
    if (b < 7) {
      const auto ro = RSRC(wo, NQ * DM * 2);
      const int offo = (kvh * 512 + s * 64 + (lane >> 3)) * 896 + b * 128 + (lane & 7) * 16;
      const unsigned lb = (unsigned)(uintptr_t)(L + L_WO);
#pragma unroll
      for (int i = 0; i < 16; ++i) {
        if (i >= WW) vmwait<WW - 1>();
        dma16(ro, offo + ((i >> 3) * 256 + (i & 7) * 8) * 896, lb + i * 1024);
      }
      if (fgrp >= 0) {
        const auto r2 = RSRC(w2, FF * DM * 2);
        const int off2 = (fgrp * 64 + (lane >> 3)) * 896 + b * 128 + (lane & 7) * 16;
#pragma unroll
        for (int i = 0; i < 8; ++i) {
          vmwait<WW - 1>();
          dma16(r2, off2 + i * 8 * 896, lb + 16384 + i * 1024);
        }
      }
    }
    STAMP(2);
    if (fgrp >= 0) {
      const int hoff = WS_H + (fgrp * 8 + b) * HSTR + lane * 16;
      u32x4 hg[4];
      for (unsigned n = 0; n < SPIN_CAP; ++n) {
        MEM_FENCE();
        bool ok = true;
#pragma unroll
        for (int i = 0; i < 4; ++i) hg[i] = LD128(rws, hoff + i * 1024, LDAUX);
#pragma unroll
        for (int i = 0; i < 3; ++i) ok = ok && hg[i][1] == tag && hg[i][3] == tag;
        ok = ok && (lane >= 32 || (hg[3][1] == tag && hg[3][3] == tag));
        if (__all(ok)) break;
        __builtin_amdgcn_s_sleep(8);
      }
      float a0 = (__uint_as_float(hg[0][0]) + __uint_as_float(hg[1][0])) + (__uint_as_float(hg[2][0]) + (lane < 32 ? __uint_as_float(hg[3][0]) : 0.f));
      float a1 = (__uint_as_float(hg[0][2]) + __uint_as_float(hg[1][2])) + (__uint_as_float(hg[2][2]) + (lane < 32 ? __uint_as_float(hg[3][2]) : 0.f));
      float o, t;
      t = x32_a(a0, o); a0 = rbf(t + o);
      t = x32_a(a1, o); a1 = rbf(t + o);
      const float g1 = rbf(1.f - g);
      a0 = rbf(a0 / (1.f + __expf(-a0))) * g1;
      a1 = rbf(a1 / (1.f + __expf(-a1))) * g1;
      const float n0 = dpp<XOR1>(a0), n1 = dpp<XOR1>(a1);
      const unsigned pk = (lane & 1) ? pk_bf16(n1, a1) : pk_bf16(a0, n0);
      if (lane < 32)
        __builtin_amdgcn_raw_buffer_store_b64(u32x2{pk, tag}, rws, WS_HB + (fgrp * 8 + b) * 256 + ((lane >> 4) * 16 + (lane & 1) * 8 + ((lane & 15) >> 1)) * 8, 0, STAUX);
    }
    STAMP(3);
    f32x4 acc[4] = {{0, 0, 0, 0}, {0, 0, 0, 0}, {0, 0, 0, 0}, {0, 0, 0, 0}};
    u32x4 wf[4][4];
    const bool hi8 = lane & 8;
    const int frag = (lane >> 4) * 32 + ((lane >> 3) & 1) * 16;
    if (b < 7) {
      vmwait<0>();
      LAS unsigned char* tb = L + L_WO + ((lane >> 4) * 8 + ((lane >> 2) & 3)) * 128 + (lane & 3) * 8;
      if (fgrp >= 0) {
        const int hoff = WS_HB + fgrp * 2048 + (lane & 7) * 256 + frag;
        u32x4 hb[2];
        for (unsigned n = 0; n < SPIN_CAP; ++n) {
          __builtin_amdgcn_s_sleep(16);
          MEM_FENCE();
          hb[0] = LD128(rws, hoff, LDAUX);
          hb[1] = LD128(rws, hoff + 128, LDAUX);
          if (__all(hb[0][1] == tag && hb[0][3] == tag && hb[1][1] == tag && hb[1][3] == tag)) break;
        }
#pragma unroll
        for (int ks = 0; ks < 2; ++ks) {
          const u32x4 bf = {hi8 ? 0u : hb[ks][0], hi8 ? 0u : hb[ks][2], hi8 ? hb[ks][0] : 0u, hi8 ? hb[ks][2] : 0u};
#pragma unroll
          for (int ct = 0; ct < 4; ++ct) {
            const u32x2 p0 = trd(tb + 16384 + ks * 4096 + ct * 32), p1 = trd(tb + 16384 + ks * 4096 + 512 + ct * 32);
            acc[ct] = mfma(u32x4{p0[0], p0[1], p1[0], p1[1]}, bf, acc[ct]);
          }
        }
      }
#pragma unroll
      for (int ks = 0; ks < 4; ++ks)
#pragma unroll
        for (int ct = 0; ct < 4; ++ct) {
          const u32x2 p0 = trd(tb + ks * 4096 + ct * 32), p1 = trd(tb + ks * 4096 + 512 + ct * 32);
          wf[ks][ct] = u32x4{p0[0], p0[1], p1[0], p1[1]};
          asm volatile("" : "+v"(wf[ks][ct]));
        }
    }
    STAMP(4);
    lds_wait_ge(flag + F_OCNT, 8u);
    {
      f32x2 ov = {0.f, 0.f};
#pragma unroll
      for (int w = 0; w < 8; ++w) ov += *(const LAS f32x2*)(L + L_OP + w * 512 + lane * 8);
      __builtin_amdgcn_raw_buffer_store_b64(u32x2{pk_bf16(ov[0] * g, ov[1] * g), tag}, rws, WS_OB + (grp * 8 + b) * 512 + lane * 8, 0, STAUX);
    }
    STAMP(5);
    if (b < 7) {
      const int ooff = WS_OB + grp * 4096 + (lane & 7) * 512 + frag;
      u32x4 ob[4];
      for (unsigned n = 0; n < SPIN_CAP; ++n) {
        MEM_FENCE();
        bool ok = true;
#pragma unroll
        for (int ks = 0; ks < 4; ++ks) ob[ks] = LD128(rws, ooff + ks * 128, LDAUX);
#pragma unroll
        for (int ks = 0; ks < 4; ++ks) ok = ok && ob[ks][1] == tag && ob[ks][3] == tag;
        if (__all(ok)) break;
      }
      STAMP(6);
#pragma unroll
      for (int ks = 0; ks < 4; ++ks) {
        const u32x4 bf = {hi8 ? 0u : ob[ks][0], hi8 ? 0u : ob[ks][2], hi8 ? ob[ks][0] : 0u, hi8 ? ob[ks][2] : 0u};
#pragma unroll
        for (int ct = 0; ct < 4; ++ct) acc[ct] = mfma(wf[ks][ct], bf, acc[ct]);
      }
      const int po = WS_PO + (b * 32 + (lane >> 3)) * PSTR + grp * 128 + (lane & 7) * 16;
#pragma unroll
      for (int ct = 0; ct < 4; ++ct) {
        const float k0 = hi8 ? acc[ct][2] : acc[ct][0], k1 = hi8 ? acc[ct][3] : acc[ct][1];
        const float s0 = hi8 ? acc[ct][0] : acc[ct][2], s1 = hi8 ? acc[ct][1] : acc[ct][3];
        const u32x4 pv = {__float_as_uint(k0 + dpp<ROR8>(s0)), tag, __float_as_uint(k1 + dpp<ROR8>(s1)), tag};
        ST128(pv, rws, po + ct * 8 * PSTR, STAUX);
      }
    }
  } else if (wave < 8) {
    __builtin_amdgcn_s_setprio(2);
    const int kw = wave - 2, u0 = kw < 2 ? kw * 6 : 2 + kw * 5, nu = kw < 2 ? 6 : 5;
    const int urot = u0 + (bid >> 3) * 3, brot = wave + (bid >> 3);
    const auto rk = RSRC(kc + kvbase + s * 256 * DH, 256 * DH * 2);
    u32x4 r[24];
    const int lo = (lane & 7) * 512 + ((lane >> 3) & 1) * 64 + (lane >> 4) * 16;
    const int lo6 = kw < 2 ? lo : 0x7f000000;
#pragma unroll
    for (int i = 0; i < 24; ++i) {
      if (i >= WK) vmwait<WK - 1>();
      const int so = ((urot + (i >> 2)) & 31) * 4096 + ((brot + i) & 3) * 128;
      r[i] = __builtin_amdgcn_raw_buffer_load_b128(rk, i < 20 ? lo : lo6, so, AUX_NT);
      SCHED_FENCE();
      if (i == 19) lds_inc(L, F_KISS, lane);
    }
    STAMP(1);
    lds_wait_ge(flag + F_QRDY, 1u);
    const unsigned tag = __builtin_amdgcn_readfirstlane(flag[F_TAG]);
    STAMP(2);
    u32x4 qa[4];
#pragma unroll
    for (int t = 0; t < 4; ++t) qa[t] = *(const LAS u32x4*)(L + L_Q + (lane & 1) * 512 + ((brot + t) & 3) * 128 + ((lane >> 1) & 1) * 64 + (lane >> 4) * 16);
    float s0 = 0.f, s1 = 0.f;
#pragma unroll
    for (int u = 0; u < 6; ++u) {
      f32x4 c = {0, 0, 0, 0};
#pragma unroll
      for (int t = 0; t < 4; ++t) c = mfma(qa[t], r[4 * u + t], c);
      float v0 = (lane & 8) ? c[2] : c[0], v1 = (lane & 8) ? c[3] : c[1];
      v0 += dpp<ROR8>(v0); v1 += dpp<ROR8>(v1);
      if ((lane >> 3) == u) { s0 = v0; s1 = v1; }
    }
    if ((lane >> 3) < nu)
      __builtin_amdgcn_raw_buffer_store_b64(u32x2{pk_bf16(s0, s1), tag}, rws,
                                            WS_S + (b * 8 + kvh) * 8192 + (s * 256 + ((urot + (lane >> 3)) & 31) * 8 + (lane & 7)) * 8, 0, STAUX);
    lds_inc(L, F_KCNT, lane);
    STAMP(3);
  } else {
    const int w = wave - 8;
    __builtin_amdgcn_s_setprio(1);
    const auto rv = RSRC(vc + kvbase, WIN * DH * 2);
    u32x4 r[16];
    const int trot = w + (bid >> 3);
    const int voff = (w * 128 + (lane >> 4) * 8 + ((lane >> 3) & 1)) * 512 + s * 128 + (lane & 7) * 16;
    const Sel sel = make_sel(lane);
    lds_wait_ge(flag + F_KISS, w < 4 ? 3u : 6u);
    STAMP(2);
#define V_ISSUE(i) do { if ((i) >= WV) vmwait<WV - 1>(); \
      r[i] = __builtin_amdgcn_raw_buffer_load_b128(rv, voff + ((i) & 3) * 1024, ((((i) >> 2) + trot) & 3) * 16384, 0); SCHED_FENCE(); } while (0)
#define V_USE(i) do { a0 = mfma(place(pp[(i) >> 2][(i) & 3], sel.lo), r[i], a0); a1 = mfma(place(pp[(i) >> 2][(i) & 3], sel.hi), r[i], a1); } while (0)
#pragma unroll
    for (int i = 0; i < VSPLIT; ++i) V_ISSUE(i);
    STAMP(3);
    lds_wait_ge(flag + F_PRDY, 1u);
    STAMP(4);
    u32x4 pp[4];
#pragma unroll
    for (int t = 0; t < 4; ++t) pp[t] = *(const LAS u32x4*)(L + L_P + ((lane >> 3) & 1) * 2048 + (w * 128 + ((t + trot) & 3) * 32 + (lane >> 4) * 8) * 2);
    f32x4 a0 = {0, 0, 0, 0}, a1 = {0, 0, 0, 0};
    constexpr int PER = VSPLIT < 16 ? (VSPLIT - WV + (16 - VSPLIT) - 1) / (16 - VSPLIT) + 1 : 0;
#pragma unroll
    for (int i = VSPLIT; i < 16; ++i) {
      V_ISSUE(i);
#pragma unroll
      for (int k = 0; k < 16; ++k)
        if (k >= (i - VSPLIT) * PER && k < (i - VSPLIT + 1) * PER && k <= i - WV) V_USE(k);
      SCHED_FENCE();
    }
#pragma unroll
    for (int k = 0; k < 16; ++k) {
      bool done = false;
#pragma unroll
      for (int i = VSPLIT; i < 16; ++i) done = done || (k >= (i - VSPLIT) * PER && k < (i - VSPLIT + 1) * PER && k <= i - WV);
      if (!done) V_USE(k);
    }
    f32x4 t = (lane & 8) ? a1 : a0;
#pragma unroll
    for (int k = 0; k < 4; ++k) t[k] += dpp<ROR8>(t[k]);
    if (!(lane & 8)) *(LAS f32x4*)(L + L_OP + w * 512 + ((lane >> 5) * 64 + (lane & 7) * 8 + ((lane >> 4) & 1) * 4) * 4) = t;
    LGKM0();
    lds_inc(L, F_OCNT, lane);
    STAMP(5);
  }
}

extern "C" void kernel_launch(void* const* d_in, const int* in_sizes, int n_in, void* d_out, int out_size, void* d_ws, size_t ws_size,
                              hipStream_t stream) {
  fused_kernel<<<256, 1024, 0, stream>>>((const u16*)d_in[0], (const u16*)d_in[1], (const u16*)d_in[2], (const u16*)d_in[3], (const u16*)d_in[4],
                                         (const u16*)d_in[6], (const u16*)d_in[7], (const u16*)d_in[8], (u16*)d_out, (unsigned char*)d_ws + (WS_AT_END ? ws_size - (8u << 20) : 0));
}
```

```cpp
#define DSEL 0
#include <hip/hip_runtime.h>
#include <hip/hip_bf16.h>
#include <stdint.h>

#define STAMPS 0
#define QSLEEP 40
#define WK 3
#define WV 2
#define WW 3
#define VSPLIT 12

typedef unsigned u32x4 __attribute__((ext_vector_type(4)));
typedef unsigned u32x2 __attribute__((ext_vector_type(2)));
typedef float f32x4 __attribute__((ext_vector_type(4)));
typedef float f32x2 __attribute__((ext_vector_type(2)));
typedef short s16x4 __attribute__((ext_vector_type(4)));
typedef short s16x8 __attribute__((ext_vector_type(8)));
typedef __bf16 bf16x2 __attribute__((ext_vector_type(2)));
typedef __bf16 bf16x8 __attribute__((ext_vector_type(8)));
typedef unsigned short u16;
#define LAS __attribute__((address_space(3)))
#define GAS __attribute__((address_space(1)))

constexpr int NB = 8, DM = 448, HKV = 8, SK = 4096, DH = 256, WIN = 1024, FF = 1792, NQ = 4096;
constexpr int QSTR = 16384 + 512, HSTR = 4096 + 512, PSTR = 4096 + 128;
constexpr int WS_EPOCH = 0;
constexpr int WS_Q = 16384;
constexpr int WS_H = WS_Q + 64 * QSTR;
constexpr int WS_S = WS_H + 224 * HSTR;
constexpr int WS_HB = WS_S + 524288;
constexpr int WS_OB = WS_HB + 65536;
constexpr int WS_PO = WS_OB + 131072;
constexpr int WS_END = WS_PO + 224 * PSTR;
constexpr int WS_STAMP = 4 << 20;
constexpr int L_FLAG = 0, L_Q = 256, L_P = L_Q + 1024, L_OP = L_P + 4096, L_WQ = L_OP + 4096, L_W1 = L_WQ + 14336, L_WO = L_W1 + 8192,
              L_W2 = L_WO + 16384, L_END = L_W2 + 8192;
enum { F_TAGV = 0, F_TAG = 1, F_QRDY = 2, F_PRDY = 3, F_KCNT = 4, F_KISS = 5, F_OCNT = 6 };
#define LDAUX 16
#define STAUX 16
#define WS_AT_END 0
constexpr int AUX_NT = 2, AUX_SC1 = 16;
constexpr unsigned SPIN_CAP = 1u << 18;

#define RSRC(p, n) __builtin_amdgcn_make_buffer_rsrc((void*)(p), 0, (n), 0x00020000)
#define LD128(r, off, aux) __builtin_amdgcn_raw_buffer_load_b128((r), (off), 0, (aux))
#define ST128(v, r, off, aux) __builtin_amdgcn_raw_buffer_store_b128((v), (r), (off), 0, (aux))
#define SCHED_FENCE() __builtin_amdgcn_sched_barrier(0)
#define MEM_FENCE() asm volatile("" ::: "memory")
template <int N> __device__ __forceinline__ void vmwait() { asm volatile("s_waitcnt vmcnt(%0)" ::"n"(N) : "memory"); }
#define LGKM0() asm volatile("s_waitcnt lgkmcnt(0)" ::: "memory")

__device__ __forceinline__ f32x4 mfma(u32x4 a, u32x4 b, f32x4 c) {
  return __builtin_amdgcn_mfma_f32_16x16x32_bf16(__builtin_bit_cast(bf16x8, a), __builtin_bit_cast(bf16x8, b), c, 0, 0, 0);
}
__device__ __forceinline__ u32x2 trd(LAS unsigned char* p) {
  return __builtin_bit_cast(u32x2, __builtin_amdgcn_ds_read_tr16_b64_v4i16((LAS s16x4*)p));
}
__device__ __forceinline__ unsigned pk_bf16(float lo, float hi) {
  f32x2 v = {lo, hi};
  return __builtin_bit_cast(unsigned, __builtin_convertvector(v, bf16x2));
}
__device__ __forceinline__ float bf_lo(unsigned u) { return __uint_as_float(u << 16); }
__device__ __forceinline__ float bf_hi(unsigned u) { return __uint_as_float(u & 0xffff0000u); }
__device__ __forceinline__ float rbf(float f) { return bf_lo(pk_bf16(f, f)); }
template <int CTRL> __device__ __forceinline__ float dpp(float x) {
  return __builtin_bit_cast(float, __builtin_amdgcn_mov_dpp(__builtin_bit_cast(int, x), CTRL, 0xf, 0xf, true));
}
constexpr int XOR1 = 0xB1, XOR2 = 0x4E, HMIRROR = 0x141, ROR8 = 0x128;
__device__ __forceinline__ float x16_a(float x, float& o) {
  auto s = __builtin_amdgcn_permlane16_swap(__float_as_uint(x), __float_as_uint(x), false, false);
  o = __uint_as_float(s[1]);
  return __uint_as_float(s[0]);
}
__device__ __forceinline__ float x32_a(float x, float& o) {
  auto s = __builtin_amdgcn_permlane32_swap(__float_as_uint(x), __float_as_uint(x), false, false);
  o = __uint_as_float(s[1]);
  return __uint_as_float(s[0]);
}
__device__ __forceinline__ float wave_max(float x) {
  x = fmaxf(x, dpp<XOR1>(x)); x = fmaxf(x, dpp<XOR2>(x)); x = fmaxf(x, dpp<HMIRROR>(x)); x = fmaxf(x, dpp<ROR8>(x));
  float o, a = x16_a(x, o); x = fmaxf(a, o);
  a = x32_a(x, o); return fmaxf(a, o);
}
__device__ __forceinline__ float wave_sum(float x) {
  x += dpp<XOR1>(x); x += dpp<XOR2>(x); x += dpp<HMIRROR>(x); x += dpp<ROR8>(x);
  float o, a = x16_a(x, o); x = a + o;
  a = x32_a(x, o); return a + o;
}
__device__ __forceinline__ void lds_wait_ge(volatile LAS unsigned* f, unsigned want) {
  for (unsigned n = 0; n < (1u << 22); ++n) {
    if (__builtin_amdgcn_readfirstlane(*f) >= want) break;
    __builtin_amdgcn_s_sleep(1);
  }
  MEM_FENCE();
}
__device__ __forceinline__ void lds_inc(LAS unsigned char* L, int f, int lane) {
  if (lane == 0) __hip_atomic_fetch_add((LAS unsigned*)L + f, 1u, __ATOMIC_RELAXED, __HIP_MEMORY_SCOPE_WORKGROUP);
}
__device__ __forceinline__ void dma16(__amdgpu_buffer_rsrc_t r, int voff, unsigned ldsdst) {
  unsigned keep;
  asm volatile("s_mov_b32 %0, m0\n\ts_mov_b32 m0, %3\n\ts_nop 0\n\tbuffer_load_dwordx4 %1, %2, 0 offen nt lds\n\ts_mov_b32 m0, %0"
               : "=&s"(keep) : "v"(voff), "s"(r), "s"(ldsdst) : "memory");
}
struct Sel { unsigned lo[4], hi[4]; };
__device__ __forceinline__ Sel make_sel(int lane) {
  Sel s;
  const int j = lane & 7;
#pragma unroll
  for (int t = 0; t < 4; ++t) {
    const bool on = (j >> 1) == t;
    s.lo[t] = on ? ((j & 1) ? 0x01000c0cu : 0x0c0c0100u) : 0x0c0c0c0cu;
    s.hi[t] = on ? ((j & 1) ? 0x03020c0cu : 0x0c0c0302u) : 0x0c0c0c0cu;
  }
  return s;
}
__device__ __forceinline__ u32x4 place(unsigned src, const unsigned (&sel)[4]) {
  u32x4 r;
#pragma unroll
  for (int t = 0; t < 4; ++t) r[t] = __builtin_amdgcn_perm(src, src, sel[t]);
  return r;
}

#define STAMP(i) do { SCHED_FENCE(); st[i] = __builtin_amdgcn_s_memrealtime(); SCHED_FENCE(); } while (0)

__global__ void __launch_bounds__(1024) fused_kernel(const u16* __restrict__ x, const u16* __restrict__ kc, const u16* __restrict__ vc,
                                                     const u16* __restrict__ wq, const u16* __restrict__ wo, const u16* __restrict__ w1,
                                                     const u16* __restrict__ w2, const u16* __restrict__ gw, u16* __restrict__ out,
                                                     unsigned char* __restrict__ ws) {
  __shared__ __attribute__((aligned(16))) unsigned char smem[L_END];
  LAS unsigned char* L = (LAS unsigned char*)smem;
  volatile LAS unsigned* flag = (volatile LAS unsigned*)(L + L_FLAG);
  unsigned long long st[8] = {0, 0, 0, 0, 0, 0, 0, 0};
  STAMP(0);
  const int bid = blockIdx.x, kvh = bid & 7, b = (bid >> 3) & 7, s = bid >> 6;
  const int tid = threadIdx.x, lane = tid & 63, wave = __builtin_amdgcn_readfirstlane(tid >> 6);
  const int grp = kvh * 4 + s;
  const int fgrp = s < 3 ? kvh * 3 + s : (kvh < 4 ? 24 + kvh : -1);
  const size_t kvbase = ((size_t)(b * HKV + kvh) * SK + (SK - WIN)) * DH;
  const auto rws = RSRC(ws, WS_END);
  if (tid < 16) flag[tid] = 0u;
  asm volatile("s_waitcnt lgkmcnt(0)" ::"s"(x), "s"(kc), "s"(vc), "s"(wq), "s"(wo), "s"(w1), "s"(w2), "s"(gw), "s"(out), "s"(ws) : "memory");
  if (wave >= 2 || (wave == 1 && (fgrp < 0 || b == 7))) asm volatile("s_barrier" ::: "memory");

  if (wave == 0) {
    __builtin_amdgcn_s_setprio(3);
    unsigned epoch, tag;
    asm volatile("s_load_dword %0, %1, 0x0" : "=s"(epoch) : "s"(ws) : "memory");
    {
      const int cb = ((bid >> 3) & 15) + 16 * (kvh >> 1), rb = (bid >> 7) + 2 * (kvh & 1);
      const auto rq = RSRC(wq, DM * NQ * 2);
      u32x4 wr[14], xa[3];
      const int qoffs = ((rb * 112 + (lane >> 3)) * NQ + cb * 64 + (lane & 7) * 8) * 2;
#pragma unroll
      for (int i = 0; i < 14; ++i) wr[i] = LD128(rq, qoffs + i * 8 * NQ * 2, AUX_NT);
      const u16* xp = x + (lane & 7) * DM + rb * 112;
#pragma unroll
      for (int i = 0; i < 3; ++i) xa[i] = *(const u32x4*)(xp + i * 32 + (lane >> 4) * 8);
      const u32x2 xb = *(const u32x2*)(xp + 96 + (lane >> 4) * 4);
      SCHED_FENCE();
      asm volatile("s_barrier\n\ts_waitcnt lgkmcnt(0)" : "+s"(epoch)::"memory");
      tag = epoch * 1664525u + 1013904223u;
      if (lane == 0) flag[F_TAG] = tag;
      LGKM0();
      if (lane == 0) flag[F_TAGV] = 1u;
#pragma unroll
      for (int i = 0; i < 14; ++i) *(LAS u32x4*)(L + L_WQ + i * 1024 + lane * 16) = wr[i];
      STAMP(1);
      LGKM0();
      LAS unsigned char* tb = L + L_WQ + ((lane >> 4) * 8 + ((lane >> 2) & 3)) * 128 + (lane & 3) * 8;
      LAS unsigned char* tb2 = L + L_WQ + (96 + (lane >> 4) * 4 + ((lane >> 2) & 3)) * 128 + (lane & 3) * 8;
      f32x4 acc[4];
#pragma unroll
      for (int ct = 0; ct < 4; ++ct) {
        const u32x2 pt = trd(tb2 + ct * 32);
        acc[ct] = mfma(u32x4{xb[0], xb[1], 0u, 0u}, u32x4{pt[0], pt[1], 0u, 0u}, f32x4{0, 0, 0, 0});
#pragma unroll
        for (int ks = 0; ks < 3; ++ks) {
          const u32x2 p0 = trd(tb + ks * 4096 + ct * 32), p1 = trd(tb + ks * 4096 + 512 + ct * 32);
          acc[ct] = mfma(xa[ks], u32x4{p0[0], p0[1], p1[0], p1[1]}, acc[ct]);
        }
      }
      const int qo = WS_Q + ((cb >> 3) * 8 + ((lane >> 4) & 1) * 4 + (lane >> 5) * 2) * QSTR + rb * 4096 + ((cb & 7) * 32 + (lane & 15)) * 16;
#pragma unroll
      for (int cp = 0; cp < 2; ++cp)
#pragma unroll
        for (int rr = 0; rr < 2; ++rr) {
          const u32x4 qv = {__float_as_uint(lane >= 32 ? acc[2 * cp][rr + 2] : acc[2 * cp][rr]), tag,
                            __float_as_uint(lane >= 32 ? acc[2 * cp + 1][rr + 2] : acc[2 * cp + 1][rr]), tag};
          ST128(qv, rws, qo + rr * QSTR + cp * 256, STAUX);
        }
    }
    STAMP(2);
    __builtin_amdgcn_s_sleep(QSLEEP);
    STAMP(6);
    {
      const int qoff = WS_Q + (kvh * 8 + b) * QSTR + lane * 16;
      u32x4 qg[16];
      for (unsigned n = 0; n < SPIN_CAP; ++n) {
        MEM_FENCE();
        bool ok = true;
#pragma unroll
        for (int i = 0; i < 16; ++i) qg[i] = LD128(rws, qoff + i * 1024, LDAUX);
#pragma unroll
        for (int i = 0; i < 16; ++i) ok = ok && qg[i][1] == tag && qg[i][3] == tag;
        if (__all(ok)) break;
      }
#pragma unroll
      for (int i = 0; i < 4; ++i) {
        const float a0 = (__uint_as_float(qg[i][0]) + __uint_as_float(qg[4 + i][0])) + (__uint_as_float(qg[8 + i][0]) + __uint_as_float(qg[12 + i][0]));
        const float a1 = (__uint_as_float(qg[i][2]) + __uint_as_float(qg[4 + i][2])) + (__uint_as_float(qg[8 + i][2]) + __uint_as_float(qg[12 + i][2]));
        const unsigned pk = pk_bf16(a0, a1);
        LAS u16* qd = (LAS u16*)(L + L_Q) + i * 128 + (lane >> 4) * 32 + (lane & 15);
        qd[0] = (u16)pk;
        qd[16] = (u16)(pk >> 16);
      }
      LGKM0();
      if (lane == 0) flag[F_QRDY] = 1u;
    }
    STAMP(3);
    lds_wait_ge(flag + F_KCNT, 6u);
    __builtin_amdgcn_s_sleep(10);
    const int soff = WS_S + (b * 8 + kvh) * 8192 + lane * 16;
    u32x4 sg[8];
    for (unsigned n = 0; n < SPIN_CAP; ++n) {
      MEM_FENCE();
      bool ok = true;
#pragma unroll
      for (int i = 0; i < 8; ++i) sg[i] = LD128(rws, soff + i * 1024, LDAUX);
#pragma unroll
      for (int i = 0; i < 8; ++i) ok = ok && sg[i][1] == tag && sg[i][3] == tag;
      if (__all(ok)) break;
    }
    STAMP(4);
    float m0 = -INFINITY, m1 = -INFINITY;
#pragma unroll
    for (int i = 0; i < 8; ++i) {
      m0 = fmaxf(m0, fmaxf(bf_lo(sg[i][0]), bf_lo(sg[i][2])));
      m1 = fmaxf(m1, fmaxf(bf_hi(sg[i][0]), bf_hi(sg[i][2])));
    }
    m0 = wave_max(m0); m1 = wave_max(m1);
    constexpr float C2 = 0.0625f * 1.4426950408889634f;
    float e0[16], e1[16], l0 = 0.f, l1 = 0.f;
#pragma unroll
    for (int i = 0; i < 8; ++i) {
      e0[2 * i] = __builtin_amdgcn_exp2f((bf_lo(sg[i][0]) - m0) * C2);
      e0[2 * i + 1] = __builtin_amdgcn_exp2f((bf_lo(sg[i][2]) - m0) * C2);
      e1[2 * i] = __builtin_amdgcn_exp2f((bf_hi(sg[i][0]) - m1) * C2);
      e1[2 * i + 1] = __builtin_amdgcn_exp2f((bf_hi(sg[i][2]) - m1) * C2);
      l0 += e0[2 * i] + e0[2 * i + 1];
      l1 += e1[2 * i] + e1[2 * i + 1];
    }
    l0 = __builtin_amdgcn_rcpf(wave_sum(l0)); l1 = __builtin_amdgcn_rcpf(wave_sum(l1));
#pragma unroll
    for (int i = 0; i < 8; ++i) {
      *(LAS unsigned*)(L + L_P + i * 256 + lane * 4) = pk_bf16(e0[2 * i] * l0, e0[2 * i + 1] * l0);
      *(LAS unsigned*)(L + L_P + 2048 + i * 256 + lane * 4) = pk_bf16(e1[2 * i] * l1, e1[2 * i + 1] * l1);
    }
    LGKM0();
    if (lane == 0) flag[F_PRDY] = 1u;
    STAMP(5);
    if (bid < 224) {
      lds_wait_ge(flag + F_OCNT, 8u);
      __builtin_amdgcn_s_sleep(24);
      const int poff = WS_PO + bid * PSTR + lane * 16;
      u32x4 pg[4];
      for (unsigned n = 0; n < SPIN_CAP; ++n) {
        MEM_FENCE();
        bool ok = true;
#pragma unroll
        for (int i = 0; i < 4; ++i) pg[i] = LD128(rws, poff + i * 1024, LDAUX);
#pragma unroll
        for (int i = 0; i < 4; ++i) ok = ok && pg[i][1] == tag && pg[i][3] == tag;
        if (__all(ok)) break;
      }
      float a0 = (__uint_as_float(pg[0][0]) + __uint_as_float(pg[1][0])) + (__uint_as_float(pg[2][0]) + __uint_as_float(pg[3][0]));
      float a1 = (__uint_as_float(pg[0][2]) + __uint_as_float(pg[1][2])) + (__uint_as_float(pg[2][2]) + __uint_as_float(pg[3][2]));
      a0 += dpp<ROR8>(a0); a1 += dpp<ROR8>(a1);
      float o, t;
      t = x16_a(a0, o); a0 = t + o; t = x16_a(a1, o); a1 = t + o;
      t = x32_a(a0, o); a0 = t + o; t = x32_a(a1, o); a1 = t + o;
      if (lane < 8) *(unsigned*)(out + lane * DM + bid * 2) = pk_bf16(a0, a1);
      if (bid == 0 && lane == 0) __hip_atomic_store((GAS unsigned*)(ws + WS_EPOCH), tag, __ATOMIC_RELAXED, __HIP_MEMORY_SCOPE_AGENT);
    }
  } else if (wave == 1) {
    unsigned tag;
    __builtin_amdgcn_s_setprio(3);
    if (fgrp < 0 || b == 7) {
      lds_wait_ge(flag + F_TAGV, 1u);
      tag = __builtin_amdgcn_readfirstlane(flag[F_TAG]);
    } else {
      const auto r1 = RSRC(w1, DM * FF * 2);
      u32x4 wr[8], xa[2];
      const int o1 = ((b * 64 + (lane >> 3)) * FF + fgrp * 64 + (lane & 7) * 8) * 2;
#pragma unroll
      for (int i = 0; i < 8; ++i) wr[i] = LD128(r1, o1 + i * 8 * FF * 2, AUX_NT);
#pragma unroll
      for (int i = 0; i < 2; ++i) xa[i] = *(const u32x4*)(x + (lane & 7) * DM + b * 64 + i * 32 + (lane >> 4) * 8);
      SCHED_FENCE();
      asm volatile("s_barrier" ::: "memory");
      lds_wait_ge(flag + F_TAGV, 1u);
      tag = __builtin_amdgcn_readfirstlane(flag[F_TAG]);
#pragma unroll
      for (int i = 0; i < 8; ++i) *(LAS u32x4*)(L + L_W1 + i * 1024 + lane * 16) = wr[i];
      LGKM0();
      LAS unsigned char* tb = L + L_W1 + ((lane >> 4) * 8 + ((lane >> 2) & 3)) * 128 + (lane & 3) * 8;
      f32x4 acc[4];
#pragma unroll
      for (int ct = 0; ct < 4; ++ct) {
        acc[ct] = f32x4{0, 0, 0, 0};
#pragma unroll
        for (int ks = 0; ks < 2; ++ks) {
          const u32x2 p0 = trd(tb + ks * 4096 + ct * 32), p1 = trd(tb + ks * 4096 + 512 + ct * 32);
          acc[ct] = mfma(xa[ks], u32x4{p0[0], p0[1], p1[0], p1[1]}, acc[ct]);
        }
      }
      const int ho = WS_H + (fgrp * 8 + ((lane >> 4) & 1) * 4 + (lane >> 5) * 2) * HSTR + b * 512 + (lane & 15) * 16;
#pragma unroll
      for (int cp = 0; cp < 2; ++cp)
#pragma unroll
        for (int rr = 0; rr < 2; ++rr) {
          const u32x4 qv = {__float_as_uint(lane >= 32 ? acc[2 * cp][rr + 2] : acc[2 * cp][rr]), tag,
                            __float_as_uint(lane >= 32 ? acc[2 * cp + 1][rr + 2] : acc[2 * cp + 1][rr]), tag};
          ST128(qv, rws, ho + rr * HSTR + cp * 256, STAUX);
        }
    }
    STAMP(1);
    float g;
    {
      u32x4 xg = {0, 0, 0, 0}, gg = {0, 0, 0, 0};
      if (lane < 56) {
        xg = *(const u32x4*)(x + b * DM + lane * 8);
        gg = *(const u32x4*)(gw + lane * 8);
      }
      float z = 0.f;
#pragma unroll
      for (int t = 0; t < 4; ++t) z += rbf(bf_lo(xg[t]) * bf_lo(gg[t])) + rbf(bf_hi(xg[t]) * bf_hi(gg[t]));
      z = rbf(wave_sum(z));
      g = rbf(1.f / (1.f + __expf(-z)));
    }
    if (b < 7) {
      const auto ro = RSRC(wo, NQ * DM * 2);
      const int offo = (kvh * 512 + s * 64 + (lane >> 3)) * 896 + b * 128 + (lane & 7) * 16;
      const unsigned lb = (unsigned)(uintptr_t)(L + L_WO);
#pragma unroll
      for (int i = 0; i < 16; ++i) {
        if (i >= WW) vmwait<WW - 1>();
        dma16(ro, offo + ((i >> 3) * 256 + (i & 7) * 8) * 896, lb + i * 1024);
      }
      if (fgrp >= 0) {
        const auto r2 = RSRC(w2, FF * DM * 2);
        const int off2 = (fgrp * 64 + (lane >> 3)) * 896 + b * 128 + (lane & 7) * 16;
#pragma unroll
        for (int i = 0; i < 8; ++i) {
          vmwait<WW - 1>();
          dma16(r2, off2 + i * 8 * 896, lb + 16384 + i * 1024);
        }
      }
    }
    STAMP(2);
    if (fgrp >= 0) {
      const int hoff = WS_H + (fgrp * 8 + b) * HSTR + lane * 16;
      u32x4 hg[4];
      for (unsigned n = 0; n < SPIN_CAP; ++n) {
        MEM_FENCE();
        bool ok = true;
#pragma unroll
        for (int i = 0; i < 4; ++i) hg[i] = LD128(rws, hoff + i * 1024, LDAUX);
#pragma unroll
        for (int i = 0; i < 3; ++i) ok = ok && hg[i][1] == tag && hg[i][3] == tag;
        ok = ok && (lane >= 32 || (hg[3][1] == tag && hg[3][3] == tag));
        if (__all(ok)) break;
        __builtin_amdgcn_s_sleep(8);
      }
      float a0 = (__uint_as_float(hg[0][0]) + __uint_as_float(hg[1][0])) + (__uint_as_float(hg[2][0]) + (lane < 32 ? __uint_as_float(hg[3][0]) : 0.f));
      float a1 = (__uint_as_float(hg[0][2]) + __uint_as_float(hg[1][2])) + (__uint_as_float(hg[2][2]) + (lane < 32 ? __uint_as_float(hg[3][2]) : 0.f));
      float o, t;
      t = x32_a(a0, o); a0 = rbf(t + o);
      t = x32_a(a1, o); a1 = rbf(t + o);
      const float g1 = rbf(1.f - g);
      a0 = rbf(a0 / (1.f + __expf(-a0))) * g1;
      a1 = rbf(a1 / (1.f + __expf(-a1))) * g1;
      const float n0 = dpp<XOR1>(a0), n1 = dpp<XOR1>(a1);
      const unsigned pk = (lane & 1) ? pk_bf16(n1, a1) : pk_bf16(a0, n0);
      if (lane < 32)
        __builtin_amdgcn_raw_buffer_store_b64(u32x2{pk, tag}, rws, WS_HB + (fgrp * 8 + b) * 256 + ((lane >> 4) * 16 + (lane & 1) * 8 + ((lane & 15) >> 1)) * 8, 0, STAUX);
    }
    STAMP(3);
    f32x4 acc[4] = {{0, 0, 0, 0}, {0, 0, 0, 0}, {0, 0, 0, 0}, {0, 0, 0, 0}};
    u32x4 wf[4][4];
    const bool hi8 = lane & 8;
    const int frag = (lane >> 4) * 32 + ((lane >> 3) & 1) * 16;
    if (b < 7) {
      vmwait<0>();
      LAS unsigned char* tb = L + L_WO + ((lane >> 4) * 8 + ((lane >> 2) & 3)) * 128 + (lane & 3) * 8;
      if (fgrp >= 0) {
        const int hoff = WS_HB + fgrp * 2048 + (lane & 7) * 256 + frag;
        u32x4 hb[2];
        for (unsigned n = 0; n < SPIN_CAP; ++n) {
          __builtin_amdgcn_s_sleep(16);
          MEM_FENCE();
          hb[0] = LD128(rws, hoff, LDAUX);
          hb[1] = LD128(rws, hoff + 128, LDAUX);
          if (__all(hb[0][1] == tag && hb[0][3] == tag && hb[1][1] == tag && hb[1][3] == tag)) break;
        }
#pragma unroll
        for (int ks = 0; ks < 2; ++ks) {
          const u32x4 bf = {hi8 ? 0u : hb[ks][0], hi8 ? 0u : hb[ks][2], hi8 ? hb[ks][0] : 0u, hi8 ? hb[ks][2] : 0u};
#pragma unroll
          for (int ct = 0; ct < 4; ++ct) {
            const u32x2 p0 = trd(tb + 16384 + ks * 4096 + ct * 32), p1 = trd(tb + 16384 + ks * 4096 + 512 + ct * 32);
            acc[ct] = mfma(u32x4{p0[0], p0[1], p1[0], p1[1]}, bf, acc[ct]);
          }
        }
      }
#pragma unroll
      for (int ks = 0; ks < 4; ++ks)
#pragma unroll
        for (int ct = 0; ct < 4; ++ct) {
          const u32x2 p0 = trd(tb + ks * 4096 + ct * 32), p1 = trd(tb + ks * 4096 + 512 + ct * 32);
          wf[ks][ct] = u32x4{p0[0], p0[1], p1[0], p1[1]};
          asm volatile("" : "+v"(wf[ks][ct]));
        }
    }
    STAMP(4);
    lds_wait_ge(flag + F_OCNT, 8u);
    {
      f32x2 ov = {0.f, 0.f};
#pragma unroll
      for (int w = 0; w < 8; ++w) ov += *(const LAS f32x2*)(L + L_OP + w * 512 + lane * 8);
      __builtin_amdgcn_raw_buffer_store_b64(u32x2{pk_bf16(ov[0] * g, ov[1] * g), tag}, rws, WS_OB + (grp * 8 + b) * 512 + lane * 8, 0, STAUX);
    }
    STAMP(5);
    if (b < 7) {
      const int ooff = WS_OB + grp * 4096 + (lane & 7) * 512 + frag;
      u32x4 ob[4];
      for (unsigned n = 0; n < SPIN_CAP; ++n) {
        MEM_FENCE();
        bool ok = true;
#pragma unroll
        for (int ks = 0; ks < 4; ++ks) ob[ks] = LD128(rws, ooff + ks * 128, LDAUX);
#pragma unroll
        for (int ks = 0; ks < 4; ++ks) ok = ok && ob[ks][1] == tag && ob[ks][3] == tag;
        if (__all(ok)) break;
      }
      STAMP(6);
#pragma unroll
      for (int ks = 0; ks < 4; ++ks) {
        const u32x4 bf = {hi8 ? 0u : ob[ks][0], hi8 ? 0u : ob[ks][2], hi8 ? ob[ks][0] : 0u, hi8 ? ob[ks][2] : 0u};
#pragma unroll
        for (int ct = 0; ct < 4; ++ct) acc[ct] = mfma(wf[ks][ct], bf, acc[ct]);
      }
      const int po = WS_PO + (b * 32 + (lane >> 3)) * PSTR + grp * 128 + (lane & 7) * 16;
#pragma unroll
      for (int ct = 0; ct < 4; ++ct) {
        const float k0 = hi8 ? acc[ct][2] : acc[ct][0], k1 = hi8 ? acc[ct][3] : acc[ct][1];
        const float s0 = hi8 ? acc[ct][0] : acc[ct][2], s1 = hi8 ? acc[ct][1] : acc[ct][3];
        const u32x4 pv = {__float_as_uint(k0 + dpp<ROR8>(s0)), tag, __float_as_uint(k1 + dpp<ROR8>(s1)), tag};
        ST128(pv, rws, po + ct * 8 * PSTR, STAUX);
      }
    }
  } else if (wave < 8) {
    __builtin_amdgcn_s_setprio(2);
    const int kw = wave - 2, u0 = kw < 2 ? kw * 6 : 2 + kw * 5, nu = kw < 2 ? 6 : 5;
    const int urot = u0 + (bid >> 3) * 3, brot = wave + (bid >> 3);
    const auto rk = RSRC(kc + kvbase + s * 256 * DH, 256 * DH * 2);
    u32x4 r[24];
    const int lo = (lane & 7) * 512 + ((lane >> 3) & 1) * 64 + (lane >> 4) * 16;
    const int lo6 = kw < 2 ? lo : 0x7f000000;
#pragma unroll
    for (int i = 0; i < 24; ++i) {
      if (i >= WK) vmwait<WK - 1>();
      const int so = ((urot + (i >> 2)) & 31) * 4096 + ((brot + i) & 3) * 128;
      r[i] = __builtin_amdgcn_raw_buffer_load_b128(rk, i < 20 ? lo : lo6, so, AUX_NT);
      SCHED_FENCE();
      if (i == 19) lds_inc(L, F_KISS, lane);
    }
    STAMP(1);
    lds_wait_ge(flag + F_QRDY, 1u);
    const unsigned tag = __builtin_amdgcn_readfirstlane(flag[F_TAG]);
    STAMP(2);
    u32x4 qa[4];
#pragma unroll
    for (int t = 0; t < 4; ++t) qa[t] = *(const LAS u32x4*)(L + L_Q + (lane & 1) * 512 + ((brot + t) & 3) * 128 + ((lane >> 1) & 1) * 64 + (lane >> 4) * 16);
    float s0 = 0.f, s1 = 0.f;
#pragma unroll
    for (int u = 0; u < 6; ++u) {
      f32x4 c = {0, 0, 0, 0};
#pragma unroll
      for (int t = 0; t < 4; ++t) c = mfma(qa[t], r[4 * u + t], c);
      float v0 = (lane & 8) ? c[2] : c[0], v1 = (lane & 8) ? c[3] : c[1];
      v0 += dpp<ROR8>(v0); v1 += dpp<ROR8>(v1);
      if ((lane >> 3) == u) { s0 = v0; s1 = v1; }
    }
    if ((lane >> 3) < nu)
      __builtin_amdgcn_raw_buffer_store_b64(u32x2{pk_bf16(s0, s1), tag}, rws,
                                            WS_S + (b * 8 + kvh) * 8192 + (s * 256 + ((urot + (lane >> 3)) & 31) * 8 + (lane & 7)) * 8, 0, STAUX);
    lds_inc(L, F_KCNT, lane);
    STAMP(3);
  } else {
    const int w = wave - 8;
    __builtin_amdgcn_s_setprio(1);
    const auto rv = RSRC(vc + kvbase, WIN * DH * 2);
    u32x4 r[16];
    const int trot = w + (bid >> 3);
    const int voff = (w * 128 + (lane >> 4) * 8 + ((lane >> 3) & 1)) * 512 + s * 128 + (lane & 7) * 16;
    const Sel sel = make_sel(lane);
    lds_wait_ge(flag + F_KISS, w < 4 ? 3u : 6u);
    STAMP(2);
#define V_ISSUE(i) do { if ((i) >= WV) vmwait<WV - 1>(); \
      r[i] = __builtin_amdgcn_raw_buffer_load_b128(rv, voff + ((i) & 3) * 1024, ((((i) >> 2) + trot) & 3) * 16384, AUX_NT); SCHED_FENCE(); } while (0)
#define V_USE(i) do { a0 = mfma(place(pp[(i) >> 2][(i) & 3], sel.lo), r[i], a0); a1 = mfma(place(pp[(i) >> 2][(i) & 3], sel.hi), r[i], a1); } while (0)
#pragma unroll
    for (int i = 0; i < VSPLIT; ++i) V_ISSUE(i);
    STAMP(3);
    lds_wait_ge(flag + F_PRDY, 1u);
    STAMP(4);
    u32x4 pp[4];
#pragma unroll
    for (int t = 0; t < 4; ++t) pp[t] = *(const LAS u32x4*)(L + L_P + ((lane >> 3) & 1) * 2048 + (w * 128 + ((t + trot) & 3) * 32 + (lane >> 4) * 8) * 2);
    f32x4 a0 = {0, 0, 0, 0}, a1 = {0, 0, 0, 0};
    constexpr int PER = VSPLIT < 16 ? (VSPLIT - WV + (16 - VSPLIT) - 1) / (16 - VSPLIT) + 1 : 0;
#pragma unroll
    for (int i = VSPLIT; i < 16; ++i) {
      V_ISSUE(i);
#pragma unroll
      for (int k = 0; k < 16; ++k)
        if (k >= (i - VSPLIT) * PER && k < (i - VSPLIT + 1) * PER && k <= i - WV) V_USE(k);
      SCHED_FENCE();
    }
#pragma unroll
    for (int k = 0; k < 16; ++k) {
      bool done = false;
#pragma unroll
      for (int i = VSPLIT; i < 16; ++i) done = done || (k >= (i - VSPLIT) * PER && k < (i - VSPLIT + 1) * PER && k <= i - WV);
      if (!done) V_USE(k);
    }
    f32x4 t = (lane & 8) ? a1 : a0;
#pragma unroll
    for (int k = 0; k < 4; ++k) t[k] += dpp<ROR8>(t[k]);
    if (!(lane & 8)) *(LAS f32x4*)(L + L_OP + w * 512 + ((lane >> 5) * 64 + (lane & 7) * 8 + ((lane >> 4) & 1) * 4) * 4) = t;
    LGKM0();
    lds_inc(L, F_OCNT, lane);
    STAMP(5);
  }
  STAMP(7);
  if (wave == 0 && bid == 8) {
    const unsigned long long d = (st[DSEL] - st[0]) * 8;
    while (__builtin_amdgcn_s_memrealtime() - st[7] < d) __builtin_amdgcn_s_sleep(1);
  }
}

extern "C" void kernel_launch(void* const* d_in, const int* in_sizes, int n_in, void* d_out, int out_size, void* d_ws, size_t ws_size,
                              hipStream_t stream) {
  fused_kernel<<<256, 1024, 0, stream>>>((const u16*)d_in[0], (const u16*)d_in[1], (const u16*)d_in[2], (const u16*)d_in[3], (const u16*)d_in[4],
                                         (const u16*)d_in[6], (const u16*)d_in[7], (const u16*)d_in[8], (u16*)d_out, (unsigned char*)d_ws + (WS_AT_END ? ws_size - (8u << 20) : 0));
}
```

```cpp
#include <hip/hip_runtime.h>
#include <hip/hip_bf16.h>
#include <stdint.h>

#define STAMPS 0
#define QSLEEP 40
#define WK 3
#define WV 2
#define WW 3
#define VSPLIT 12

typedef unsigned u32x4 __attribute__((ext_vector_type(4)));
typedef unsigned u32x2 __attribute__((ext_vector_type(2)));
typedef float f32x4 __attribute__((ext_vector_type(4)));
typedef float f32x2 __attribute__((ext_vector_type(2)));
typedef short s16x4 __attribute__((ext_vector_type(4)));
typedef short s16x8 __attribute__((ext_vector_type(8)));
typedef __bf16 bf16x2 __attribute__((ext_vector_type(2)));
typedef __bf16 bf16x8 __attribute__((ext_vector_type(8)));
typedef unsigned short u16;
#define LAS __attribute__((address_space(3)))
#define GAS __attribute__((address_space(1)))

constexpr int NB = 8, DM = 448, HKV = 8, SK = 4096, DH = 256, WIN = 1024, FF = 1792, NQ = 4096;
constexpr int QSTR = 16384 + 512, HSTR = 4096 + 512, PSTR = 4096 + 128;
constexpr int WS_EPOCH = 0;
constexpr int WS_Q = 16384;
constexpr int WS_H = WS_Q + 64 * QSTR;
constexpr int WS_S = WS_H + 224 * HSTR;
constexpr int WS_HB = WS_S + 524288;
constexpr int WS_OB = WS_HB + 65536;
constexpr int WS_PO = WS_OB + 131072;
constexpr int WS_END = WS_PO + 224 * PSTR;
constexpr int WS_STAMP = 4 << 20;
constexpr int L_FLAG = 0, L_Q = 256, L_P = L_Q + 1024, L_OP = L_P + 4096, L_WQ = L_OP + 4096, L_W1 = L_WQ + 14336, L_WO = L_W1 + 8192,
              L_W2 = L_WO + 16384, L_END = L_W2 + 8192;
enum { F_TAGV = 0, F_TAG = 1, F_QRDY = 2, F_PRDY = 3, F_KCNT = 4, F_KISS = 5, F_OCNT = 6 };
#define LDAUX 16
#define STAUX 16
#define WS_AT_END 0
constexpr int AUX_NT = 2, AUX_SC1 = 16;
constexpr unsigned SPIN_CAP = 1u << 18;

#define RSRC(p, n) __builtin_amdgcn_make_buffer_rsrc((void*)(p), 0, (n), 0x00020000)
#define LD128(r, off, aux) __builtin_amdgcn_raw_buffer_load_b128((r), (off), 0, (aux))
#define ST128(v, r, off, aux) __builtin_amdgcn_raw_buffer_store_b128((v), (r), (off), 0, (aux))
#define SCHED_FENCE() __builtin_amdgcn_sched_barrier(0)
#define MEM_FENCE() asm volatile("" ::: "memory")
template <int N> __device__ __forceinline__ void vmwait() { asm volatile("s_waitcnt vmcnt(%0)" ::"n"(N) : "memory"); }
#define LGKM0() asm volatile("s_waitcnt lgkmcnt(0)" ::: "memory")

__device__ __forceinline__ f32x4 mfma(u32x4 a, u32x4 b, f32x4 c) {
  return __builtin_amdgcn_mfma_f32_16x16x32_bf16(__builtin_bit_cast(bf16x8, a), __builtin_bit_cast(bf16x8, b), c, 0, 0, 0);
}
__device__ __forceinline__ u32x2 trd(LAS unsigned char* p) {
  return __builtin_bit_cast(u32x2, __builtin_amdgcn_ds_read_tr16_b64_v4i16((LAS s16x4*)p));
}
__device__ __forceinline__ unsigned pk_bf16(float lo, float hi) {
  f32x2 v = {lo, hi};
  return __builtin_bit_cast(unsigned, __builtin_convertvector(v, bf16x2));
}
__device__ __forceinline__ float bf_lo(unsigned u) { return __uint_as_float(u << 16); }
__device__ __forceinline__ float bf_hi(unsigned u) { return __uint_as_float(u & 0xffff0000u); }
__device__ __forceinline__ float rbf(float f) { return bf_lo(pk_bf16(f, f)); }
template <int CTRL> __device__ __forceinline__ float dpp(float x) {
  return __builtin_bit_cast(float, __builtin_amdgcn_mov_dpp(__builtin_bit_cast(int, x), CTRL, 0xf, 0xf, true));
}
constexpr int XOR1 = 0xB1, XOR2 = 0x4E, HMIRROR = 0x141, ROR8 = 0x128;
__device__ __forceinline__ float x16_a(float x, float& o) {
  auto s = __builtin_amdgcn_permlane16_swap(__float_as_uint(x), __float_as_uint(x), false, false);
  o = __uint_as_float(s[1]);
  return __uint_as_float(s[0]);
}
__device__ __forceinline__ float x32_a(float x, float& o) {
  auto s = __builtin_amdgcn_permlane32_swap(__float_as_uint(x), __float_as_uint(x), false, false);
  o = __uint_as_float(s[1]);
  return __uint_as_float(s[0]);
}
__device__ __forceinline__ float wave_max(float x) {
  x = fmaxf(x, dpp<XOR1>(x)); x = fmaxf(x, dpp<XOR2>(x)); x = fmaxf(x, dpp<HMIRROR>(x)); x = fmaxf(x, dpp<ROR8>(x));
  float o, a = x16_a(x, o); x = fmaxf(a, o);
  a = x32_a(x, o); return fmaxf(a, o);
}
__device__ __forceinline__ float wave_sum(float x) {
  x += dpp<XOR1>(x); x += dpp<XOR2>(x); x += dpp<HMIRROR>(x); x += dpp<ROR8>(x);
  float o, a = x16_a(x, o); x = a + o;
  a = x32_a(x, o); return a + o;
}
__device__ __forceinline__ void lds_wait_ge(volatile LAS unsigned* f, unsigned want) {
  for (unsigned n = 0; n < (1u << 22); ++n) {
    if (__builtin_amdgcn_readfirstlane(*f) >= want) break;
    __builtin_amdgcn_s_sleep(1);
  }
  MEM_FENCE();
}
__device__ __forceinline__ void lds_inc(LAS unsigned char* L, int f, int lane) {
  if (lane == 0) __hip_atomic_fetch_add((LAS unsigned*)L + f, 1u, __ATOMIC_RELAXED, __HIP_MEMORY_SCOPE_WORKGROUP);
}
__device__ __forceinline__ void dma16(__amdgpu_buffer_rsrc_t r, int voff, unsigned ldsdst) {
  unsigned keep;
  asm volatile("s_mov_b32 %0, m0\n\ts_mov_b32 m0, %3\n\ts_nop 0\n\tbuffer_load_dwordx4 %1, %2, 0 offen nt lds\n\ts_mov_b32 m0, %0"
               : "=&s"(keep) : "v"(voff), "s"(r), "s"(ldsdst) : "memory");
}
struct Sel { unsigned lo[4], hi[4]; };
__device__ __forceinline__ Sel make_sel(int lane) {
  Sel s;
  const int j = lane & 7;
#pragma unroll
  for (int t = 0; t < 4; ++t) {
    const bool on = (j >> 1) == t;
    s.lo[t] = on ? ((j & 1) ? 0x01000c0cu : 0x0c0c0100u) : 0x0c0c0c0cu;
    s.hi[t] = on ? ((j & 1) ? 0x03020c0cu : 0x0c0c0302u) : 0x0c0c0c0cu;
  }
  return s;
}
__device__ __forceinline__ u32x4 place(unsigned src, const unsigned (&sel)[4]) {
  u32x4 r;
#pragma unroll
  for (int t = 0; t < 4; ++t) r[t] = __builtin_amdgcn_perm(src, src, sel[t]);
  return r;
}

#define STAMP(i) do {} while (0)

__global__ void __launch_bounds__(1024) fused_kernel(const u16* __restrict__ x, const u16* __restrict__ kc, const u16* __restrict__ vc,
                                                     const u16* __restrict__ wq, const u16* __restrict__ wo, const u16* __restrict__ w1,
                                                     const u16* __restrict__ w2, const u16* __restrict__ gw, u16* __restrict__ out,
                                                     unsigned char* __restrict__ ws) {
  __shared__ __attribute__((aligned(16))) unsigned char smem[L_END];
  LAS unsigned char* L = (LAS unsigned char*)smem;
  volatile LAS unsigned* flag = (volatile LAS unsigned*)(L + L_FLAG);
  STAMP(0);
  const int bid = blockIdx.x, kvh = bid & 7, b = (bid >> 3) & 7, s = bid >> 6;
  const int tid = threadIdx.x, lane = tid & 63, wave = __builtin_amdgcn_readfirstlane(tid >> 6);
  const int grp = kvh * 4 + s;
  const int fgrp = s < 3 ? kvh * 3 + s : (kvh < 4 ? 24 + kvh : -1);
  const size_t kvbase = ((size_t)(b * HKV + kvh) * SK + (SK - WIN)) * DH;
  const auto rws = RSRC(ws, WS_END);
  if (tid < 16) flag[tid] = 0u;
  asm volatile("s_waitcnt lgkmcnt(0)" ::"s"(x), "s"(kc), "s"(vc), "s"(wq), "s"(wo), "s"(w1), "s"(w2), "s"(gw), "s"(out), "s"(ws) : "memory");
  if (wave >= 2 || (wave == 1 && (fgrp < 0 || b == 7))) asm volatile("s_barrier" ::: "memory");

  if (wave == 0) {
    __builtin_amdgcn_s_setprio(3);
    unsigned epoch, tag;
    asm volatile("s_load_dword %0, %1, 0x0" : "=s"(epoch) : "s"(ws) : "memory");
    {
      const int cb = ((bid >> 3) & 15) + 16 * (kvh >> 1), rb = (bid >> 7) + 2 * (kvh & 1);
      const auto rq = RSRC(wq, DM * NQ * 2);
      u32x4 wr[14], xa[3];
      const int qoffs = ((rb * 112 + (lane >> 3)) * NQ + cb * 64 + (lane & 7) * 8) * 2;
#pragma unroll
      for (int i = 0; i < 14; ++i) wr[i] = LD128(rq, qoffs + i * 8 * NQ * 2, AUX_NT);
      const u16* xp = x + (lane & 7) * DM + rb * 112;
#pragma unroll
      for (int i = 0; i < 3; ++i) xa[i] = *(const u32x4*)(xp + i * 32 + (lane >> 4) * 8);
      const u32x2 xb = *(const u32x2*)(xp + 96 + (lane >> 4) * 4);
      SCHED_FENCE();
      asm volatile("s_barrier\n\ts_waitcnt lgkmcnt(0)" : "+s"(epoch)::"memory");
      tag = epoch * 1664525u + 1013904223u;
      if (lane == 0) flag[F_TAG] = tag;
      LGKM0();
      if (lane == 0) flag[F_TAGV] = 1u;
#pragma unroll
      for (int i = 0; i < 14; ++i) *(LAS u32x4*)(L + L_WQ + i * 1024 + lane * 16) = wr[i];
      STAMP(1);
      LGKM0();
      LAS unsigned char* tb = L + L_WQ + ((lane >> 4) * 8 + ((lane >> 2) & 3)) * 128 + (lane & 3) * 8;
      LAS unsigned char* tb2 = L + L_WQ + (96 + (lane >> 4) * 4 + ((lane >> 2) & 3)) * 128 + (lane & 3) * 8;
      f32x4 acc[4];
#pragma unroll
      for (int ct = 0; ct < 4; ++ct) {
        const u32x2 pt = trd(tb2 + ct * 32);
        acc[ct] = mfma(u32x4{xb[0], xb[1], 0u, 0u}, u32x4{pt[0], pt[1], 0u, 0u}, f32x4{0, 0, 0, 0});
#pragma unroll
        for (int ks = 0; ks < 3; ++ks) {
          const u32x2 p0 = trd(tb + ks * 4096 + ct * 32), p1 = trd(tb + ks * 4096 + 512 + ct * 32);
          acc[ct] = mfma(xa[ks], u32x4{p0[0], p0[1], p1[0], p1[1]}, acc[ct]);
        }
      }
      const int qo = WS_Q + ((cb >> 3) * 8 + ((lane >> 4) & 1) * 4 + (lane >> 5) * 2) * QSTR + rb * 4096 + ((cb & 7) * 32 + (lane & 15)) * 16;
#pragma unroll
      for (int cp = 0; cp < 2; ++cp)
#pragma unroll
        for (int rr = 0; rr < 2; ++rr) {
          const u32x4 qv = {__float_as_uint(lane >= 32 ? acc[2 * cp][rr + 2] : acc[2 * cp][rr]), tag,
                            __float_as_uint(lane >= 32 ? acc[2 * cp + 1][rr + 2] : acc[2 * cp + 1][rr]), tag};
          ST128(qv, rws, qo + rr * QSTR + cp * 256, STAUX);
        }
    }
    STAMP(2);
    __builtin_amdgcn_s_sleep(QSLEEP);
    STAMP(6);
    {
      const int qoff = WS_Q + (kvh * 8 + b) * QSTR + lane * 16;
      u32x4 qg[16];
      for (unsigned n = 0; n < SPIN_CAP; ++n) {
        MEM_FENCE();
        bool ok = true;
#pragma unroll
        for (int i = 0; i < 16; ++i) qg[i] = LD128(rws, qoff + i * 1024, LDAUX);
#pragma unroll
        for (int i = 0; i < 16; ++i) ok = ok && qg[i][1] == tag && qg[i][3] == tag;
        if (__all(ok)) break;
      }
#pragma unroll
      for (int i = 0; i < 4; ++i) {
        const float a0 = (__uint_as_float(qg[i][0]) + __uint_as_float(qg[4 + i][0])) + (__uint_as_float(qg[8 + i][0]) + __uint_as_float(qg[12 + i][0]));
        const float a1 = (__uint_as_float(qg[i][2]) + __uint_as_float(qg[4 + i][2])) + (__uint_as_float(qg[8 + i][2]) + __uint_as_float(qg[12 + i][2]));
        const unsigned pk = pk_bf16(a0, a1);
        LAS u16* qd = (LAS u16*)(L + L_Q) + i * 128 + (lane >> 4) * 32 + (lane & 15);
        qd[0] = (u16)pk;
        qd[16] = (u16)(pk >> 16);
      }
      LGKM0();
      if (lane == 0) flag[F_QRDY] = 1u;
    }
    STAMP(3);
    lds_wait_ge(flag + F_KCNT, 6u);
    __builtin_amdgcn_s_sleep(10);
    const int soff = WS_S + (b * 8 + kvh) * 8192 + lane * 16;
    u32x4 sg[8];
    for (unsigned n = 0; n < SPIN_CAP; ++n) {
      MEM_FENCE();
      bool ok = true;
#pragma unroll
      for (int i = 0; i < 8; ++i) sg[i] = LD128(rws, soff + i * 1024, LDAUX);
#pragma unroll
      for (int i = 0; i < 8; ++i) ok = ok && sg[i][1] == tag && sg[i][3] == tag;
      if (__all(ok)) break;
    }
    STAMP(4);
    float m0 = -INFINITY, m1 = -INFINITY;
#pragma unroll
    for (int i = 0; i < 8; ++i) {
      m0 = fmaxf(m0, fmaxf(bf_lo(sg[i][0]), bf_lo(sg[i][2])));
      m1 = fmaxf(m1, fmaxf(bf_hi(sg[i][0]), bf_hi(sg[i][2])));
    }
    m0 = wave_max(m0); m1 = wave_max(m1);
    constexpr float C2 = 0.0625f * 1.4426950408889634f;
    float e0[16], e1[16], l0 = 0.f, l1 = 0.f;
#pragma unroll
    for (int i = 0; i < 8; ++i) {
      e0[2 * i] = __builtin_amdgcn_exp2f((bf_lo(sg[i][0]) - m0) * C2);
      e0[2 * i + 1] = __builtin_amdgcn_exp2f((bf_lo(sg[i][2]) - m0) * C2);
      e1[2 * i] = __builtin_amdgcn_exp2f((bf_hi(sg[i][0]) - m1) * C2);
      e1[2 * i + 1] = __builtin_amdgcn_exp2f((bf_hi(sg[i][2]) - m1) * C2);
      l0 += e0[2 * i] + e0[2 * i + 1];
      l1 += e1[2 * i] + e1[2 * i + 1];
    }
    l0 = __builtin_amdgcn_rcpf(wave_sum(l0)); l1 = __builtin_amdgcn_rcpf(wave_sum(l1));
#pragma unroll
    for (int i = 0; i < 8; ++i) {
      *(LAS unsigned*)(L + L_P + i * 256 + lane * 4) = pk_bf16(e0[2 * i] * l0, e0[2 * i + 1] * l0);
      *(LAS unsigned*)(L + L_P + 2048 + i * 256 + lane * 4) = pk_bf16(e1[2 * i] * l1, e1[2 * i + 1] * l1);
    }
    LGKM0();
    if (lane == 0) flag[F_PRDY] = 1u;
    STAMP(5);
    if (bid < 224) {
      lds_wait_ge(flag + F_OCNT, 8u);
      __builtin_amdgcn_s_sleep(24);
      const int poff = WS_PO + bid * PSTR + lane * 16;
      u32x4 pg[4];
      for (unsigned n = 0; n < SPIN_CAP; ++n) {
        MEM_FENCE();
        bool ok = true;
#pragma unroll
        for (int i = 0; i < 4; ++i) pg[i] = LD128(rws, poff + i * 1024, LDAUX);
#pragma unroll
        for (int i = 0; i < 4; ++i) ok = ok && pg[i][1] == tag && pg[i][3] == tag;
        if (__all(ok)) break;
      }
      float a0 = (__uint_as_float(pg[0][0]) + __uint_as_float(pg[1][0])) + (__uint_as_float(pg[2][0]) + __uint_as_float(pg[3][0]));
      float a1 = (__uint_as_float(pg[0][2]) + __uint_as_float(pg[1][2])) + (__uint_as_float(pg[2][2]) + __uint_as_float(pg[3][2]));
      a0 += dpp<ROR8>(a0); a1 += dpp<ROR8>(a1);
      float o, t;
      t = x16_a(a0, o); a0 = t + o; t = x16_a(a1, o); a1 = t + o;
      t = x32_a(a0, o); a0 = t + o; t = x32_a(a1, o); a1 = t + o;
      if (lane < 8) *(unsigned*)(out + lane * DM + bid * 2) = pk_bf16(a0, a1);
      if (bid == 0 && lane == 0) __hip_atomic_store((GAS unsigned*)(ws + WS_EPOCH), tag, __ATOMIC_RELAXED, __HIP_MEMORY_SCOPE_AGENT);
    }
  } else if (wave == 1) {
    unsigned tag;
    __builtin_amdgcn_s_setprio(3);
    if (fgrp < 0 || b == 7) {
      lds_wait_ge(flag + F_TAGV, 1u);
      tag = __builtin_amdgcn_readfirstlane(flag[F_TAG]);
    } else {
      const auto r1 = RSRC(w1, DM * FF * 2);
      u32x4 wr[8], xa[2];
      const int o1 = ((b * 64 + (lane >> 3)) * FF + fgrp * 64 + (lane & 7) * 8) * 2;
#pragma unroll
      for (int i = 0; i < 8; ++i) wr[i] = LD128(r1, o1 + i * 8 * FF * 2, AUX_NT);
#pragma unroll
      for (int i = 0; i < 2; ++i) xa[i] = *(const u32x4*)(x + (lane & 7) * DM + b * 64 + i * 32 + (lane >> 4) * 8);
      SCHED_FENCE();
      asm volatile("s_barrier" ::: "memory");
      lds_wait_ge(flag + F_TAGV, 1u);
      tag = __builtin_amdgcn_readfirstlane(flag[F_TAG]);
#pragma unroll
      for (int i = 0; i < 8; ++i) *(LAS u32x4*)(L + L_W1 + i * 1024 + lane * 16) = wr[i];
      LGKM0();
      LAS unsigned char* tb = L + L_W1 + ((lane >> 4) * 8 + ((lane >> 2) & 3)) * 128 + (lane & 3) * 8;
      f32x4 acc[4];
#pragma unroll
      for (int ct = 0; ct < 4; ++ct) {
        acc[ct] = f32x4{0, 0, 0, 0};
#pragma unroll
        for (int ks = 0; ks < 2; ++ks) {
          const u32x2 p0 = trd(tb + ks * 4096 + ct * 32), p1 = trd(tb + ks * 4096 + 512 + ct * 32);
          acc[ct] = mfma(xa[ks], u32x4{p0[0], p0[1], p1[0], p1[1]}, acc[ct]);
        }
      }
      const int ho = WS_H + (fgrp * 8 + ((lane >> 4) & 1) * 4 + (lane >> 5) * 2) * HSTR + b * 512 + (lane & 15) * 16;
#pragma unroll
      for (int cp = 0; cp < 2; ++cp)
#pragma unroll
        for (int rr = 0; rr < 2; ++rr) {
          const u32x4 qv = {__float_as_uint(lane >= 32 ? acc[2 * cp][rr + 2] : acc[2 * cp][rr]), tag,
                            __float_as_uint(lane >= 32 ? acc[2 * cp + 1][rr + 2] : acc[2 * cp + 1][rr]), tag};
          ST128(qv, rws, ho + rr * HSTR + cp * 256, STAUX);
        }
    }
    STAMP(1);
    float g;
    {
      u32x4 xg = {0, 0, 0, 0}, gg = {0, 0, 0, 0};
      if (lane < 56) {
        xg = *(const u32x4*)(x + b * DM + lane * 8);
        gg = *(const u32x4*)(gw + lane * 8);
      }
      float z = 0.f;
#pragma unroll
      for (int t = 0; t < 4; ++t) z += rbf(bf_lo(xg[t]) * bf_lo(gg[t])) + rbf(bf_hi(xg[t]) * bf_hi(gg[t]));
      z = rbf(wave_sum(z));
      g = rbf(1.f / (1.f + __expf(-z)));
    }
    if (b < 7) {
      const auto ro = RSRC(wo, NQ * DM * 2);
      const int offo = (kvh * 512 + s * 64 + (lane >> 3)) * 896 + b * 128 + (lane & 7) * 16;
      const unsigned lb = (unsigned)(uintptr_t)(L + L_WO);
#pragma unroll
      for (int i = 0; i < 16; ++i) {
        if (i >= WW) vmwait<WW - 1>();
        dma16(ro, offo + ((i >> 3) * 256 + (i & 7) * 8) * 896, lb + i * 1024);
      }
      if (fgrp >= 0) {
        const auto r2 = RSRC(w2, FF * DM * 2);
        const int off2 = (fgrp * 64 + (lane >> 3)) * 896 + b * 128 + (lane & 7) * 16;
#pragma unroll
        for (int i = 0; i < 8; ++i) {
          vmwait<WW - 1>();
          dma16(r2, off2 + i * 8 * 896, lb + 16384 + i * 1024);
        }
      }
    }
    STAMP(2);
    if (fgrp >= 0) {
      const int hoff = WS_H + (fgrp * 8 + b) * HSTR + lane * 16;
      u32x4 hg[4];
      for (unsigned n = 0; n < SPIN_CAP; ++n) {
        MEM_FENCE();
        bool ok = true;
#pragma unroll
        for (int i = 0; i < 4; ++i) hg[i] = LD128(rws, hoff + i * 1024, LDAUX);
#pragma unroll
        for (int i = 0; i < 3; ++i) ok = ok && hg[i][1] == tag && hg[i][3] == tag;
        ok = ok && (lane >= 32 || (hg[3][1] == tag && hg[3][3] == tag));
        if (__all(ok)) break;
        __builtin_amdgcn_s_sleep(8);
      }
      float a0 = (__uint_as_float(hg[0][0]) + __uint_as_float(hg[1][0])) + (__uint_as_float(hg[2][0]) + (lane < 32 ? __uint_as_float(hg[3][0]) : 0.f));
      float a1 = (__uint_as_float(hg[0][2]) + __uint_as_float(hg[1][2])) + (__uint_as_float(hg[2][2]) + (lane < 32 ? __uint_as_float(hg[3][2]) : 0.f));
      float o, t;
      t = x32_a(a0, o); a0 = rbf(t + o);
      t = x32_a(a1, o); a1 = rbf(t + o);
      const float g1 = rbf(1.f - g);
      a0 = rbf(a0 / (1.f + __expf(-a0))) * g1;
      a1 = rbf(a1 / (1.f + __expf(-a1))) * g1;
      const float n0 = dpp<XOR1>(a0), n1 = dpp<XOR1>(a1);
      const unsigned pk = (lane & 1) ? pk_bf16(n1, a1) : pk_bf16(a0, n0);
      if (lane < 32)
        __builtin_amdgcn_raw_buffer_store_b64(u32x2{pk, tag}, rws, WS_HB + (fgrp * 8 + b) * 256 + ((lane >> 4) * 16 + (lane & 1) * 8 + ((lane & 15) >> 1)) * 8, 0, STAUX);
    }
    STAMP(3);
    f32x4 acc[4] = {{0, 0, 0, 0}, {0, 0, 0, 0}, {0, 0, 0, 0}, {0, 0, 0, 0}};
    u32x4 wf[4][4];
    const bool hi8 = lane & 8;
    const int frag = (lane >> 4) * 32 + ((lane >> 3) & 1) * 16;
    if (b < 7) {
      vmwait<0>();
      LAS unsigned char* tb = L + L_WO + ((lane >> 4) * 8 + ((lane >> 2) & 3)) * 128 + (lane & 3) * 8;
      if (fgrp >= 0) {
        const int hoff = WS_HB + fgrp * 2048 + (lane & 7) * 256 + frag;
        u32x4 hb[2];
        for (unsigned n = 0; n < SPIN_CAP; ++n) {
          __builtin_amdgcn_s_sleep(16);
          MEM_FENCE();
          hb[0] = LD128(rws, hoff, LDAUX);
          hb[1] = LD128(rws, hoff + 128, LDAUX);
          if (__all(hb[0][1] == tag && hb[0][3] == tag && hb[1][1] == tag && hb[1][3] == tag)) break;
        }
#pragma unroll
        for (int ks = 0; ks < 2; ++ks) {
          const u32x4 bf = {hi8 ? 0u : hb[ks][0], hi8 ? 0u : hb[ks][2], hi8 ? hb[ks][0] : 0u, hi8 ? hb[ks][2] : 0u};
#pragma unroll
          for (int ct = 0; ct < 4; ++ct) {
            const u32x2 p0 = trd(tb + 16384 + ks * 4096 + ct * 32), p1 = trd(tb + 16384 + ks * 4096 + 512 + ct * 32);
            acc[ct] = mfma(u32x4{p0[0], p0[1], p1[0], p1[1]}, bf, acc[ct]);
          }
        }
      }
#pragma unroll
      for (int ks = 0; ks < 4; ++ks)
#pragma unroll
        for (int ct = 0; ct < 4; ++ct) {
          const u32x2 p0 = trd(tb + ks * 4096 + ct * 32), p1 = trd(tb + ks * 4096 + 512 + ct * 32);
          wf[ks][ct] = u32x4{p0[0], p0[1], p1[0], p1[1]};
          asm volatile("" : "+v"(wf[ks][ct]));
        }
    }
    STAMP(4);
    lds_wait_ge(flag + F_OCNT, 8u);
    {
      f32x2 ov = {0.f, 0.f};
#pragma unroll
      for (int w = 0; w < 8; ++w) ov += *(const LAS f32x2*)(L + L_OP + w * 512 + lane * 8);
      __builtin_amdgcn_raw_buffer_store_b64(u32x2{pk_bf16(ov[0] * g, ov[1] * g), tag}, rws, WS_OB + (grp * 8 + b) * 512 + lane * 8, 0, STAUX);
    }
    STAMP(5);
    if (b < 7) {
      const int ooff = WS_OB + grp * 4096 + (lane & 7) * 512 + frag;
      u32x4 ob[4];
      for (unsigned n = 0; n < SPIN_CAP; ++n) {
        MEM_FENCE();
        bool ok = true;
#pragma unroll
        for (int ks = 0; ks < 4; ++ks) ob[ks] = LD128(rws, ooff + ks * 128, LDAUX);
#pragma unroll
        for (int ks = 0; ks < 4; ++ks) ok = ok && ob[ks][1] == tag && ob[ks][3] == tag;
        if (__all(ok)) break;
      }
      STAMP(6);
#pragma unroll
      for (int ks = 0; ks < 4; ++ks) {
        const u32x4 bf = {hi8 ? 0u : ob[ks][0], hi8 ? 0u : ob[ks][2], hi8 ? ob[ks][0] : 0u, hi8 ? ob[ks][2] : 0u};
#pragma unroll
        for (int ct = 0; ct < 4; ++ct) acc[ct] = mfma(wf[ks][ct], bf, acc[ct]);
      }
      const int po = WS_PO + (b * 32 + (lane >> 3)) * PSTR + grp * 128 + (lane & 7) * 16;
#pragma unroll
      for (int ct = 0; ct < 4; ++ct) {
        const float k0 = hi8 ? acc[ct][2] : acc[ct][0], k1 = hi8 ? acc[ct][3] : acc[ct][1];
        const float s0 = hi8 ? acc[ct][0] : acc[ct][2], s1 = hi8 ? acc[ct][1] : acc[ct][3];
        const u32x4 pv = {__float_as_uint(k0 + dpp<ROR8>(s0)), tag, __float_as_uint(k1 + dpp<ROR8>(s1)), tag};
        ST128(pv, rws, po + ct * 8 * PSTR, STAUX);
      }
    }
  } else if (wave < 8) {
    __builtin_amdgcn_s_setprio(2);
    const int kw = wave - 2, u0 = kw < 2 ? kw * 6 : 2 + kw * 5, nu = kw < 2 ? 6 : 5;
    const int urot = u0 + (bid >> 3) * 3, brot = wave + (bid >> 3);
    const auto rk = RSRC(kc + kvbase + s * 256 * DH, 256 * DH * 2);
    u32x4 r[24];
    const int lo = (lane & 7) * 512 + ((lane >> 3) & 1) * 64 + (lane >> 4) * 16;
    const int lo6 = kw < 2 ? lo : 0x7f000000;
#pragma unroll
    for (int i = 0; i < 24; ++i) {
      if (i >= WK) vmwait<WK - 1>();
      const int so = ((urot + (i >> 2)) & 31) * 4096 + ((brot + i) & 3) * 128;
      r[i] = __builtin_amdgcn_raw_buffer_load_b128(rk, i < 20 ? lo : lo6, so, AUX_NT);
      SCHED_FENCE();
      if (i == 19) lds_inc(L, F_KISS, lane);
    }
    STAMP(1);
    lds_wait_ge(flag + F_QRDY, 1u);
    const unsigned tag = __builtin_amdgcn_readfirstlane(flag[F_TAG]);
    STAMP(2);
    u32x4 qa[4];
#pragma unroll
    for (int t = 0; t < 4; ++t) qa[t] = *(const LAS u32x4*)(L + L_Q + (lane & 1) * 512 + ((brot + t) & 3) * 128 + ((lane >> 1) & 1) * 64 + (lane >> 4) * 16);
    float s0 = 0.f, s1 = 0.f;
#pragma unroll
    for (int u = 0; u < 6; ++u) {
      f32x4 c = {0, 0, 0, 0};
#pragma unroll
      for (int t = 0; t < 4; ++t) c = mfma(qa[t], r[4 * u + t], c);
      float v0 = (lane & 8) ? c[2] : c[0], v1 = (lane & 8) ? c[3] : c[1];
      v0 += dpp<ROR8>(v0); v1 += dpp<ROR8>(v1);
      if ((lane >> 3) == u) { s0 = v0; s1 = v1; }
    }
    if ((lane >> 3) < nu)
      __builtin_amdgcn_raw_buffer_store_b64(u32x2{pk_bf16(s0, s1), tag}, rws,
                                            WS_S + (b * 8 + kvh) * 8192 + (s * 256 + ((urot + (lane >> 3)) & 31) * 8 + (lane & 7)) * 8, 0, STAUX);
    lds_inc(L, F_KCNT, lane);
    STAMP(3);
  } else {
    const int w = wave - 8;
    __builtin_amdgcn_s_setprio(1);
    const auto rv = RSRC(vc + kvbase, WIN * DH * 2);
    u32x4 r[16];
    const int trot = w + (bid >> 3);
    const int voff = (w * 128 + (lane >> 4) * 8 + ((lane >> 3) & 1)) * 512 + s * 128 + (lane & 7) * 16;
    if (w == 7 && bid < 8) { __builtin_amdgcn_fence(__ATOMIC_RELEASE, ""); asm volatile("s_waitcnt vmcnt(0)" ::: "memory"); }
    const Sel sel = make_sel(lane);
    lds_wait_ge(flag + F_KISS, w < 4 ? 3u : 6u);
    STAMP(2);
#define V_ISSUE(i) do { if ((i) >= WV) vmwait<WV - 1>(); \
      r[i] = __builtin_amdgcn_raw_buffer_load_b128(rv, voff + ((i) & 3) * 1024, ((((i) >> 2) + trot) & 3) * 16384, AUX_NT); SCHED_FENCE(); } while (0)
#define V_USE(i) do { a0 = mfma(place(pp[(i) >> 2][(i) & 3], sel.lo), r[i], a0); a1 = mfma(place(pp[(i) >> 2][(i) & 3], sel.hi), r[i], a1); } while (0)
#pragma unroll
    for (int i = 0; i < VSPLIT; ++i) V_ISSUE(i);
    STAMP(3);
    lds_wait_ge(flag + F_PRDY, 1u);
    STAMP(4);
    u32x4 pp[4];
#pragma unroll
    for (int t = 0; t < 4; ++t) pp[t] = *(const LAS u32x4*)(L + L_P + ((lane >> 3) & 1) * 2048 + (w * 128 + ((t + trot) & 3) * 32 + (lane >> 4) * 8) * 2);
    f32x4 a0 = {0, 0, 0, 0}, a1 = {0, 0, 0, 0};
    constexpr int PER = VSPLIT < 16 ? (VSPLIT - WV + (16 - VSPLIT) - 1) / (16 - VSPLIT) + 1 : 0;
#pragma unroll
    for (int i = VSPLIT; i < 16; ++i) {
      V_ISSUE(i);
#pragma unroll
      for (int k = 0; k < 16; ++k)
        if (k >= (i - VSPLIT) * PER && k < (i - VSPLIT + 1) * PER && k <= i - WV) V_USE(k);
      SCHED_FENCE();
    }
#pragma unroll
    for (int k = 0; k < 16; ++k) {
      bool done = false;
#pragma unroll
      for (int i = VSPLIT; i < 16; ++i) done = done || (k >= (i - VSPLIT) * PER && k < (i - VSPLIT + 1) * PER && k <= i - WV);
      if (!done) V_USE(k);
    }
    f32x4 t = (lane & 8) ? a1 : a0;
#pragma unroll
    for (int k = 0; k < 4; ++k) t[k] += dpp<ROR8>(t[k]);
    if (!(lane & 8)) *(LAS f32x4*)(L + L_OP + w * 512 + ((lane >> 5) * 64 + (lane & 7) * 8 + ((lane >> 4) & 1) * 4) * 4) = t;
    LGKM0();
    lds_inc(L, F_OCNT, lane);
    STAMP(5);
  }
}

extern "C" void kernel_launch(void* const* d_in, const int* in_sizes, int n_in, void* d_out, int out_size, void* d_ws, size_t ws_size,
                              hipStream_t stream) {
  fused_kernel<<<256, 1024, 0, stream>>>((const u16*)d_in[0], (const u16*)d_in[1], (const u16*)d_in[2], (const u16*)d_in[3], (const u16*)d_in[4],
                                         (const u16*)d_in[6], (const u16*)d_in[7], (const u16*)d_in[8], (u16*)d_out, (unsigned char*)d_ws + (WS_AT_END ? ws_size - (8u << 20) : 0));
}
```

```cpp
#include <hip/hip_runtime.h>
#include <hip/hip_bf16.h>
#include <stdint.h>

#define STAMPS 0
#define QSLEEP1 16
#define QSLEEP2 16
#define FSLEEP 32
#define WK 4
#define WV 3
#define WT 2
#define SPOLL0 9
#define VSPLIT 12
#define VTRIG 15
#define KTRIG 22

typedef unsigned u32x4 __attribute__((ext_vector_type(4)));
typedef unsigned u32x2 __attribute__((ext_vector_type(2)));
typedef float f32x4 __attribute__((ext_vector_type(4)));
typedef float f32x2 __attribute__((ext_vector_type(2)));
typedef short s16x4 __attribute__((ext_vector_type(4)));
typedef short s16x8 __attribute__((ext_vector_type(8)));
typedef __bf16 bf16x2 __attribute__((ext_vector_type(2)));
typedef __bf16 bf16x8 __attribute__((ext_vector_type(8)));
typedef unsigned short u16;
#define LAS __attribute__((address_space(3)))
#define GAS __attribute__((address_space(1)))

constexpr int NB = 8, DM = 448, HKV = 8, SK = 4096, DH = 256, WIN = 1024, FF = 1792, NQ = 4096;
constexpr int PSTR = 4096 + 128;
constexpr int QPSTR = 4096 + 256, QSTR = 2048 + 128, HSTR = 256;
constexpr int WS_EPOCH = 0;
constexpr int WS_G = 4096;
constexpr int WS_QP = 16384;
constexpr int WS_Q = WS_QP + 256 * QPSTR;
constexpr int WS_HB = WS_Q + 64 * QSTR;
constexpr int WS_S = WS_HB + 65536;
constexpr int WS_OB = WS_S + 524288;
constexpr int WS_PO = WS_OB + 131072;
constexpr int WS_END = WS_PO + 224 * PSTR;
constexpr int WS_STAMP = 4 << 20;
constexpr int L_FLAG = 0, L_Q = 256, L_QO = L_Q + 1024, L_ST = L_QO + 512, L_FW = L_ST + 128, L_P = L_FW + 64, L_OP = L_P + 4096, L_WO = L_OP + 4096, L_W2 = L_WO + 16384,
              L_WQ = L_W2 + 8192, L_T = L_WQ + 14336, L_END = L_T + 28672;
enum { F_TAGV = 0, F_TAG = 1, F_QRDY = 2, F_WQC = 3, F_QPC = 4, F_KISS = 5, F_OCNT = 6, F_TCNT = 7, F_VISS = 8, F_SCNT = 9, F_FRDY = 10 };
#define LDAUX 16
#define STAUX 16
#define WS_AT_END 0
constexpr int AUX_NT = 2, AUX_SC1 = 16;
constexpr float C2 = 0.0625f * 1.4426950408889634f;
constexpr unsigned SPIN_CAP = 1u << 18;

#define RSRC(p, n) __builtin_amdgcn_make_buffer_rsrc((void*)(p), 0, (n), 0x00020000)
#define LD128(r, off, aux) __builtin_amdgcn_raw_buffer_load_b128((r), (off), 0, (aux))
#define ST128(v, r, off, aux) __builtin_amdgcn_raw_buffer_store_b128((v), (r), (off), 0, (aux))
#define SCHED_FENCE() __builtin_amdgcn_sched_barrier(0)
#define MEM_FENCE() asm volatile("" ::: "memory")
template <int N> __device__ __forceinline__ void vmwait() { asm volatile("s_waitcnt vmcnt(%0)" ::"n"(N) : "memory"); }
#define LGKM0() asm volatile("s_waitcnt lgkmcnt(0)" ::: "memory")

__device__ __forceinline__ f32x4 mfma(u32x4 a, u32x4 b, f32x4 c) {
  return __builtin_amdgcn_mfma_f32_16x16x32_bf16(__builtin_bit_cast(bf16x8, a), __builtin_bit_cast(bf16x8, b), c, 0, 0, 0);
}
__device__ __forceinline__ u32x2 trd(LAS unsigned char* p) {
  return __builtin_bit_cast(u32x2, __builtin_amdgcn_ds_read_tr16_b64_v4i16((LAS s16x4*)p));
}
__device__ __forceinline__ unsigned pk_bf16(float lo, float hi) {
  f32x2 v = {lo, hi};
  return __builtin_bit_cast(unsigned, __builtin_convertvector(v, bf16x2));
}
__device__ __forceinline__ float bf_lo(unsigned u) { return __uint_as_float(u << 16); }
__device__ __forceinline__ float bf_hi(unsigned u) { return __uint_as_float(u & 0xffff0000u); }
__device__ __forceinline__ float rbf(float f) { return bf_lo(pk_bf16(f, f)); }
template <int CTRL> __device__ __forceinline__ float dpp(float x) {
  return __builtin_bit_cast(float, __builtin_amdgcn_mov_dpp(__builtin_bit_cast(int, x), CTRL, 0xf, 0xf, true));
}
constexpr int XOR1 = 0xB1, XOR2 = 0x4E, HMIRROR = 0x141, ROR8 = 0x128;
__device__ __forceinline__ float x16_a(float x, float& o) {
  auto s = __builtin_amdgcn_permlane16_swap(__float_as_uint(x), __float_as_uint(x), false, false);
  o = __uint_as_float(s[1]);
  return __uint_as_float(s[0]);
}
__device__ __forceinline__ float x32_a(float x, float& o) {
  auto s = __builtin_amdgcn_permlane32_swap(__float_as_uint(x), __float_as_uint(x), false, false);
  o = __uint_as_float(s[1]);
  return __uint_as_float(s[0]);
}
__device__ __forceinline__ float wave_max(float x) {
  x = fmaxf(x, dpp<XOR1>(x)); x = fmaxf(x, dpp<XOR2>(x)); x = fmaxf(x, dpp<HMIRROR>(x)); x = fmaxf(x, dpp<ROR8>(x));
  float o, a = x16_a(x, o); x = fmaxf(a, o);
  a = x32_a(x, o); return fmaxf(a, o);
}
__device__ __forceinline__ float wave_sum(float x) {
  x += dpp<XOR1>(x); x += dpp<XOR2>(x); x += dpp<HMIRROR>(x); x += dpp<ROR8>(x);
  float o, a = x16_a(x, o); x = a + o;
  a = x32_a(x, o); return a + o;
}
__device__ __forceinline__ void lds_wait_ge(volatile LAS unsigned* f, unsigned want) {
#pragma nounroll
  for (unsigned n = 0; n < (1u << 22); ++n) {
    if (__builtin_amdgcn_readfirstlane(*f) >= want) break;
    __builtin_amdgcn_s_sleep(1);
  }
  MEM_FENCE();
}
__device__ __forceinline__ unsigned bperm(unsigned v, int srclane) { return __builtin_amdgcn_ds_bpermute(srclane << 2, v); }
__device__ __forceinline__ void lds_inc(LAS unsigned char* L, int f, int lane) {
  if (lane == 0) asm volatile("ds_add_u32 %0, %1" ::"v"((unsigned)(uintptr_t)(L + f * 4)), "v"(1u) : "memory");
}
__device__ __forceinline__ unsigned lds_inc_ret(LAS unsigned char* L, int f, int lane) {
  unsigned old = 0;
  if (lane == 0) asm volatile("ds_add_rtn_u32 %0, %1, %2\n\ts_waitcnt lgkmcnt(0)" : "=&v"(old) : "v"((unsigned)(uintptr_t)(L + f * 4)), "v"(1u) : "memory");
  return __builtin_amdgcn_readfirstlane(old);
}
__device__ __forceinline__ unsigned get_tag(volatile LAS unsigned* flag) {
  lds_wait_ge(flag + 0, 1u);
  return __builtin_amdgcn_readfirstlane(flag[1]);
}
__device__ __forceinline__ void dma16(__amdgpu_buffer_rsrc_t r, int voff, unsigned ldsdst) {
  unsigned keep;
  asm volatile("s_mov_b32 %0, m0\n\ts_mov_b32 m0, %3\n\ts_nop 0\n\tbuffer_load_dwordx4 %1, %2, 0 offen nt lds\n\ts_mov_b32 m0, %0"
               : "=&s"(keep) : "v"(voff), "s"(r), "s"(ldsdst) : "memory");
}
template <int N, class F>
__device__ __forceinline__ void gather2(u32x4 (&a)[N], unsigned tag, F ld) {
#pragma nounroll
  for (unsigned n = 0; n < SPIN_CAP; ++n) {
    MEM_FENCE();
#pragma unroll
    for (int i = 0; i < N; ++i) a[i] = ld(i);
    unsigned bad = 0;
#pragma unroll
    for (int i = 0; i < N; ++i) bad |= (a[i][1] ^ tag) | (a[i][3] ^ tag);
    if (__all(bad == 0u)) break;
  }
}
struct Sel { unsigned lo[4], hi[4]; };
__device__ __forceinline__ Sel make_sel(int lane) {
  Sel s;
  const int j = lane & 7;
#pragma unroll
  for (int t = 0; t < 4; ++t) {
    const bool on = (j >> 1) == t;
    s.lo[t] = on ? ((j & 1) ? 0x01000c0cu : 0x0c0c0100u) : 0x0c0c0c0cu;
    s.hi[t] = on ? ((j & 1) ? 0x03020c0cu : 0x0c0c0302u) : 0x0c0c0c0cu;
  }
  return s;
}
__device__ __forceinline__ u32x4 place(unsigned src, const unsigned (&sel)[4]) {
  u32x4 r;
#pragma unroll
  for (int t = 0; t < 4; ++t) r[t] = __builtin_amdgcn_perm(src, src, sel[t]);
  return r;
}

#define STAMP(i) do {} while (0)

__global__ void __launch_bounds__(1024) fused_kernel(const u16* __restrict__ x, const u16* __restrict__ kc, const u16* __restrict__ vc,
                                                     const u16* __restrict__ wq, const u16* __restrict__ wo, const u16* __restrict__ w1,
                                                     const u16* __restrict__ w2, const u16* __restrict__ gw, u16* __restrict__ out,
                                                     unsigned char* __restrict__ ws) {
  __shared__ __attribute__((aligned(16))) unsigned char smem[L_END];
  LAS unsigned char* L = (LAS unsigned char*)smem;
  volatile LAS unsigned* flag = (volatile LAS unsigned*)(L + L_FLAG);
  STAMP(0);
  if (threadIdx.x < 16) asm volatile("ds_write_b32 %0, %1" ::"v"((unsigned)(uintptr_t)L + threadIdx.x * 4), "v"(0u) : "memory");
  const int bid = blockIdx.x, kvh = bid & 7, b = (bid >> 3) & 7, s = bid >> 6;
  const int tid = threadIdx.x, lane = tid & 63, wave = __builtin_amdgcn_readfirstlane(tid >> 6);
  const int cb = ((bid >> 3) & 15) + 16 * (kvh >> 1), rb = (bid >> 7) + 2 * (kvh & 1);
  asm volatile("s_waitcnt lgkmcnt(0)" ::"s"(x), "s"(kc), "s"(vc), "s"(wq), "s"(wo), "s"(w1), "s"(w2), "s"(gw), "s"(out), "s"(ws) : "memory");
  unsigned epoch = 0;
  {
    const auto rq = RSRC(wq, DM * NQ * 2);
    const unsigned lb = (unsigned)(uintptr_t)(L + L_WQ) + (wave - 9) * 2048;
    const int o = ((rb * 112 + (wave - 9) * 16 + (lane >> 3)) * NQ + cb * 64 + (lane & 7) * 8) * 2;
    unsigned keep;
    asm volatile(
        "s_cmp_lt_u32 %[w], 9\n\ts_cbranch_scc1 1f\n\t"
        "s_mov_b32 %[k], m0\n\ts_mov_b32 m0, %[lb]\n\ts_nop 0\n\tbuffer_load_dwordx4 %[o0], %[r], 0 offen nt lds\n\t"
        "s_add_u32 m0, %[lb], 0x400\n\ts_nop 0\n\tbuffer_load_dwordx4 %[o1], %[r], 0 offen nt lds\n\ts_mov_b32 m0, %[k]\n"
        "1:\n\ts_cmp_lg_u32 %[w], 8\n\ts_cbranch_scc1 2f\n\ts_load_dword %[e], %[ws], 0x0\n"
        "2:\n\ts_barrier\n\ts_waitcnt lgkmcnt(0)"
        : [e] "+s"(epoch), [k] "=&s"(keep)
        : [w] "s"(wave), [lb] "s"(lb), [o0] "v"(o), [o1] "v"(o + 8 * NQ * 2), [r] "s"(rq), [ws] "s"(ws)
        : "memory", "scc");
  }
  const int grp = kvh * 4 + s;
  const size_t kvbase = ((size_t)(b * HKV + kvh) * SK + (SK - WIN)) * DH;
  const auto rws = RSRC(ws, WS_END);
  const int nkw = s == 1 ? 4 : 6;

  if (__builtin_expect(wave >= 1 && wave <= nkw, 1)) {
    const int kw = wave - 1;
    const int nu = (s & 1) && wave == nkw ? 4 : 6;
    const int u0 = (s == 0 ? 0 : s == 1 ? 36 : s == 2 ? 58 : 94) + kw * 6;
    const int brot = wave + (bid >> 3);
    const auto rk = RSRC(kc + kvbase, WIN * DH * 2);
    u32x4 r[24];
    const int urot = (b + kw) & 1;
    const int lo = (lane & 7) * 512 + ((lane >> 3) & 1) * 64 + (lane >> 4) * 16 + u0 * 4096;
    const int lo5 = nu == 6 ? lo : 0x7f000000;
    STAMP(4);
#pragma unroll
    for (int i = 0; i < 24; ++i) {
      if (i >= WK) vmwait<WK - 1>();
      if (i == WK) STAMP(5);
      r[i] = __builtin_amdgcn_raw_buffer_load_b128(rk, i < 16 ? lo : lo5, (((i >> 2) ^ urot) * 4096) + ((brot + i) & 3) * 128, AUX_NT);
      SCHED_FENCE();
      if (i == KTRIG) lds_inc(L, F_KISS, lane);
    }
    STAMP(1);
    __builtin_amdgcn_s_setprio(2);
    lds_wait_ge(flag + F_QRDY, 1u);
    const unsigned tag = __builtin_amdgcn_readfirstlane(flag[F_TAG]);
    STAMP(2);
    u32x4 qa[4];
#pragma unroll
    for (int t = 0; t < 4; ++t) qa[t] = *(const LAS u32x4*)(L + L_Q + (lane & 1) * 512 + ((brot + t) & 3) * 128 + ((lane >> 1) & 1) * 64 + (lane >> 4) * 16);
    float s0 = 0.f, s1 = 0.f;
#pragma unroll
    for (int u = 0; u < 6; ++u) {
      f32x4 c = {0, 0, 0, 0};
#pragma unroll
      for (int t = 0; t < 4; ++t) c = mfma(qa[t], r[4 * u + t], c);
      float v0 = (lane & 8) ? c[2] : c[0], v1 = (lane & 8) ? c[3] : c[1];
      v0 += dpp<ROR8>(v0); v1 += dpp<ROR8>(v1);
      if (((lane >> 3) ^ urot) == u) { s0 = v0; s1 = v1; }
    }
    const unsigned pk = pk_bf16(s0, s1);
    const unsigned pa = bperm(pk, (2 * lane) & 63), pb = bperm(pk, (2 * lane + 1) & 63);
    if (lane < 4 * nu) ST128((u32x4{pa, tag, pb, tag}), rws, WS_S + (b * 8 + kvh) * 8192 + u0 * 64 + lane * 16, STAUX);
    STAMP(3);
  } else if (wave >= 8) {
    const int w = wave - 8;
    unsigned tag;
    if (w == 0) {
      tag = epoch * 1664525u + 1013904223u;
      if (lane == 0) flag[F_TAG] = tag;
      LGKM0();
      if (lane == 0) flag[F_TAGV] = 1u;
    } else if (w < 4) {
      vmwait<0>();
      lds_inc(L, F_WQC, lane);
      tag = get_tag(flag);
    } else {
      __builtin_amdgcn_s_setprio(3);
      const int ct = w - 4;
      u32x4 xa[3];
      const u16* xp = x + (lane & 7) * DM + rb * 112;
#pragma unroll
      for (int i = 0; i < 3; ++i) xa[i] = *(const u32x4*)(xp + i * 32 + (lane >> 4) * 8);
      const u32x2 xb = *(const u32x2*)(xp + 96 + (lane >> 4) * 4);
      SCHED_FENCE();
      vmwait<4>();
      lds_inc(L, F_WQC, lane);
      LAS unsigned char* tb = L + L_WQ + ((lane >> 4) * 8 + ((lane >> 2) & 3)) * 128 + (lane & 3) * 8 + ct * 32;
      LAS unsigned char* tb2 = L + L_WQ + (96 + (lane >> 4) * 4 + ((lane >> 2) & 3)) * 128 + (lane & 3) * 8 + ct * 32;
      const int dest = ((lane >> 4) & 1) * 2 + (lane >> 5);
      const int qo = WS_QP + (cb * 4 + dest) * QPSTR + rb * 1024 + ct * 256 + (lane & 15) * 16;
      tag = get_tag(flag);
      lds_wait_ge(flag + F_WQC, 7u);
      STAMP(1);
      const u32x2 pt = trd(tb2);
      f32x4 acc = mfma(u32x4{xb[0], xb[1], 0u, 0u}, u32x4{pt[0], pt[1], 0u, 0u}, f32x4{0, 0, 0, 0});
#pragma unroll
      for (int ks = 0; ks < 3; ++ks) {
        const u32x2 p0 = trd(tb + ks * 4096), p1 = trd(tb + ks * 4096 + 512);
        acc = mfma(xa[ks], u32x4{p0[0], p0[1], p1[0], p1[1]}, acc);
      }
      const float v0 = lane >= 32 ? acc[2] : acc[0], v1 = lane >= 32 ? acc[3] : acc[1];
      if (dest != rb) ST128((u32x4{__float_as_uint(v0), tag, __float_as_uint(v1), tag}), rws, qo, STAUX);
      else *(LAS f32x2*)(L + L_QO + (ct * 16 + (lane & 15)) * 8) = f32x2{v0, v1};
      LGKM0();
      lds_inc(L, F_QPC, lane);
      __builtin_amdgcn_s_setprio(0);
    }
    const auto rv = RSRC(vc + kvbase, WIN * DH * 2);
    u32x4 r[16];
    const int trot = w + (bid >> 3);
    const int voff = (w * 128 + (lane >> 4) * 8 + ((lane >> 3) & 1)) * 512 + s * 128 + (lane & 7) * 16;
    const Sel sel = make_sel(lane);
    const int soff = WS_S + (b * 8 + kvh) * 8192 + w * 1024 + lane * 16;
    lds_wait_ge(flag + F_KISS, (unsigned)nkw);
    STAMP(2);
#define V_ISSUE(i) do { if ((i) > SPOLL0 && (i) < SPOLL0 + WV) vmwait<WV>(); else if ((i) >= WV) vmwait<WV - 1>(); \
      r[i] = __builtin_amdgcn_raw_buffer_load_b128(rv, voff + ((i) & 3) * 1024, ((((i) >> 2) + trot) & 3) * 16384, AUX_NT); SCHED_FENCE(); \
      if ((i) == VTRIG) lds_inc(L, F_VISS, lane); } while (0)
#define V_USE(i) do { a0 = mfma(place(pp[(i) >> 2][(i) & 3], sel.lo), r[i], a0); a1 = mfma(place(pp[(i) >> 2][(i) & 3], sel.hi), r[i], a1); } while (0)
#define S_OK() __all(sg[1] == tag && sg[3] == tag)
    u32x4 sg;
#pragma unroll
    for (int i = 0; i < VSPLIT; ++i) {
      V_ISSUE(i);
      if (i == SPOLL0) { MEM_FENCE(); sg = LD128(rws, soff, LDAUX); SCHED_FENCE(); }
    }
    STAMP(3);
    u32x4 pp[4];
    f32x4 a0 = {0, 0, 0, 0}, a1 = {0, 0, 0, 0};
#define P_MAKE() do { \
      const float m0 = wave_max(fmaxf(bf_lo(sg[0]), bf_lo(sg[2]))), m1 = wave_max(fmaxf(bf_hi(sg[0]), bf_hi(sg[2]))); \
      const float ea = __builtin_amdgcn_exp2f((bf_lo(sg[0]) - m0) * C2), eb = __builtin_amdgcn_exp2f((bf_lo(sg[2]) - m0) * C2); \
      const float ec = __builtin_amdgcn_exp2f((bf_hi(sg[0]) - m1) * C2), ed = __builtin_amdgcn_exp2f((bf_hi(sg[2]) - m1) * C2); \
      *(LAS unsigned*)(L + L_P + w * 512 + lane * 4) = pk_bf16(ea, eb); \
      *(LAS unsigned*)(L + L_P + w * 512 + 256 + lane * 4) = pk_bf16(ec, ed); \
      LGKM0(); \
      _Pragma("unroll") for (int t = 0; t < 4; ++t) \
        pp[t] = *(const LAS u32x4*)(L + L_P + w * 512 + ((lane >> 3) & 1) * 256 + (((t + trot) & 3) * 32 + (lane >> 4) * 8) * 2); \
      const float l0 = wave_sum(ea + eb), l1 = wave_sum(ec + ed); \
      if (lane == 0) *(LAS f32x4*)(L + L_ST + w * 16) = f32x4{m0, l0, m1, l1}; \
      LGKM0(); \
      lds_inc(L, F_SCNT, lane); } while (0)
    if (__builtin_expect(S_OK(), 1)) {
      STAMP(4);
      V_ISSUE(VSPLIT);
      P_MAKE();
      constexpr int NI = 15 - VSPLIT, PER = (VSPLIT + 1 - WV + NI - 1) / NI + 1;
#pragma unroll
      for (int i = VSPLIT + 1; i < 16; ++i) {
        V_ISSUE(i);
#pragma unroll
        for (int k = 0; k < 16; ++k)
          if (k >= (i - VSPLIT - 1) * PER && k < (i - VSPLIT) * PER && k <= i - WV) V_USE(k);
        SCHED_FENCE();
      }
#pragma unroll
      for (int k = 0; k < 16; ++k) {
        bool done = false;
#pragma unroll
        for (int i = VSPLIT + 1; i < 16; ++i) done = done || (k >= (i - VSPLIT - 1) * PER && k < (i - VSPLIT) * PER && k <= i - WV);
        if (!done) V_USE(k);
      }
    } else {
      MEM_FENCE();
      sg = LD128(rws, soff, LDAUX);
      SCHED_FENCE();
#pragma unroll
      for (int i = VSPLIT; i < 16; ++i) V_ISSUE(i);
      STAMP(6);
#pragma nounroll
      for (unsigned n = 0; n < SPIN_CAP; ++n) {
        if (S_OK()) break;
        MEM_FENCE();
        sg = LD128(rws, soff, LDAUX);
      }
      P_MAKE();
#pragma unroll
      for (int k = 0; k < 16; ++k) V_USE(k);
    }
    lds_wait_ge(flag + F_FRDY, 1u);
    const float f = *(const LAS float*)(L + L_FW + w * 8 + (lane >> 5) * 4);
    f32x4 t = (lane & 8) ? a1 : a0;
#pragma unroll
    for (int k = 0; k < 4; ++k) t[k] = (t[k] + dpp<ROR8>(t[k])) * f;
    if (!(lane & 8)) *(LAS f32x4*)(L + L_OP + w * 512 + ((lane >> 5) * 64 + (lane & 7) * 8 + ((lane >> 4) & 1) * 4) * 4) = t;
    STAMP(5);
    if (lds_inc_ret(L, F_OCNT, lane) == 7u) {
      f32x4 ov = {0.f, 0.f, 0.f, 0.f};
#pragma unroll
      for (int k = 0; k < 4; ++k) ov += *(const LAS f32x4*)(L + L_OP + ((lane >> 5) * 4 + k) * 512 + (lane & 31) * 16);
#pragma unroll
      for (int k = 0; k < 4; ++k) { float hi, lo = x32_a(ov[k], hi); ov[k] = lo + hi; }
      if (lane < 32) ST128((u32x4{pk_bf16(ov[0], ov[1]), tag, pk_bf16(ov[2], ov[3]), tag}), rws, WS_OB + (grp * 8 + b) * 512 + lane * 16, STAUX);
    }
  } else if (wave == 0) {
    __builtin_amdgcn_s_setprio(3);
    u32x4 xg = {0, 0, 0, 0}, gg = {0, 0, 0, 0};
    if (lane < 56) {
      xg = *(const u32x4*)(x + b * DM + lane * 8);
      gg = *(const u32x4*)(gw + lane * 8);
    }
    float z = 0.f;
#pragma unroll
    for (int t = 0; t < 4; ++t) z += rbf(bf_lo(xg[t]) * bf_lo(gg[t])) + rbf(bf_hi(xg[t]) * bf_hi(gg[t]));
    z = rbf(wave_sum(z));
    const float g = rbf(1.f / (1.f + __expf(-z)));
    const unsigned tag = get_tag(flag);
    const unsigned g1u = __float_as_uint(rbf(1.f - g));
    if (s == 0 && lane < 8) ST128((u32x4{g1u, tag, g1u, tag}), rws, WS_G + (kvh * 8 + b) * 128 + lane * 16, STAUX);
    lds_wait_ge(flag + F_QPC, 4u);
    STAMP(2);
    __builtin_amdgcn_s_sleep(QSLEEP1);
    {
      const int po = WS_QP + (cb * 4 + rb) * QPSTR + lane * 16;
      u32x4 pg[3];
      gather2<3>(pg, tag, [&](int i) { return LD128(rws, po + ((rb + 1 + i) & 3) * 1024, LDAUX); });
      const f32x2 own = *(const LAS f32x2*)(L + L_QO + lane * 8);
      const float a0 = (own[0] + __uint_as_float(pg[0][0])) + (__uint_as_float(pg[1][0]) + __uint_as_float(pg[2][0]));
      const float a1 = (own[1] + __uint_as_float(pg[0][2])) + (__uint_as_float(pg[1][2]) + __uint_as_float(pg[2][2]));
      const float n0 = dpp<XOR1>(a0), n1 = dpp<XOR1>(a1);
      const unsigned pk = (lane & 1) ? pk_bf16(n1, a1) : pk_bf16(a0, n0);
      const int src = (lane & 15) * 4 + ((lane >> 4) & 1);
      const unsigned pa = bperm(pk, src), pb = bperm(pk, src + 2);
      if (lane < 32) ST128((u32x4{pa, tag, pb, tag}), rws, WS_Q + ((cb >> 3) * 8 + rb * 2 + (lane >> 4)) * QSTR + (cb & 7) * 256 + (lane & 15) * 16, STAUX);
    }
    STAMP(6);
    __builtin_amdgcn_s_sleep(QSLEEP2);
    {
      const int qoff = WS_Q + (kvh * 8 + b) * QSTR + lane * 16;
      u32x4 qq[2];
      gather2<2>(qq, tag, [&](int i) { return LD128(rws, qoff + i * 1024, LDAUX); });
      *(LAS u32x2*)(L + L_Q + lane * 8) = u32x2{qq[0][0], qq[0][2]};
      *(LAS u32x2*)(L + L_Q + 512 + lane * 8) = u32x2{qq[1][0], qq[1][2]};
      LGKM0();
      if (lane == 0) flag[F_QRDY] = 1u;
    }
    STAMP(3);
    lds_wait_ge(flag + F_SCNT, 8u);
    {
      const f32x2 ml = *(const LAS f32x2*)(L + L_ST + (lane & 15) * 8);
      float M = fmaxf(ml[0], dpp<XOR2>(ml[0]));
      M = fmaxf(M, dpp<0x124>(M)); M = fmaxf(M, dpp<ROR8>(M));
      const float al = __builtin_amdgcn_exp2f((ml[0] - M) * C2);
      float Ls = al * ml[1];
      Ls += dpp<XOR2>(Ls); Ls += dpp<0x124>(Ls); Ls += dpp<ROR8>(Ls);
      if (lane < 16) *(LAS float*)(L + L_FW + lane * 4) = al * (g / Ls);
      LGKM0();
      if (lane == 0) flag[F_FRDY] = 1u;
    }
    STAMP(4);
    if (bid < 224) {
      lds_wait_ge(flag + F_OCNT, 8u);
      STAMP(5);
      __builtin_amdgcn_s_sleep(FSLEEP);
      const int poff = WS_PO + bid * PSTR + lane * 16;
      u32x4 pg[4];
      gather2<4>(pg, tag, [&](int i) { return LD128(rws, poff + i * 1024, LDAUX); });
      float a0 = (__uint_as_float(pg[0][0]) + __uint_as_float(pg[1][0])) + (__uint_as_float(pg[2][0]) + __uint_as_float(pg[3][0]));
      float a1 = (__uint_as_float(pg[0][2]) + __uint_as_float(pg[1][2])) + (__uint_as_float(pg[2][2]) + __uint_as_float(pg[3][2]));
      a0 += dpp<ROR8>(a0); a1 += dpp<ROR8>(a1);
      float o, t;
      t = x16_a(a0, o); a0 = t + o; t = x16_a(a1, o); a1 = t + o;
      t = x32_a(a0, o); a0 = t + o; t = x32_a(a1, o); a1 = t + o;
      if (lane < 8) *(unsigned*)(out + lane * DM + bid * 2) = pk_bf16(a0, a1);
      if (bid == 0 && lane == 0) __hip_atomic_store((GAS unsigned*)(ws + WS_EPOCH), tag, __ATOMIC_RELAXED, __HIP_MEMORY_SCOPE_AGENT);
    }
  } else if (wave == 7) {
    if (b < 7) {
      const int fgrp = s < 3 ? kvh * 3 + s : (kvh < 4 ? 24 + kvh : -1);
      const unsigned lb = (unsigned)(uintptr_t)(L + L_WO);
      const auto r2 = RSRC(w2, FF * DM * 2);
      const auto ro = RSRC(wo, NQ * DM * 2);
      const int off2 = (fgrp * 64 + (lane >> 3)) * 896 + b * 128 + (lane & 7) * 16;
      const int offo = (kvh * 512 + s * 64 + (lane >> 3)) * 896 + b * 128 + (lane & 7) * 16;
      const unsigned tag = get_tag(flag);
      const bool hi8 = lane & 8;
      u32x4 hb[3];
      lds_wait_ge(flag + F_SCNT, 4u);
      if (fgrp >= 0) {
        const int hoff = WS_HB + fgrp * 2048 + lane * 16, goff = WS_G + (kvh * 8 + (lane & 7)) * 128;
        gather2<3>(hb, tag, [&](int i) { return LD128(rws, i < 2 ? hoff + i * 1024 : goff, LDAUX); });
      }
      STAMP(1);
      lds_wait_ge(flag + F_VISS, 8u);
      STAMP(2);
      if (fgrp >= 0) {
#pragma unroll
        for (int i = 0; i < 8; ++i) dma16(r2, off2 + i * 8 * 896, lb + 16384 + i * 1024);
      }
#pragma unroll
      for (int i = 0; i < 16; ++i) dma16(ro, offo + ((i >> 3) * 256 + (i & 7) * 8) * 896, lb + i * 1024);
      STAMP(3);
      __builtin_amdgcn_s_setprio(3);
      LAS unsigned char* tb = L + L_WO + ((lane >> 4) * 8 + ((lane >> 2) & 3)) * 128 + (lane & 3) * 8;
      f32x4 acc[4] = {{0, 0, 0, 0}, {0, 0, 0, 0}, {0, 0, 0, 0}, {0, 0, 0, 0}};
      if (fgrp >= 0) {
        vmwait<16>();
#pragma unroll
        for (int ks = 0; ks < 2; ++ks) {
          const u32x4 bf = {hi8 ? 0u : hb[ks][0], hi8 ? 0u : hb[ks][2], hi8 ? hb[ks][0] : 0u, hi8 ? hb[ks][2] : 0u};
#pragma unroll
          for (int ct = 0; ct < 4; ++ct) {
            const u32x2 p0 = trd(tb + 16384 + ks * 4096 + ct * 32), p1 = trd(tb + 16384 + ks * 4096 + 512 + ct * 32);
            acc[ct] = mfma(u32x4{p0[0], p0[1], p1[0], p1[1]}, bf, acc[ct]);
          }
        }
        const float g1 = __uint_as_float(hb[2][0]);
#pragma unroll
        for (int ct = 0; ct < 4; ++ct) acc[ct] *= g1;
      }
      u32x4 wf[4][4];
#pragma unroll
      for (int ks = 0; ks < 4; ++ks) {
        if (ks == 0) vmwait<12>(); else if (ks == 1) vmwait<8>(); else if (ks == 2) vmwait<4>(); else vmwait<0>();
#pragma unroll
        for (int ct = 0; ct < 4; ++ct) {
          const u32x2 p0 = trd(tb + ks * 4096 + ct * 32), p1 = trd(tb + ks * 4096 + 512 + ct * 32);
          wf[ks][ct] = u32x4{p0[0], p0[1], p1[0], p1[1]};
        }
      }
      STAMP(4);
      const int ooff = WS_OB + grp * 4096 + (lane & 7) * 512 + (lane >> 3) * 16;
      u32x4 ob[4];
      gather2<4>(ob, tag, [&](int i) { return LD128(rws, ooff + i * 128, LDAUX); });
      STAMP(6);
#pragma unroll
      for (int ks = 0; ks < 4; ++ks) {
        const u32x4 bf = {hi8 ? 0u : ob[ks][0], hi8 ? 0u : ob[ks][2], hi8 ? ob[ks][0] : 0u, hi8 ? ob[ks][2] : 0u};
#pragma unroll
        for (int ct = 0; ct < 4; ++ct) acc[ct] = mfma(wf[ks][ct], bf, acc[ct]);
      }
      const int po = WS_PO + (b * 32 + (lane >> 3)) * PSTR + grp * 128 + (lane & 7) * 16;
#pragma unroll
      for (int ct = 0; ct < 4; ++ct) {
        const float k0 = hi8 ? acc[ct][2] : acc[ct][0], k1 = hi8 ? acc[ct][3] : acc[ct][1];
        const float s0 = hi8 ? acc[ct][0] : acc[ct][2], s1 = hi8 ? acc[ct][1] : acc[ct][3];
        const u32x4 pv = {__float_as_uint(k0 + dpp<ROR8>(s0)), tag, __float_as_uint(k1 + dpp<ROR8>(s1)), tag};
        ST128(pv, rws, po + ct * 8 * PSTR, STAUX);
      }
    }
  } else if (kvh < 7) {
    const int j = wave - 5, t = kvh * 8 + b;
    const unsigned lb = (unsigned)(uintptr_t)(L + L_T) + j * 14336;
    const auto r1 = RSRC(w1, DM * FF * 2);
    const int o = ((j * 224 + (lane >> 2)) * FF + t * 32 + (lane & 3) * 8) * 2;
#pragma unroll
    for (int i = 0; i < 14; ++i) {
      if (i >= WT) vmwait<WT - 1>();
      dma16(r1, o + i * 16 * FF * 2, lb + i * 1024);
    }
    u32x4 xa[14];
#pragma unroll
    for (int i = 0; i < 14; ++i) xa[i] = *(const u32x4*)(x + (lane & 7) * DM + i * 32 + (lane >> 4) * 8);
    SCHED_FENCE();
    const unsigned tag = get_tag(flag);
    vmwait<0>();
    lds_inc(L, F_TCNT, lane);
    lds_wait_ge(flag + F_TCNT, 2u);
    STAMP(6);
    LAS unsigned char* tb = L + L_T + ((lane >> 4) * 8 + ((lane >> 2) & 3)) * 64 + j * 32 + (lane & 3) * 8;
    f32x4 acc0 = {0, 0, 0, 0}, acc1 = {0, 0, 0, 0};
#pragma unroll
    for (int ks = 0; ks < 14; ++ks) {
      const u32x2 p0 = trd(tb + ks * 2048), p1 = trd(tb + ks * 2048 + 256);
      if (ks & 1) acc1 = mfma(u32x4{p0[0], p0[1], p1[0], p1[1]}, xa[ks], acc1);
      else acc0 = mfma(u32x4{p0[0], p0[1], p1[0], p1[1]}, xa[ks], acc0);
    }
    acc0 += acc1;
    float hv[4];
#pragma unroll
    for (int k = 0; k < 4; ++k) {
      const float v = rbf(acc0[k]);
      hv[k] = v / (1.f + __expf(-v));
    }
    if (!(lane & 8))
      ST128((u32x4{pk_bf16(hv[0], hv[1]), tag, pk_bf16(hv[2], hv[3]), tag}), rws, WS_HB + t * 1024 + j * 512 + (lane >> 4) * 128 + (lane & 7) * 16, STAUX);
  }
}

extern "C" void kernel_launch(void* const* d_in, const int* in_sizes, int n_in, void* d_out, int out_size, void* d_ws, size_t ws_size,
                              hipStream_t stream) {
  fused_kernel<<<256, 1024, 0, stream>>>((const u16*)d_in[0], (const u16*)d_in[1], (const u16*)d_in[2], (const u16*)d_in[3], (const u16*)d_in[4],
                                         (const u16*)d_in[6], (const u16*)d_in[7], (const u16*)d_in[8], (u16*)d_out, (unsigned char*)d_ws);
}
```

```cpp
#include <hip/hip_runtime.h>
#include <hip/hip_bf16.h>
#include <stdint.h>

#define STAMPS 0
#define CODEPF 1
#define CODE_BYTES 20480
#define VAUX 2
#define KAUX 2
#define WNT " nt"
#define NPA 1
#define NPB 1
#define PSTAG 9
#define QSLEEP1 10
#define QSLEEP2 10
#define FSLEEP 32
#define WK 4
#define WV 3
#define WT 3
#define SPOLL0 7
#define VSPLIT 10
#define VTRIG 15
#define KTRIG 22

typedef unsigned u32x4 __attribute__((ext_vector_type(4)));
typedef unsigned u32x2 __attribute__((ext_vector_type(2)));
typedef float f32x4 __attribute__((ext_vector_type(4)));
typedef float f32x2 __attribute__((ext_vector_type(2)));
typedef short s16x4 __attribute__((ext_vector_type(4)));
typedef short s16x8 __attribute__((ext_vector_type(8)));
typedef __bf16 bf16x2 __attribute__((ext_vector_type(2)));
typedef __bf16 bf16x8 __attribute__((ext_vector_type(8)));
typedef unsigned short u16;
#define LAS __attribute__((address_space(3)))
#define GAS __attribute__((address_space(1)))

constexpr int NB = 8, DM = 448, HKV = 8, SK = 4096, DH = 256, WIN = 1024, FF = 1792, NQ = 4096;
constexpr int PSTR = 4096 + 128;
constexpr int QPSTR = 4096 + 256, QSTR = 2048 + 128, HSTR = 256;
constexpr int WS_EPOCH = 0;
constexpr int WS_G = 4096;
constexpr int WS_QP = 16384;
constexpr int WS_Q = WS_QP + 256 * QPSTR;
constexpr int WS_HB = WS_Q + 64 * QSTR;
constexpr int WS_S = WS_HB + 65536;
constexpr int WS_OB = WS_S + 524288;
constexpr int WS_PO = WS_OB + 131072;
constexpr int WS_END = WS_PO + 224 * PSTR;
constexpr int WS_STAMP = 4 << 20;
constexpr int L_FLAG = 0, L_Q = 256, L_QO = L_Q + 1024, L_ST = L_QO + 512, L_FW = L_ST + 128, L_OM = L_FW + 64, L_SM = L_OM + 512, L_P = L_SM + 2304, L_OP = L_P + 4096, L_WO = L_OP + 4096, L_W2 = L_WO + 16384,
              L_X = L_W2 + 8192, L_WQ = L_X + 2048, L_T = L_WQ + 14336, L_HB = L_T + 28672, L_END = L_HB + 3072;
enum { F_TAGV = 0, F_TAG = 1, F_QRDY = 2, F_WQC = 3, F_QPC = 4, F_KISS = 5, F_OCNT = 6, F_TCNT = 7, F_VISS = 8, F_SCNT = 9, F_FRDY = 10, F_KCNT = 11, F_OMR = 12, F_WRDY = 13, F_CLAIM = 14, F_PDONE = 15 };
#define LDAUX 16
#define STAUX 16
#define WS_AT_END 0
constexpr int AUX_NT = 2, AUX_SC1 = 16;
constexpr float C2 = 0.0625f * 1.4426950408889634f;
constexpr int OOB = 0x7f000000;
constexpr unsigned SPIN_CAP = 1u << 18;

#define RSRC(p, n) __builtin_amdgcn_make_buffer_rsrc((void*)(p), 0, (n), 0x00020000)
#define LD128(r, off, aux) __builtin_amdgcn_raw_buffer_load_b128((r), (off), 0, (aux))
#define ST128(v, r, off, aux) __builtin_amdgcn_raw_buffer_store_b128((v), (r), (off), 0, (aux))
#define SCHED_FENCE() __builtin_amdgcn_sched_barrier(0)
#define MEM_FENCE() asm volatile("" ::: "memory")
template <int N> __device__ __forceinline__ void vmwait() { asm volatile("s_waitcnt vmcnt(%0)" ::"n"(N) : "memory"); }
#define LGKM0() asm volatile("s_waitcnt lgkmcnt(0)" ::: "memory")

__device__ __forceinline__ f32x4 mfma(u32x4 a, u32x4 b, f32x4 c) {
  return __builtin_amdgcn_mfma_f32_16x16x32_bf16(__builtin_bit_cast(bf16x8, a), __builtin_bit_cast(bf16x8, b), c, 0, 0, 0);
}
__device__ __forceinline__ u32x2 trd(LAS unsigned char* p) {
  return __builtin_bit_cast(u32x2, __builtin_amdgcn_ds_read_tr16_b64_v4i16((LAS s16x4*)p));
}
__device__ __forceinline__ unsigned pk_bf16(float lo, float hi) {
  f32x2 v = {lo, hi};
  return __builtin_bit_cast(unsigned, __builtin_convertvector(v, bf16x2));
}
__device__ __forceinline__ float bf_lo(unsigned u) { return __uint_as_float(u << 16); }
__device__ __forceinline__ float bf_hi(unsigned u) { return __uint_as_float(u & 0xffff0000u); }
__device__ __forceinline__ float rbf(float f) { return bf_lo(pk_bf16(f, f)); }
template <int CTRL> __device__ __forceinline__ float dpp(float x) {
  return __builtin_bit_cast(float, __builtin_amdgcn_mov_dpp(__builtin_bit_cast(int, x), CTRL, 0xf, 0xf, true));
}
constexpr int XOR1 = 0xB1, XOR2 = 0x4E, HMIRROR = 0x141, ROR8 = 0x128;
__device__ __forceinline__ float x16_a(float x, float& o) {
  auto s = __builtin_amdgcn_permlane16_swap(__float_as_uint(x), __float_as_uint(x), false, false);
  o = __uint_as_float(s[1]);
  return __uint_as_float(s[0]);
}
__device__ __forceinline__ float x32_a(float x, float& o) {
  auto s = __builtin_amdgcn_permlane32_swap(__float_as_uint(x), __float_as_uint(x), false, false);
  o = __uint_as_float(s[1]);
  return __uint_as_float(s[0]);
}
__device__ __forceinline__ float wave_max(float x) {
  x = fmaxf(x, dpp<XOR1>(x)); x = fmaxf(x, dpp<XOR2>(x)); x = fmaxf(x, dpp<HMIRROR>(x)); x = fmaxf(x, dpp<ROR8>(x));
  float o, a = x16_a(x, o); x = fmaxf(a, o);
  a = x32_a(x, o); return fmaxf(a, o);
}
__device__ __forceinline__ float wave_sum(float x) {
  x += dpp<XOR1>(x); x += dpp<XOR2>(x); x += dpp<HMIRROR>(x); x += dpp<ROR8>(x);
  float o, a = x16_a(x, o); x = a + o;
  a = x32_a(x, o); return a + o;
}
__device__ __forceinline__ void lds_wait_ge(volatile LAS unsigned* f, unsigned want) {
#pragma nounroll
  for (unsigned n = 0; n < (1u << 22); ++n) {
    if (__builtin_amdgcn_readfirstlane(*f) >= want) break;
    __builtin_amdgcn_s_sleep(1);
  }
  MEM_FENCE();
}
__device__ __forceinline__ unsigned bperm(unsigned v, int srclane) { return __builtin_amdgcn_ds_bpermute(srclane << 2, v); }
__device__ __forceinline__ void lds_inc(LAS unsigned char* L, int f, int lane) {
  if (lane == 0) asm volatile("ds_add_u32 %0, %1" ::"v"((unsigned)(uintptr_t)(L + f * 4)), "v"(1u) : "memory");
}
__device__ __forceinline__ unsigned lds_inc_ret(LAS unsigned char* L, int f, int lane) {
  unsigned old = 0;
  if (lane == 0) asm volatile("ds_add_rtn_u32 %0, %1, %2\n\ts_waitcnt lgkmcnt(0)" : "=&v"(old) : "v"((unsigned)(uintptr_t)(L + f * 4)), "v"(1u) : "memory");
  return __builtin_amdgcn_readfirstlane(old);
}
__device__ __forceinline__ unsigned get_tag(volatile LAS unsigned* flag) {
  lds_wait_ge(flag + 0, 1u);
  return __builtin_amdgcn_readfirstlane(flag[1]);
}
__device__ __forceinline__ void dma16(__amdgpu_buffer_rsrc_t r, int voff, unsigned ldsdst) {
  unsigned keep;
  asm volatile("s_mov_b32 %0, m0\n\ts_mov_b32 m0, %3\n\ts_nop 0\n\tbuffer_load_dwordx4 %1, %2, 0 offen" WNT " lds\n\ts_mov_b32 m0, %0"
               : "=&s"(keep) : "v"(voff), "s"(r), "s"(ldsdst) : "memory");
}
template <int N>
__device__ __forceinline__ void gatherm(u32x4 (&a)[N], int (&off)[N], unsigned tag, __amdgpu_buffer_rsrc_t r) {
#pragma nounroll
  for (unsigned n = 0; n < SPIN_CAP; ++n) {
    MEM_FENCE();
    u32x4 v[N];
#pragma unroll
    for (int i = 0; i < N; ++i) v[i] = LD128(r, off[i], LDAUX);
    bool all = true;
#pragma unroll
    for (int i = 0; i < N; ++i) {
      const bool mine = off[i] != OOB, ok = v[i][1] == tag && v[i][3] == tag;
      a[i][0] = mine ? v[i][0] : a[i][0];
      a[i][2] = mine ? v[i][2] : a[i][2];
      off[i] = ok ? OOB : off[i];
      all = all && (ok || !mine);
    }
    if (__all(all)) break;
  }
}
struct Sel { unsigned lo[4], hi[4]; };
__device__ __forceinline__ Sel make_sel(int lane) {
  Sel s;
  const int j = lane & 7;
#pragma unroll
  for (int t = 0; t < 4; ++t) {
    const bool on = (j >> 1) == t;
    s.lo[t] = on ? ((j & 1) ? 0x01000c0cu : 0x0c0c0100u) : 0x0c0c0c0cu;
    s.hi[t] = on ? ((j & 1) ? 0x03020c0cu : 0x0c0c0302u) : 0x0c0c0c0cu;
  }
  return s;
}
__device__ __forceinline__ u32x4 place(unsigned src, const unsigned (&sel)[4]) {
  u32x4 r;
#pragma unroll
  for (int t = 0; t < 4; ++t) r[t] = __builtin_amdgcn_perm(src, src, sel[t]);
  return r;
}

#define STAMP(i) do {} while (0)

__global__ void __launch_bounds__(1024) fused_kernel(const u16* __restrict__ x, const u16* __restrict__ kc, const u16* __restrict__ vc,
                                                     const u16* __restrict__ wq, const u16* __restrict__ wo, const u16* __restrict__ w1,
                                                     const u16* __restrict__ w2, const u16* __restrict__ gw, u16* __restrict__ out,
                                                     unsigned char* __restrict__ ws) {
  __shared__ __attribute__((aligned(16))) unsigned char smem[L_END];
  LAS unsigned char* L = (LAS unsigned char*)smem;
  volatile LAS unsigned* flag = (volatile LAS unsigned*)(L + L_FLAG);
  STAMP(0);
  const int bid = blockIdx.x, xc = bid & 7, jj = bid >> 3, kvh = jj & 7, s = jj >> 3, b = (xc - 2 * s) & 7;
  const int tid = threadIdx.x, lane = tid & 63, wave = __builtin_amdgcn_readfirstlane(tid >> 6);
  {
    unsigned long long pc0;
    asm volatile("s_getpc_b64 %0" : "=s"(pc0));
    const auto rc = RSRC(pc0 & ~255ull, CODE_BYTES);
    unsigned keep;
    asm volatile("ds_write_b32 %[fa], %[z]\n\t"
                 "s_cmp_lg_u32 %[w], 7\n\ts_cbranch_scc1 3f\n\t"
                 "s_mov_b32 %[k], m0\n\ts_mov_b32 m0, %[lc]\n\ts_nop 0\n\tbuffer_load_dwordx4 %[oc], %[rc], 0 offen lds\n\ts_mov_b32 m0, %[k]\n"
                 "3:"
                 : [k] "=&s"(keep)
                 : [fa] "v"((unsigned)(uintptr_t)L + (tid & 15) * 4), [z] "v"(0u), [w] "s"(wave), [oc] "v"((bid >> 3) * 1024 + lane * 16), [rc] "s"(rc),
                   [lc] "s"((unsigned)(uintptr_t)(L + L_WO))
                 : "memory", "scc");
  }
  const int cb = jj + 32 * (xc >> 2), rb = (xc + 1) & 3;
  const bool isx = wave == 8;
  int o0 = (lane >> (isx ? 4 : 3)) * (isx ? DM * 2 : NQ * 2) + (lane & (isx ? 15 : 7)) * 16 + (isx ? rb * 224 : ((rb * 112 + (wave - 9) * 16) * NQ + cb * 64) * 2);
  int o1 = o0 + (isx ? 4 * DM * 2 : 8 * NQ * 2);
  const int grp = kvh * 4 + s;
  const int ulo = s == 0 ? 0 : s == 1 ? 36 : s == 2 ? 58 : 94, uhi = s == 0 ? 36 : s == 1 ? 58 : s == 2 ? 94 : 128;
  const int nkw = s == 1 ? 4 : 6;
  int klo = (lane & 7) * 512 + ((lane >> 3) & 1) * 64 + (lane >> 4) * 16 + (ulo + (wave - 1) * 6) * 4096;
  unsigned kvoff = (unsigned)(((b * HKV + kvh) * SK + (SK - WIN)) * DH * 2);
  asm volatile("" : "+v"(o0), "+v"(o1), "+v"(klo), "+s"(kvoff));
  SCHED_FENCE();
  asm volatile("s_waitcnt lgkmcnt(0)" ::"s"(x), "s"(kc), "s"(vc), "s"(wq), "s"(wo), "s"(w1), "s"(w2), "s"(gw), "s"(out), "s"(ws) : "memory");
  unsigned epoch = 0;
  {
    const auto rq = RSRC(isx ? x : wq, isx ? NB * DM * 2 : DM * NQ * 2);
    const unsigned lb = (unsigned)(uintptr_t)(L + L_X) + (wave - 8) * 2048;
    unsigned keep;
    asm volatile(
        "s_cmp_lt_u32 %[w], 8\n\ts_cbranch_scc1 1f\n\t"
        "s_mov_b32 %[k], m0\n\ts_mov_b32 m0, %[lb]\n\ts_nop 0\n\tbuffer_load_dwordx4 %[o0], %[r], 0 offen" WNT " lds\n\t"
        "s_add_u32 m0, %[lb], 0x400\n\ts_nop 0\n\tbuffer_load_dwordx4 %[o1], %[r], 0 offen" WNT " lds\n\ts_mov_b32 m0, %[k]\n"
        "1:\n\ts_cmp_lg_u32 %[w], 0\n\ts_cbranch_scc1 2f\n\ts_load_dword %[e], %[ws], 0x0\n"
        "2:\n\ts_barrier\n\ts_waitcnt lgkmcnt(0)"
        : [e] "+s"(epoch), [k] "=&s"(keep)
        : [w] "s"(wave), [lb] "s"(lb), [o0] "v"(o0), [o1] "v"(o1), [r] "s"(rq), [ws] "s"(ws)
        : "memory", "scc");
  }
  if (wave == 0) {
    if (lane == 0) *(volatile LAS u32x2*)flag = u32x2{1u, epoch * 1664525u + 1013904223u};
  }
  const auto rws = RSRC(ws, WS_END);
  unsigned tag = 0;

  if (__builtin_expect(wave >= 1 && wave <= nkw, 1)) {
    const int kw = wave - 1;
    const int nu = (s & 1) && wave == nkw ? 4 : 6;
    const int u0 = ulo + kw * 6;
    const int brot = wave + jj + xc;
    const auto rk = RSRC((const unsigned char*)kc + kvoff, WIN * DH * 2);
    u32x4 r[24];
    const int urot = (kvh + kw) & 1;
    const int lo = klo;
    const int lo5 = nu == 6 ? lo : OOB;
    STAMP(4);
#pragma unroll
    for (int i = 0; i < 24; ++i) {
      if (i >= WK) vmwait<WK - 1>();
      if (i == WK) STAMP(5);
      r[i] = __builtin_amdgcn_raw_buffer_load_b128(rk, i < 16 ? lo : lo5, (((i >> 2) ^ urot) * 4096) + ((brot + i) & 3) * 128, KAUX);
      SCHED_FENCE();
      if (i == KTRIG) lds_inc(L, F_KISS, lane);
    }
    STAMP(1);
    __builtin_amdgcn_s_setprio(2);
    lds_wait_ge(flag + F_QRDY, 1u);
    tag = __builtin_amdgcn_readfirstlane(flag[F_TAG]);
    STAMP(2);
    u32x4 qa[4];
#pragma unroll
    for (int t = 0; t < 4; ++t) qa[t] = *(const LAS u32x4*)(L + L_Q + (lane & 1) * 512 + ((brot + t) & 3) * 128 + ((lane >> 1) & 1) * 64 + (lane >> 4) * 16);
    float s0 = 0.f, s1 = 0.f;
#pragma unroll
    for (int u = 0; u < 6; ++u) {
      f32x4 c = {0, 0, 0, 0};
#pragma unroll
      for (int t = 0; t < 4; ++t) c = mfma(qa[t], r[4 * u + t], c);
      float v0 = (lane & 8) ? c[2] : c[0], v1 = (lane & 8) ? c[3] : c[1];
      v0 += dpp<ROR8>(v0); v1 += dpp<ROR8>(v1);
      if (((lane >> 3) ^ urot) == u) { s0 = v0; s1 = v1; }
    }
    const unsigned pk = pk_bf16(s0, s1);
    const unsigned pa = bperm(pk, (2 * lane) & 63), pb = bperm(pk, (2 * lane + 1) & 63);
    if (lane < 4 * nu) {
      ST128((u32x4{pa, tag, pb, tag}), rws, WS_S + (b * 8 + kvh) * 8192 + u0 * 64 + lane * 16, STAUX);
      *(LAS u32x4*)(L + L_SM + kw * 384 + lane * 16) = u32x4{pa, tag, pb, tag};
    }
    MEM_FENCE();
    lds_inc(L, F_KCNT, lane);
    STAMP(3);
  } else if (wave >= 8) {
    const int w = wave - 8;
    vmwait<0>();
    lds_inc(L, F_WQC, lane);
    tag = get_tag(flag);
    if (w >= 4) {
      __builtin_amdgcn_s_setprio(3);
      const int ct = w - 4;
      const int xrow = ((lane & 7) >> 1) + 4 * (lane & 1);
      LAS unsigned char* xp = L + L_X + xrow * 256 + (lane >> 4) * 16;
      LAS unsigned char* xp2 = L + L_X + xrow * 256 + 192 + (lane >> 4) * 8;
      LAS unsigned char* tb = L + L_WQ + ((lane >> 4) * 8 + ((lane >> 2) & 3)) * 128 + (lane & 3) * 8 + ct * 32;
      LAS unsigned char* tb2 = L + L_WQ + (96 + (lane >> 4) * 4 + ((lane >> 2) & 3)) * 128 + (lane & 3) * 8 + ct * 32;
      const int dest = ((lane >> 4) & 1) * 2 + (lane >> 5);
      const int qo = WS_QP + (cb * 4 + dest) * QPSTR + rb * 1024 + ct * 256 + (lane & 15) * 16;
      lds_wait_ge(flag + F_WQC, 8u);
      STAMP(1);
      const u32x2 pt = trd(tb2), xb = *(const LAS u32x2*)xp2;
      f32x4 acc = mfma(u32x4{xb[0], xb[1], 0u, 0u}, u32x4{pt[0], pt[1], 0u, 0u}, f32x4{0, 0, 0, 0});
#pragma unroll
      for (int ks = 0; ks < 3; ++ks) {
        const u32x2 p0 = trd(tb + ks * 4096), p1 = trd(tb + ks * 4096 + 512);
        acc = mfma(*(const LAS u32x4*)(xp + ks * 64), u32x4{p0[0], p0[1], p1[0], p1[1]}, acc);
      }
      const float v0 = lane >= 32 ? acc[2] : acc[0], v1 = lane >= 32 ? acc[3] : acc[1];
      if (dest != rb) ST128((u32x4{__float_as_uint(v0), tag, __float_as_uint(v1), tag}), rws, qo, STAUX);
      else *(LAS f32x2*)(L + L_QO + (ct * 16 + (lane & 15)) * 8) = f32x2{v0, v1};
      LGKM0();
      lds_inc(L, F_QPC, lane);
      __builtin_amdgcn_s_setprio(0);
    }
    const auto rv = RSRC((const unsigned char*)vc + kvoff, WIN * DH * 2);
    u32x4 r[16];
    const int trot = w + jj + xc;
    const int voff = (w * 128 + (lane >> 4) * 8 + ((lane >> 3) & 1)) * 512 + s * 128 + (lane & 7) * 16;
    const Sel sel = make_sel(lane);
    const int myu = w * 16 + (lane >> 2);
    const bool own = myu >= ulo && myu < uhi;
    const int soff = own ? OOB : WS_S + (b * 8 + kvh) * 8192 + w * 1024 + lane * 16;
    LAS unsigned char* smp = L + L_SM + (own ? (w * 16 - ulo) * 64 + lane * 16 : 0);
    lds_wait_ge(flag + F_KISS, (unsigned)nkw);
    STAMP(2);
#define V_ISSUE(i) do { if ((i) > SPOLL0 && (i) < SPOLL0 + WV) vmwait<WV>(); else if ((i) >= WV) vmwait<WV - 1>(); \
      r[i] = __builtin_amdgcn_raw_buffer_load_b128(rv, voff + ((i) & 3) * 1024, ((((i) >> 2) + trot) & 3) * 16384, VAUX); SCHED_FENCE(); \
      if ((i) == VTRIG) lds_inc(L, F_VISS, lane); } while (0)
#define V_USE(i) do { a0 = mfma(place(pp[(i) >> 2][(i) & 3], sel.lo), r[i], a0); a1 = mfma(place(pp[(i) >> 2][(i) & 3], sel.hi), r[i], a1); } while (0)
#define S_OK() (kdone = flag[F_KCNT], sg = own ? *(const volatile LAS u32x4*)smp : sg, __all(own ? kdone >= (unsigned)nkw : sg[1] == tag && sg[3] == tag))
    unsigned kdone;
    u32x4 sg;
#pragma unroll
    for (int i = 0; i < VSPLIT; ++i) {
      V_ISSUE(i);
      if (i == SPOLL0) { MEM_FENCE(); sg = LD128(rws, soff, LDAUX); SCHED_FENCE(); }
    }
    STAMP(3);
    u32x4 pp[4];
    f32x4 a0 = {0, 0, 0, 0}, a1 = {0, 0, 0, 0};
#define P_MAKE() do { \
      const float m0 = wave_max(fmaxf(bf_lo(sg[0]), bf_lo(sg[2]))), m1 = wave_max(fmaxf(bf_hi(sg[0]), bf_hi(sg[2]))); \
      const float ea = __builtin_amdgcn_exp2f((bf_lo(sg[0]) - m0) * C2), eb = __builtin_amdgcn_exp2f((bf_lo(sg[2]) - m0) * C2); \
      const float ec = __builtin_amdgcn_exp2f((bf_hi(sg[0]) - m1) * C2), ed = __builtin_amdgcn_exp2f((bf_hi(sg[2]) - m1) * C2); \
      *(LAS unsigned*)(L + L_P + w * 512 + lane * 4) = pk_bf16(ea, eb); \
      *(LAS unsigned*)(L + L_P + w * 512 + 256 + lane * 4) = pk_bf16(ec, ed); \
      LGKM0(); \
      _Pragma("unroll") for (int t = 0; t < 4; ++t) \
        pp[t] = *(const LAS u32x4*)(L + L_P + w * 512 + ((lane >> 3) & 1) * 256 + (((t + trot) & 3) * 32 + (lane >> 4) * 8) * 2); \
      const float l0 = wave_sum(ea + eb), l1 = wave_sum(ec + ed); \
      if (lane == 0) *(LAS f32x4*)(L + L_ST + w * 16) = f32x4{m0, l0, m1, l1}; \
      LGKM0(); \
      lds_inc(L, F_SCNT, lane); } while (0)
    if (__builtin_expect(S_OK(), 1)) {
      STAMP(4);
      V_ISSUE(VSPLIT);
      P_MAKE();
      constexpr int NI = 15 - VSPLIT, PER = (VSPLIT + 1 - WV + NI - 1) / NI + 1;
#pragma unroll
      for (int i = VSPLIT + 1; i < 16; ++i) {
        V_ISSUE(i);
#pragma unroll
        for (int k = 0; k < 16; ++k)
          if (k >= (i - VSPLIT - 1) * PER && k < (i - VSPLIT) * PER && k <= i - WV) V_USE(k);
        SCHED_FENCE();
      }
#pragma unroll
      for (int k = 0; k < 16; ++k) {
        bool done = false;
#pragma unroll
        for (int i = VSPLIT + 1; i < 16; ++i) done = done || (k >= (i - VSPLIT - 1) * PER && k < (i - VSPLIT) * PER && k <= i - WV);
        if (!done) V_USE(k);
      }
    } else {
      MEM_FENCE();
      sg = LD128(rws, soff, LDAUX);
      SCHED_FENCE();
#pragma unroll
      for (int i = VSPLIT; i < 16; ++i) V_ISSUE(i);
      STAMP(6);
#pragma nounroll
      for (unsigned n = 0; n < SPIN_CAP; ++n) {
        if (S_OK()) break;
        MEM_FENCE();
        sg = LD128(rws, soff, LDAUX);
      }
      P_MAKE();
#pragma unroll
      for (int k = 0; k < 16; ++k) V_USE(k);
    }
    lds_wait_ge(flag + F_FRDY, 1u);
    const float f = *(const LAS float*)(L + L_FW + w * 8 + (lane >> 5) * 4);
    f32x4 t = (lane & 8) ? a1 : a0;
#pragma unroll
    for (int k = 0; k < 4; ++k) t[k] = (t[k] + dpp<ROR8>(t[k])) * f;
    if (!(lane & 8)) *(LAS f32x4*)(L + L_OP + w * 512 + ((lane >> 5) * 64 + (lane & 7) * 8 + ((lane >> 4) & 1) * 4) * 4) = t;
    STAMP(5);
    if (lds_inc_ret(L, F_OCNT, lane) == 7u) {
      f32x4 ov = {0.f, 0.f, 0.f, 0.f};
#pragma unroll
      for (int k = 0; k < 4; ++k) ov += *(const LAS f32x4*)(L + L_OP + ((lane >> 5) * 4 + k) * 512 + (lane & 31) * 16);
#pragma unroll
      for (int k = 0; k < 4; ++k) { float hi, lo = x32_a(ov[k], hi); ov[k] = lo + hi; }
      if (lane < 32) {
        const u32x4 ogr = {pk_bf16(ov[0], ov[1]), tag, pk_bf16(ov[2], ov[3]), tag};
        ST128(ogr, rws, WS_OB + (grp * 8 + b) * 512 + lane * 16, STAUX);
        *(LAS u32x4*)(L + L_OM + lane * 16) = ogr;
      }
      MEM_FENCE();
      lds_inc(L, F_OMR, lane);
    }
  } else if (wave == 0) {
    __builtin_amdgcn_s_setprio(3);
    tag = epoch * 1664525u + 1013904223u;
    lds_wait_ge(flag + F_QPC, 4u);
    STAMP(2);
    __builtin_amdgcn_s_sleep(QSLEEP1);
    {
      const int po = WS_QP + (cb * 4 + rb) * QPSTR + lane * 16;
      u32x4 pg[3];
      int of[3] = {po + ((rb + 1) & 3) * 1024, po + ((rb + 2) & 3) * 1024, po + ((rb + 3) & 3) * 1024};
      gatherm<3>(pg, of, tag, rws);
      const f32x2 own = *(const LAS f32x2*)(L + L_QO + lane * 8);
      const float a0 = (own[0] + __uint_as_float(pg[0][0])) + (__uint_as_float(pg[1][0]) + __uint_as_float(pg[2][0]));
      const float a1 = (own[1] + __uint_as_float(pg[0][2])) + (__uint_as_float(pg[1][2]) + __uint_as_float(pg[2][2]));
      const float n0 = dpp<XOR1>(a0), n1 = dpp<XOR1>(a1);
      const unsigned pk = (lane & 1) ? pk_bf16(n1, a1) : pk_bf16(a0, n0);
      const int src = (lane & 15) * 4 + ((lane >> 4) & 1);
      const unsigned pa = bperm(pk, src), pb = bperm(pk, src + 2);
      if (lane < 32) ST128((u32x4{pa, tag, pb, tag}), rws, WS_Q + ((cb >> 3) * 8 + rb + 4 * (lane >> 4)) * QSTR + (cb & 7) * 256 + (lane & 15) * 16, STAUX);
    }
    STAMP(6);
    __builtin_amdgcn_s_sleep(QSLEEP2);
    {
      const int qoff = WS_Q + (kvh * 8 + b) * QSTR + lane * 16;
      u32x4 qq[2];
      int of[2] = {qoff, qoff + 1024};
      gatherm<2>(qq, of, tag, rws);
      *(LAS u32x2*)(L + L_Q + lane * 8) = u32x2{qq[0][0], qq[0][2]};
      *(LAS u32x2*)(L + L_Q + 512 + lane * 8) = u32x2{qq[1][0], qq[1][2]};
      LGKM0();
      if (lane == 0) flag[F_QRDY] = 1u;
    }
    STAMP(3);
    u32x4 xg = {0, 0, 0, 0}, gg = {0, 0, 0, 0};
    if (lane < 56) {
      xg = *(const u32x4*)(x + b * DM + lane * 8);
      gg = *(const u32x4*)(gw + lane * 8);
    }
    float z = 0.f;
#pragma unroll
    for (int t = 0; t < 4; ++t) z += rbf(bf_lo(xg[t]) * bf_lo(gg[t])) + rbf(bf_hi(xg[t]) * bf_hi(gg[t]));
    z = rbf(wave_sum(z));
    const float g = rbf(1.f / (1.f + __expf(-z)));
    const unsigned g1u = __float_as_uint(rbf(1.f - g));
    if (s == 0 && lane < 8) ST128((u32x4{g1u, tag, g1u, tag}), rws, WS_G + (kvh * 8 + b) * 128 + lane * 16, STAUX);
    lds_wait_ge(flag + F_SCNT, 8u);
    {
      const f32x2 ml = *(const LAS f32x2*)(L + L_ST + (lane & 15) * 8);
      float M = fmaxf(ml[0], dpp<XOR2>(ml[0]));
      M = fmaxf(M, dpp<0x124>(M)); M = fmaxf(M, dpp<ROR8>(M));
      const float al = __builtin_amdgcn_exp2f((ml[0] - M) * C2);
      float Ls = al * ml[1];
      Ls += dpp<XOR2>(Ls); Ls += dpp<0x124>(Ls); Ls += dpp<ROR8>(Ls);
      if (lane < 16) *(LAS float*)(L + L_FW + lane * 4) = al * (g / Ls);
      LGKM0();
      if (lane == 0) flag[F_FRDY] = 1u;
    }
    STAMP(4);
  } else if (wave != 7 && xc < 7) {
    const int j = wave - 5, t = xc * 8 + kvh;
    const unsigned lb = (unsigned)(uintptr_t)(L + L_T) + j * 14336;
    const auto r1 = RSRC(w1, DM * FF * 2);
    const int o = ((j * 224 + (lane >> 2)) * FF + t * 32 + (lane & 3) * 8) * 2;
#pragma unroll
    for (int i = 0; i < 14; ++i) {
      if (i >= WT) vmwait<WT - 1>();
      dma16(r1, o + i * 16 * FF * 2, lb + i * 1024);
    }
    u32x4 xa[14];
#pragma unroll
    for (int i = 0; i < 14; ++i) xa[i] = *(const u32x4*)(x + (lane & 7) * DM + i * 32 + (lane >> 4) * 8);
    SCHED_FENCE();
    tag = get_tag(flag);
    vmwait<0>();
    lds_inc(L, F_TCNT, lane);
    lds_wait_ge(flag + F_TCNT, 2u);
    STAMP(6);
    LAS unsigned char* tb = L + L_T + ((lane >> 4) * 8 + ((lane >> 2) & 3)) * 64 + j * 32 + (lane & 3) * 8;
    f32x4 acc0 = {0, 0, 0, 0}, acc1 = {0, 0, 0, 0};
#pragma unroll
    for (int ks = 0; ks < 14; ++ks) {
      const u32x2 p0 = trd(tb + ks * 2048), p1 = trd(tb + ks * 2048 + 256);
      if (ks & 1) acc1 = mfma(u32x4{p0[0], p0[1], p1[0], p1[1]}, xa[ks], acc1);
      else acc0 = mfma(u32x4{p0[0], p0[1], p1[0], p1[1]}, xa[ks], acc0);
    }
    acc0 += acc1;
    float hv[4];
#pragma unroll
    for (int k = 0; k < 4; ++k) {
      const float v = rbf(acc0[k]);
      hv[k] = v / (1.f + __expf(-v));
    }
    if (!(lane & 8))
      ST128((u32x4{pk_bf16(hv[0], hv[1]), tag, pk_bf16(hv[2], hv[3]), tag}), rws, WS_HB + t * 1024 + j * 512 + (lane >> 4) * 128 + (lane & 7) * 16, STAUX);
  }
  const int cpair = (xc ^ 1) * 32 + jj;
  if (wave > 7 - NPA && wave <= 7 && b < 7) {
    const int fgrp = s < 3 ? kvh * 3 + s : (kvh < 4 ? 24 + kvh : -1);
    tag = get_tag(flag);
    const bool hi8 = lane & 8;
    if (wave == 7) {
      const unsigned lb = (unsigned)(uintptr_t)(L + L_WO);
      const auto r2 = RSRC(w2, FF * DM * 2);
      const auto ro = RSRC(wo, NQ * DM * 2);
      const int off2 = (fgrp * 64 + (lane >> 3)) * 896 + b * 128 + (lane & 7) * 16;
      const int offo = (kvh * 512 + s * 64 + (lane >> 3)) * 896 + b * 128 + (lane & 7) * 16;
      lds_wait_ge(flag + F_SCNT, 4u);
      if (fgrp >= 0) {
        u32x4 hb[3];
        int of[3] = {WS_HB + fgrp * 2048 + lane * 16, WS_HB + fgrp * 2048 + 1024 + lane * 16, WS_G + (kvh * 8 + (lane & 7)) * 128};
        gatherm<3>(hb, of, tag, rws);
        *(LAS u32x4*)(L + L_HB + lane * 16) = u32x4{hb[0][0], hb[0][2], hb[1][0], hb[1][2]};
        *(LAS unsigned*)(L + L_HB + 1024 + lane * 4) = hb[2][0];
      }
      STAMP(1);
      lds_wait_ge(flag + F_VISS, 8u);
      STAMP(2);
      if (fgrp >= 0) {
#pragma unroll
        for (int i = 0; i < 8; ++i) dma16(r2, off2 + i * 8 * 896, lb + 16384 + i * 1024);
      }
#pragma unroll
      for (int i = 0; i < 16; ++i) dma16(ro, offo + ((i >> 3) * 256 + (i & 7) * 8) * 896, lb + i * 1024);
      STAMP(3);
      vmwait<0>();
      LGKM0();
      if (lane == 0) flag[F_WRDY] = 1u;
    } else {
      lds_wait_ge(flag + F_WRDY, 1u);
    }
    __builtin_amdgcn_s_setprio(3);
    const bool ownb = (lane & 7) == b;
    const int ooff = WS_OB + grp * 4096 + (lane & 7) * 512 + (lane >> 3) * 16;
    int of[4] = {ownb ? OOB : ooff, ownb ? OOB : ooff + 128, ownb ? OOB : ooff + 256, ownb ? OOB : ooff + 384};
    u32x4 ob[4];
    if (wave != 7) __builtin_amdgcn_s_sleep(PSTAG);
    if (wave == 5) __builtin_amdgcn_s_sleep(PSTAG);
#pragma unroll
    for (int i = 0; i < 4; ++i) ob[i] = LD128(rws, of[i], LDAUX);
    SCHED_FENCE();
    LAS unsigned char* tb = L + L_WO + ((lane >> 4) * 8 + ((lane >> 2) & 3)) * 128 + (lane & 3) * 8;
    f32x4 acc[4] = {{0, 0, 0, 0}, {0, 0, 0, 0}, {0, 0, 0, 0}, {0, 0, 0, 0}};
    if (fgrp >= 0) {
      const u32x4 hv = *(const LAS u32x4*)(L + L_HB + lane * 16);
      const float g1 = *(const LAS float*)(L + L_HB + 1024 + lane * 4);
#pragma unroll
      for (int ks = 0; ks < 2; ++ks) {
        const u32x4 bf = {hi8 ? 0u : hv[2 * ks], hi8 ? 0u : hv[2 * ks + 1], hi8 ? hv[2 * ks] : 0u, hi8 ? hv[2 * ks + 1] : 0u};
#pragma unroll
        for (int ct = 0; ct < 4; ++ct) {
          const u32x2 p0 = trd(tb + 16384 + ks * 4096 + ct * 32), p1 = trd(tb + 16384 + ks * 4096 + 512 + ct * 32);
          acc[ct] = mfma(u32x4{p0[0], p0[1], p1[0], p1[1]}, bf, acc[ct]);
        }
      }
#pragma unroll
      for (int ct = 0; ct < 4; ++ct) acc[ct] *= g1;
    }
    u32x4 wf[4][4];
#pragma unroll
    for (int ks = 0; ks < 4; ++ks)
#pragma unroll
      for (int ct = 0; ct < 4; ++ct) {
        const u32x2 p0 = trd(tb + ks * 4096 + ct * 32), p1 = trd(tb + ks * 4096 + 512 + ct * 32);
        wf[ks][ct] = u32x4{p0[0], p0[1], p1[0], p1[1]};
      }
    const int po = WS_PO + (b * 32 + (lane >> 3)) * PSTR + grp * 128 + (lane & 7) * 16;
    if (wave == 7) STAMP(4);
    lds_wait_ge(flag + F_OMR, 1u);
    bool all = true;
#pragma unroll
    for (int i = 0; i < 4; ++i) {
      const u32x4 v = *(const LAS u32x4*)(L + L_OM + i * 128 + (lane >> 3) * 16);
      const bool ok = ownb || (ob[i][1] == tag && ob[i][3] == tag);
      ob[i][0] = ownb ? v[0] : ob[i][0];
      ob[i][2] = ownb ? v[2] : ob[i][2];
      of[i] = ok ? OOB : of[i];
      all = all && ok;
    }
    if (!__all(all)) gatherm<4>(ob, of, tag, rws);
    if (wave == 7) STAMP(6);
    unsigned first = 0;
    if (NPA > 1 && lane == 0) asm volatile("ds_add_rtn_u32 %0, %1, %2" : "=&v"(first) : "v"((unsigned)(uintptr_t)(L + F_CLAIM * 4)), "v"(1u) : "memory");
#pragma unroll
    for (int ks = 0; ks < 4; ++ks) {
      const u32x4 bf = {hi8 ? 0u : ob[ks][0], hi8 ? 0u : ob[ks][2], hi8 ? ob[ks][0] : 0u, hi8 ? ob[ks][2] : 0u};
#pragma unroll
      for (int ct = 0; ct < 4; ++ct) acc[ct] = mfma(wf[ks][ct], bf, acc[ct]);
    }
    asm volatile("s_waitcnt lgkmcnt(0)" : "+v"(first)::"memory");
    if (__builtin_amdgcn_readfirstlane(first) == 0u) {
#pragma unroll
      for (int ct = 0; ct < 4; ++ct) {
        const float k0 = hi8 ? acc[ct][2] : acc[ct][0], k1 = hi8 ? acc[ct][3] : acc[ct][1];
        const float s0 = hi8 ? acc[ct][0] : acc[ct][2], s1 = hi8 ? acc[ct][1] : acc[ct][3];
        const u32x4 pv = {__float_as_uint(k0 + dpp<ROR8>(s0)), tag, __float_as_uint(k1 + dpp<ROR8>(s1)), tag};
        ST128(pv, rws, po + ct * 8 * PSTR, STAUX);
      }
    }
  } else if (wave < NPB && cpair < 224) {
    __builtin_amdgcn_s_setprio(3);
    lds_wait_ge(flag + F_OCNT, 8u);
    if (wave == 0) STAMP(5);
    __builtin_amdgcn_s_sleep(FSLEEP);
    if (wave >= 1) __builtin_amdgcn_s_sleep(PSTAG);
    if (wave >= 2) __builtin_amdgcn_s_sleep(PSTAG);
    const int poff = WS_PO + cpair * PSTR + lane * 16;
    u32x4 pg[4];
    int of[4] = {poff, poff + 1024, poff + 2048, poff + 3072};
#pragma nounroll
    for (unsigned n = 0; n < SPIN_CAP; ++n) {
      MEM_FENCE();
      u32x4 v[4];
#pragma unroll
      for (int i = 0; i < 4; ++i) v[i] = LD128(rws, of[i], LDAUX);
      const unsigned done = NPB > 1 ? flag[F_PDONE] : 0u;
      bool all = true;
#pragma unroll
      for (int i = 0; i < 4; ++i) {
        const bool mine = of[i] != OOB, ok = v[i][1] == tag && v[i][3] == tag;
        pg[i][0] = mine ? v[i][0] : pg[i][0];
        pg[i][2] = mine ? v[i][2] : pg[i][2];
        of[i] = ok ? OOB : of[i];
        all = all && (ok || !mine);
      }
      if (__all(all)) {
        float a0 = (__uint_as_float(pg[0][0]) + __uint_as_float(pg[1][0])) + (__uint_as_float(pg[2][0]) + __uint_as_float(pg[3][0]));
        float a1 = (__uint_as_float(pg[0][2]) + __uint_as_float(pg[1][2])) + (__uint_as_float(pg[2][2]) + __uint_as_float(pg[3][2]));
        a0 += dpp<ROR8>(a0); a1 += dpp<ROR8>(a1);
        float o, t;
        t = x16_a(a0, o); a0 = t + o; t = x16_a(a1, o); a1 = t + o;
        t = x32_a(a0, o); a0 = t + o; t = x32_a(a1, o); a1 = t + o;
        if (lane < 8) *(unsigned*)(out + lane * DM + cpair * 2) = pk_bf16(a0, a1);
        if (bid == 0 && lane == 0) __hip_atomic_store((GAS unsigned*)(ws + WS_EPOCH), tag, __ATOMIC_RELAXED, __HIP_MEMORY_SCOPE_AGENT);
        if (NPB > 1 && lane == 0) flag[F_PDONE] = 1u;
        break;
      }
      if (__builtin_amdgcn_readfirstlane(done) != 0u) break;
    }
  }
}

extern "C" void kernel_launch(void* const* d_in, const int* in_sizes, int n_in, void* d_out, int out_size, void* d_ws, size_t ws_size,
                              hipStream_t stream) {
  fused_kernel<<<256, 1024, 0, stream>>>((const u16*)d_in[0], (const u16*)d_in[1], (const u16*)d_in[2], (const u16*)d_in[3], (const u16*)d_in[4],
                                         (const u16*)d_in[6], (const u16*)d_in[7], (const u16*)d_in[8], (u16*)d_out, (unsigned char*)d_ws);
}
```

```cpp
#include <hip/hip_runtime.h>
#include <hip/hip_bf16.h>
#include <stdint.h>

#define STAMPS 0
#define CODEPF 1
#define CODE_BYTES 21248
#define VAUX 2
#define KAUX 2
#define WNT " nt"
#define WK2 WK
#define WV2 WV
#define WVE WV
#define QSLEEP1 10
#define QSLEEP2 10
#define FSLEEP 32
#define WK 4
#define WV 3
#define WT 3
#define SPOLL0 7
#define VSPLIT 10
#define VTRIG 15
#define KTRIG 22

typedef unsigned u32x4 __attribute__((ext_vector_type(4)));
typedef unsigned u32x2 __attribute__((ext_vector_type(2)));
typedef float f32x4 __attribute__((ext_vector_type(4)));
typedef float f32x2 __attribute__((ext_vector_type(2)));
typedef short s16x4 __attribute__((ext_vector_type(4)));
typedef short s16x8 __attribute__((ext_vector_type(8)));
typedef __bf16 bf16x2 __attribute__((ext_vector_type(2)));
typedef __bf16 bf16x8 __attribute__((ext_vector_type(8)));
typedef unsigned short u16;
typedef unsigned u32x16 __attribute__((ext_vector_type(16)));
typedef unsigned long long u64;
#define LAS __attribute__((address_space(3)))
#define GAS __attribute__((address_space(1)))

constexpr int NB = 8, DM = 448, HKV = 8, SK = 4096, DH = 256, WIN = 1024, FF = 1792, NQ = 4096;
constexpr int PSTR = 4096 + 128;
constexpr int QPSTR = 4096 + 256, QSTR = 2048 + 128, HSTR = 256;
constexpr int WS_EPOCH = 0;
constexpr int WS_G = 4096;
constexpr int WS_QP = 16384;
constexpr int WS_Q = WS_QP + 256 * QPSTR;
constexpr int WS_HB = WS_Q + 64 * QSTR;
constexpr int WS_S = WS_HB + 65536;
constexpr int WS_OB = WS_S + 524288;
constexpr int WS_PO = WS_OB + 131072;
constexpr int WS_END = WS_PO + 224 * PSTR;
constexpr int WS_STAMP = 8 << 20;
constexpr int L_FLAG = 0, L_Q = 256, L_QO = L_Q + 1024, L_ST = L_QO + 512, L_FW = L_ST + 128, L_OM = L_FW + 64, L_SM = L_OM + 512, L_P = L_SM + 2304, L_OP = L_P + 4096, L_WO = L_OP + 4096, L_W2 = L_WO + 16384,
              L_X = L_W2 + 8192, L_WQ = L_X + 2048, L_T = L_WQ + 14336, L_XA = L_T + 28672, L_END = L_XA + 8192;
enum { F_TAGV = 0, F_TAG = 1, F_QRDY = 2, F_WQC = 3, F_QPC = 4, F_KISS = 5, F_OCNT = 6, F_TCNT = 7, F_VISS = 8, F_SCNT = 9, F_FRDY = 10, F_KCNT = 11, F_OMR = 12 };
#define LDAUX 16
#define STAUX 16
#define WS_AT_END 0
constexpr int AUX_NT = 2, AUX_SC1 = 16;
constexpr float C2 = 0.0625f * 1.4426950408889634f;
constexpr int OOB = 0x7f000000;
constexpr unsigned SPIN_CAP = 1u << 18;

#define RSRC(p, n) __builtin_amdgcn_make_buffer_rsrc((void*)(p), 0, (n), 0x00020000)
#define LD128(r, off, aux) __builtin_amdgcn_raw_buffer_load_b128((r), (off), 0, (aux))
#define ST128(v, r, off, aux) __builtin_amdgcn_raw_buffer_store_b128((v), (r), (off), 0, (aux))
#define SCHED_FENCE() __builtin_amdgcn_sched_barrier(0)
#define MEM_FENCE() asm volatile("" ::: "memory")
template <int N> __device__ __forceinline__ void vmwait() { asm volatile("s_waitcnt vmcnt(%0)" ::"n"(N) : "memory"); }
#define LGKM0() asm volatile("s_waitcnt lgkmcnt(0)" ::: "memory")

__device__ __forceinline__ f32x4 mfma(u32x4 a, u32x4 b, f32x4 c) {
  return __builtin_amdgcn_mfma_f32_16x16x32_bf16(__builtin_bit_cast(bf16x8, a), __builtin_bit_cast(bf16x8, b), c, 0, 0, 0);
}
__device__ __forceinline__ u32x2 trd(LAS unsigned char* p) {
  return __builtin_bit_cast(u32x2, __builtin_amdgcn_ds_read_tr16_b64_v4i16((LAS s16x4*)p));
}
__device__ __forceinline__ unsigned pk_bf16(float lo, float hi) {
  f32x2 v = {lo, hi};
  return __builtin_bit_cast(unsigned, __builtin_convertvector(v, bf16x2));
}
__device__ __forceinline__ float bf_lo(unsigned u) { return __uint_as_float(u << 16); }
__device__ __forceinline__ float bf_hi(unsigned u) { return __uint_as_float(u & 0xffff0000u); }
__device__ __forceinline__ float rbf(float f) { return bf_lo(pk_bf16(f, f)); }
template <int CTRL> __device__ __forceinline__ float dpp(float x) {
  return __builtin_bit_cast(float, __builtin_amdgcn_mov_dpp(__builtin_bit_cast(int, x), CTRL, 0xf, 0xf, true));
}
constexpr int XOR1 = 0xB1, XOR2 = 0x4E, HMIRROR = 0x141, ROR8 = 0x128;
__device__ __forceinline__ float x16_a(float x, float& o) {
  auto s = __builtin_amdgcn_permlane16_swap(__float_as_uint(x), __float_as_uint(x), false, false);
  o = __uint_as_float(s[1]);
  return __uint_as_float(s[0]);
}
__device__ __forceinline__ float x32_a(float x, float& o) {
  auto s = __builtin_amdgcn_permlane32_swap(__float_as_uint(x), __float_as_uint(x), false, false);
  o = __uint_as_float(s[1]);
  return __uint_as_float(s[0]);
}
__device__ __forceinline__ float wave_max(float x) {
  x = fmaxf(x, dpp<XOR1>(x)); x = fmaxf(x, dpp<XOR2>(x)); x = fmaxf(x, dpp<HMIRROR>(x)); x = fmaxf(x, dpp<ROR8>(x));
  float o, a = x16_a(x, o); x = fmaxf(a, o);
  a = x32_a(x, o); return fmaxf(a, o);
}
__device__ __forceinline__ float wave_sum(float x) {
  x += dpp<XOR1>(x); x += dpp<XOR2>(x); x += dpp<HMIRROR>(x); x += dpp<ROR8>(x);
  float o, a = x16_a(x, o); x = a + o;
  a = x32_a(x, o); return a + o;
}
__device__ __forceinline__ void lds_wait_ge(volatile LAS unsigned* f, unsigned want) {
#pragma nounroll
  for (unsigned n = 0; n < (1u << 22); ++n) {
    if (__builtin_amdgcn_readfirstlane(*f) >= want) break;
    __builtin_amdgcn_s_sleep(1);
  }
  MEM_FENCE();
}
__device__ __forceinline__ unsigned bperm(unsigned v, int srclane) { return __builtin_amdgcn_ds_bpermute(srclane << 2, v); }
__device__ __forceinline__ void lds_inc(LAS unsigned char* L, int f, int lane) {
  if (lane == 0) asm volatile("ds_add_u32 %0, %1" ::"v"((unsigned)(uintptr_t)(L + f * 4)), "v"(1u) : "memory");
}
__device__ __forceinline__ unsigned lds_inc_ret(LAS unsigned char* L, int f, int lane) {
  unsigned old = 0;
  if (lane == 0) asm volatile("ds_add_rtn_u32 %0, %1, %2\n\ts_waitcnt lgkmcnt(0)" : "=&v"(old) : "v"((unsigned)(uintptr_t)(L + f * 4)), "v"(1u) : "memory");
  return __builtin_amdgcn_readfirstlane(old);
}
__device__ __forceinline__ unsigned get_tag(volatile LAS unsigned* flag) {
  lds_wait_ge(flag + 0, 1u);
  return __builtin_amdgcn_readfirstlane(flag[1]);
}
__device__ __forceinline__ void dma16(__amdgpu_buffer_rsrc_t r, int voff, unsigned ldsdst) {
  unsigned keep;
  asm volatile("s_mov_b32 %0, m0\n\ts_mov_b32 m0, %3\n\ts_nop 0\n\tbuffer_load_dwordx4 %1, %2, 0 offen" WNT " lds\n\ts_mov_b32 m0, %0"
               : "=&s"(keep) : "v"(voff), "s"(r), "s"(ldsdst) : "memory");
}
template <int N>
__device__ __forceinline__ void gatherm(u32x4 (&a)[N], int (&off)[N], unsigned tag, __amdgpu_buffer_rsrc_t r) {
#pragma nounroll
  for (unsigned n = 0; n < SPIN_CAP; ++n) {
    MEM_FENCE();
    u32x4 v[N];
#pragma unroll
    for (int i = 0; i < N; ++i) v[i] = LD128(r, off[i], LDAUX);
    bool all = true;
#pragma unroll
    for (int i = 0; i < N; ++i) {
      const bool mine = off[i] != OOB, ok = v[i][1] == tag && v[i][3] == tag;
      a[i][0] = mine ? v[i][0] : a[i][0];
      a[i][2] = mine ? v[i][2] : a[i][2];
      off[i] = ok ? OOB : off[i];
      all = all && (ok || !mine);
    }
    if (__all(all)) break;
  }
}
template <int STEP>
__device__ __forceinline__ void pass4(u32x4 (&a)[4], const u64 (&m)[4], int off, __amdgpu_buffer_rsrc_t r) {
  asm volatile(
      "s_mov_b64 exec, %[m0]\n\tbuffer_load_dwordx4 %[a0], %[o], %[r], 0 offen sc1\n\t"
      "s_mov_b64 exec, %[m1]\n\tbuffer_load_dwordx4 %[a1], %[o], %[r], 0 offen offset:%c[s1] sc1\n\t"
      "s_mov_b64 exec, %[m2]\n\tbuffer_load_dwordx4 %[a2], %[o], %[r], 0 offen offset:%c[s2] sc1\n\t"
      "s_mov_b64 exec, %[m3]\n\tbuffer_load_dwordx4 %[a3], %[o], %[r], 0 offen offset:%c[s3] sc1\n\t"
      "s_mov_b64 exec, -1\n\ts_waitcnt vmcnt(0)"
      : [a0] "+v"(a[0]), [a1] "+v"(a[1]), [a2] "+v"(a[2]), [a3] "+v"(a[3])
      : [m0] "s"(m[0]), [m1] "s"(m[1]), [m2] "s"(m[2]), [m3] "s"(m[3]), [o] "v"(off), [r] "s"(r), [s1] "i"(STEP), [s2] "i"(2 * STEP), [s3] "i"(3 * STEP)
      : "memory");
}
__device__ __forceinline__ bool missing4(const u32x4 (&a)[4], u64 (&m)[4], unsigned tag) {
#pragma unroll
  for (int i = 0; i < 4; ++i) m[i] &= __builtin_amdgcn_ballot_w64(a[i][1] != tag) | __builtin_amdgcn_ballot_w64(a[i][3] != tag);
  return (m[0] | m[1] | m[2] | m[3]) != 0ull;
}
struct Sel { unsigned lo[4], hi[4]; };
__device__ __forceinline__ Sel make_sel(int lane) {
  Sel s;
  const int j = lane & 7;
#pragma unroll
  for (int t = 0; t < 4; ++t) {
    const bool on = (j >> 1) == t;
    s.lo[t] = on ? ((j & 1) ? 0x01000c0cu : 0x0c0c0100u) : 0x0c0c0c0cu;
    s.hi[t] = on ? ((j & 1) ? 0x03020c0cu : 0x0c0c0302u) : 0x0c0c0c0cu;
  }
  return s;
}
__device__ __forceinline__ u32x4 place(unsigned src, const unsigned (&sel)[4]) {
  u32x4 r;
#pragma unroll
  for (int t = 0; t < 4; ++t) r[t] = __builtin_amdgcn_perm(src, src, sel[t]);
  return r;
}

#define STAMP(i) do {} while (0)

__global__ void __launch_bounds__(1024) fused_kernel(const void*, const void*, const void*, const void*, const void*, const void*, const void*,
                                                     const void*, void*, void*) {
  u32x16 ka;
  u32x4 kb;
  asm volatile("s_load_dwordx16 %0, %2, 0x0\n\ts_load_dwordx4 %1, %2, 0x40" : "=&s"(ka), "=&s"(kb) : "s"(__builtin_amdgcn_kernarg_segment_ptr()) : "memory");
  __shared__ __attribute__((aligned(16))) unsigned char smem[L_END];
  LAS unsigned char* L = (LAS unsigned char*)smem;
  volatile LAS unsigned* flag = (volatile LAS unsigned*)(L + L_FLAG);
  STAMP(0);
  const int bid = blockIdx.x, xc = bid & 7, jj = bid >> 3, kvh = jj & 7, s = jj >> 3, b = (xc - 2 * s) & 7;
  const int tid = threadIdx.x, lane = tid & 63, wave = __builtin_amdgcn_readfirstlane(tid >> 6);
  {
    unsigned long long pc0;
    asm volatile("s_getpc_b64 %0" : "=s"(pc0));
    const auto rc = RSRC(pc0 & ~255ull, CODE_BYTES);
    unsigned keep;
    asm volatile("ds_write_b32 %[fa], %[z]\n\t"
                 "s_cmp_lg_u32 %[w], 7\n\ts_cbranch_scc1 3f\n\t"
                 "s_mov_b32 %[k], m0\n\ts_mov_b32 m0, %[lc]\n\ts_nop 0\n\tbuffer_load_dwordx4 %[oc], %[rc], 0 offen lds\n\ts_mov_b32 m0, %[k]\n"
                 "3:"
                 : [k] "=&s"(keep)
                 : [fa] "v"((unsigned)(uintptr_t)L + (tid & 15) * 4), [z] "v"(0u), [w] "s"(wave), [oc] "v"((bid >> 3) * 1024 + lane * 16), [rc] "s"(rc),
                   [lc] "s"((unsigned)(uintptr_t)(L + L_WO))
                 : "memory", "scc");
  }
  const int cb = jj + 32 * (xc >> 2), rb = (xc + 1) & 3;
  const bool isx = wave == 8;
  int o0 = (lane >> (isx ? 4 : 3)) * (isx ? DM * 2 : NQ * 2) + (lane & (isx ? 15 : 7)) * 16 + (isx ? rb * 224 : ((rb * 112 + (wave - 9) * 16) * NQ + cb * 64) * 2);
  int o1 = o0 + (isx ? 4 * DM * 2 : 8 * NQ * 2);
  const int grp = kvh * 4 + s;
  const int ulo = s == 0 ? 0 : s == 1 ? 36 : s == 2 ? 58 : 94, uhi = s == 0 ? 36 : s == 1 ? 58 : s == 2 ? 94 : 128;
  const int nkw = s == 1 ? 4 : 6;
  int klo = (lane & 7) * 512 + ((lane >> 3) & 1) * 64 + (lane >> 4) * 16 + (ulo + (wave - 1) * 6) * 4096;
  unsigned kvoff = (unsigned)(((b * HKV + kvh) * SK + (SK - WIN)) * DH * 2);
  unsigned lbq = (unsigned)(uintptr_t)(L + L_X) + (wave - 8) * 2048, nrq = isx ? NB * DM * 2 : DM * NQ * 2;
  asm volatile("s_waitcnt lgkmcnt(0)" : "+s"(ka), "+s"(kb), "+v"(o0), "+v"(o1), "+v"(klo), "+s"(kvoff), "+s"(lbq), "+s"(nrq)::"memory");
#define ARG(v, i) ((u64)v[2 * (i)] | ((u64)v[2 * (i) + 1] << 32))
  const GAS u16* x = (const GAS u16*)ARG(ka, 0);
  const u64 kc = ARG(ka, 1), vc = ARG(ka, 2), wo = ARG(ka, 4), w1 = ARG(ka, 5), w2 = ARG(ka, 6), ws = ARG(kb, 1);
  const GAS u16* gw = (const GAS u16*)ARG(ka, 7);
  GAS u16* out = (GAS u16*)ARG(kb, 0);
  unsigned epochv = 0;
  {
    const auto rq = RSRC((u64)(isx ? ka[0] : ka[6]) | ((u64)(isx ? ka[1] : ka[7]) << 32), nrq);
    const unsigned lb = lbq;
    unsigned keep;
    asm volatile(
        "s_cmp_lt_u32 %[w], 8\n\ts_cbranch_scc1 1f\n\t"
        "s_mov_b32 %[k], m0\n\ts_mov_b32 m0, %[lb]\n\ts_nop 0\n\tbuffer_load_dwordx4 %[o0], %[r], 0 offen" WNT " lds\n\t"
        "s_add_u32 m0, %[lb], 0x400\n\ts_nop 0\n\tbuffer_load_dwordx4 %[o1], %[r], 0 offen" WNT " lds\n\ts_mov_b32 m0, %[k]\n"
        "1:\n\ts_cmp_lg_u32 %[w], 0\n\ts_cbranch_scc1 2f\n\tglobal_load_dword %[e], %[e], %[ws] sc1\n\ts_barrier\n\ts_waitcnt vmcnt(0)\n\ts_branch 4f\n"
        "2:\n\ts_barrier\n"
        "4:"
        : [e] "+v"(epochv), [k] "=&s"(keep)
        : [w] "s"(wave), [lb] "s"(lb), [o0] "v"(o0), [o1] "v"(o1), [r] "s"(rq), [ws] "s"(ws)
        : "memory", "scc");
  }
  const unsigned epoch = __builtin_amdgcn_readfirstlane(epochv);
  if (wave == 0) {
    if (lane == 0) *(volatile LAS u32x2*)flag = u32x2{1u, epoch * 1664525u + 1013904223u};
  }
  const auto rws = RSRC(ws, WS_END);
  unsigned tag = 0;

  if (__builtin_expect(wave >= 1 && wave <= nkw, 1)) {
    const int kw = wave - 1;
    const int nu = (s & 1) && wave == nkw ? 4 : 6;
    const int u0 = ulo + kw * 6;
    const int brot = wave + jj + xc;
    const auto rk = RSRC(kc + kvoff, WIN * DH * 2);
    u32x4 r[24];
    const int urot = (kvh + kw) & 1;
    const int lo = klo;
    const int lo5 = nu == 6 ? lo : OOB;
    STAMP(4);
#pragma unroll
    for (int i = 0; i < 24; ++i) {
      if (i >= WK) { if (i & 1) vmwait<WK2 - 1>(); else vmwait<WK - 1>(); }
      if (i == WK) STAMP(5);
      r[i] = __builtin_amdgcn_raw_buffer_load_b128(rk, i < 16 ? lo : lo5, (((i >> 2) ^ urot) * 4096) + ((brot + i) & 3) * 128, KAUX);
      SCHED_FENCE();
      if (i == KTRIG) lds_inc(L, F_KISS, lane);
    }
    STAMP(1);
    __builtin_amdgcn_s_setprio(2);
    lds_wait_ge(flag + F_QRDY, 1u);
    tag = __builtin_amdgcn_readfirstlane(flag[F_TAG]);
    STAMP(2);
    u32x4 qa[4];
#pragma unroll
    for (int t = 0; t < 4; ++t) qa[t] = *(const LAS u32x4*)(L + L_Q + (lane & 1) * 512 + ((brot + t) & 3) * 128 + ((lane >> 1) & 1) * 64 + (lane >> 4) * 16);
    float s0 = 0.f, s1 = 0.f;
#pragma unroll
    for (int u = 0; u < 6; ++u) {
      f32x4 c = {0, 0, 0, 0};
#pragma unroll
      for (int t = 0; t < 4; ++t) c = mfma(qa[t], r[4 * u + t], c);
      float v0 = (lane & 8) ? c[2] : c[0], v1 = (lane & 8) ? c[3] : c[1];
      v0 += dpp<ROR8>(v0); v1 += dpp<ROR8>(v1);
      if (((lane >> 3) ^ urot) == u) { s0 = v0; s1 = v1; }
    }
    const unsigned pk = pk_bf16(s0, s1);
    const unsigned pa = bperm(pk, (2 * lane) & 63), pb = bperm(pk, (2 * lane + 1) & 63);
    if (lane < 4 * nu) {
      ST128((u32x4{pa, tag, pb, tag}), rws, WS_S + (b * 8 + kvh) * 8192 + u0 * 64 + lane * 16, STAUX);
      *(LAS u32x4*)(L + L_SM + kw * 384 + lane * 16) = u32x4{pa, tag, pb, tag};
    }
    MEM_FENCE();
    lds_inc(L, F_KCNT, lane);
    STAMP(3);
  } else if (wave >= 8) {
    const int w = wave - 8;
    vmwait<0>();
    lds_inc(L, F_WQC, lane);
    tag = get_tag(flag);
    if (w >= 4) {
      __builtin_amdgcn_s_setprio(3);
      const int ct = w - 4;
      const int xrow = ((lane & 7) >> 1) + 4 * (lane & 1);
      LAS unsigned char* xp = L + L_X + xrow * 256 + (lane >> 4) * 16;
      LAS unsigned char* xp2 = L + L_X + xrow * 256 + 192 + (lane >> 4) * 8;
      LAS unsigned char* tb = L + L_WQ + ((lane >> 4) * 8 + ((lane >> 2) & 3)) * 128 + (lane & 3) * 8 + ct * 32;
      LAS unsigned char* tb2 = L + L_WQ + (96 + (lane >> 4) * 4 + ((lane >> 2) & 3)) * 128 + (lane & 3) * 8 + ct * 32;
      const int dest = ((lane >> 4) & 1) * 2 + (lane >> 5);
      const int qo = WS_QP + (cb * 4 + dest) * QPSTR + rb * 1024 + ct * 256 + (lane & 15) * 16;
      lds_wait_ge(flag + F_WQC, 8u);
      STAMP(1);
      const u32x2 pt = trd(tb2), xb = *(const LAS u32x2*)xp2;
      f32x4 acc = mfma(u32x4{xb[0], xb[1], 0u, 0u}, u32x4{pt[0], pt[1], 0u, 0u}, f32x4{0, 0, 0, 0});
#pragma unroll
      for (int ks = 0; ks < 3; ++ks) {
        const u32x2 p0 = trd(tb + ks * 4096), p1 = trd(tb + ks * 4096 + 512);
        acc = mfma(*(const LAS u32x4*)(xp + ks * 64), u32x4{p0[0], p0[1], p1[0], p1[1]}, acc);
      }
      const float v0 = lane >= 32 ? acc[2] : acc[0], v1 = lane >= 32 ? acc[3] : acc[1];
      if (dest != rb) ST128((u32x4{__float_as_uint(v0), tag, __float_as_uint(v1), tag}), rws, qo, STAUX);
      else *(LAS f32x2*)(L + L_QO + (ct * 16 + (lane & 15)) * 8) = f32x2{v0, v1};
      LGKM0();
      lds_inc(L, F_QPC, lane);
      __builtin_amdgcn_s_setprio(0);
    }
    const auto rv = RSRC(vc + kvoff, WIN * DH * 2);
    u32x4 r[16];
    const int trot = w + jj + xc;
    const int voff = (w * 128 + (lane >> 4) * 8 + ((lane >> 3) & 1)) * 512 + s * 128 + (lane & 7) * 16;
    const Sel sel = make_sel(lane);
    const int myu = w * 16 + (lane >> 2);
    const bool own = myu >= ulo && myu < uhi;
    const int soff = own ? OOB : WS_S + (b * 8 + kvh) * 8192 + w * 1024 + lane * 16;
    LAS unsigned char* smp = L + L_SM + (own ? (w * 16 - ulo) * 64 + lane * 16 : 0);
    lds_wait_ge(flag + F_KISS, (unsigned)nkw);
    STAMP(2);
#define V_ISSUE(i) do { if ((i) > SPOLL0 && (i) < SPOLL0 + WV) vmwait<WV>(); else if ((i) > VSPLIT) vmwait<WVE - 1>(); else if ((i) >= WV) { if ((i) & 1) vmwait<WV2 - 1>(); else vmwait<WV - 1>(); } \
      r[i] = __builtin_amdgcn_raw_buffer_load_b128(rv, voff + ((i) & 3) * 1024, ((((i) >> 2) + trot) & 3) * 16384, VAUX); SCHED_FENCE(); \
      if ((i) == VTRIG) lds_inc(L, F_VISS, lane); } while (0)
#define V_USE(i) do { a0 = mfma(place(pp[(i) >> 2][(i) & 3], sel.lo), r[i], a0); a1 = mfma(place(pp[(i) >> 2][(i) & 3], sel.hi), r[i], a1); } while (0)
#define S_OK() (kdone = flag[F_KCNT], sg = own ? *(const volatile LAS u32x4*)smp : sg, __all(own ? kdone >= (unsigned)nkw : sg[1] == tag && sg[3] == tag))
    unsigned kdone;
    u32x4 sg;
#pragma unroll
    for (int i = 0; i < VSPLIT; ++i) {
      V_ISSUE(i);
      if (i == SPOLL0) { MEM_FENCE(); sg = LD128(rws, soff, LDAUX); SCHED_FENCE(); }
    }
    STAMP(3);
    u32x4 pp[4];
    f32x4 a0 = {0, 0, 0, 0}, a1 = {0, 0, 0, 0};
#define P_MAKE() do { \
      const float m0 = wave_max(fmaxf(bf_lo(sg[0]), bf_lo(sg[2]))), m1 = wave_max(fmaxf(bf_hi(sg[0]), bf_hi(sg[2]))); \
      const float n0 = m0 == -INFINITY ? 0.f : m0, n1 = m1 == -INFINITY ? 0.f : m1;     \
      const float ea = __builtin_amdgcn_exp2f((bf_lo(sg[0]) - n0) * C2), eb = __builtin_amdgcn_exp2f((bf_lo(sg[2]) - n0) * C2); \
      const float ec = __builtin_amdgcn_exp2f((bf_hi(sg[0]) - n1) * C2), ed = __builtin_amdgcn_exp2f((bf_hi(sg[2]) - n1) * C2); \
      *(LAS unsigned*)(L + L_P + w * 512 + lane * 4) = pk_bf16(ea, eb); \
      *(LAS unsigned*)(L + L_P + w * 512 + 256 + lane * 4) = pk_bf16(ec, ed); \
      LGKM0(); \
      _Pragma("unroll") for (int t = 0; t < 4; ++t) \
        pp[t] = *(const LAS u32x4*)(L + L_P + w * 512 + ((lane >> 3) & 1) * 256 + (((t + trot) & 3) * 32 + (lane >> 4) * 8) * 2); \
      const float l0 = wave_sum(ea + eb), l1 = wave_sum(ec + ed); \
      if (lane == 0) *(LAS f32x4*)(L + L_ST + w * 16) = f32x4{m0, l0, m1, l1}; \
      LGKM0(); \
      lds_inc(L, F_SCNT, lane); } while (0)
    if (__builtin_expect(S_OK(), 1)) {
      STAMP(4);
      V_ISSUE(VSPLIT);
      P_MAKE();
      constexpr int NI = 15 - VSPLIT, PER = (VSPLIT + 1 - WVE + NI - 1) / NI + 1;
#pragma unroll
      for (int i = VSPLIT + 1; i < 16; ++i) {
        V_ISSUE(i);
#pragma unroll
        for (int k = 0; k < 16; ++k)
          if (k >= (i - VSPLIT - 1) * PER && k < (i - VSPLIT) * PER && k <= i - WVE) V_USE(k);
        SCHED_FENCE();
      }
#pragma unroll
      for (int k = 0; k < 16; ++k) {
        bool done = false;
#pragma unroll
        for (int i = VSPLIT + 1; i < 16; ++i) done = done || (k >= (i - VSPLIT - 1) * PER && k < (i - VSPLIT) * PER && k <= i - WVE);
        if (!done) V_USE(k);
      }
    } else {
      MEM_FENCE();
      sg = LD128(rws, soff, LDAUX);
      SCHED_FENCE();
#pragma unroll
      for (int i = VSPLIT; i < 16; ++i) V_ISSUE(i);
      STAMP(6);
#pragma nounroll
      for (unsigned n = 0; n < SPIN_CAP; ++n) {
        if (S_OK()) break;
        MEM_FENCE();
        sg = LD128(rws, soff, LDAUX);
      }
      P_MAKE();
#pragma unroll
      for (int k = 0; k < 16; ++k) V_USE(k);
    }
    lds_wait_ge(flag + F_FRDY, 1u);
    const float f = *(const LAS float*)(L + L_FW + w * 8 + (lane >> 5) * 4);
    f32x4 t = (lane & 8) ? a1 : a0;
#pragma unroll
    for (int k = 0; k < 4; ++k) t[k] = (t[k] + dpp<ROR8>(t[k])) * f;
    if (!(lane & 8)) *(LAS f32x4*)(L + L_OP + w * 512 + ((lane >> 5) * 64 + (lane & 7) * 8 + ((lane >> 4) & 1) * 4) * 4) = t;
    STAMP(5);
    if (lds_inc_ret(L, F_OCNT, lane) == 7u) {
      f32x4 ov = {0.f, 0.f, 0.f, 0.f};
#pragma unroll
      for (int k = 0; k < 4; ++k) ov += *(const LAS f32x4*)(L + L_OP + ((lane >> 5) * 4 + k) * 512 + (lane & 31) * 16);
#pragma unroll
      for (int k = 0; k < 4; ++k) { float hi, lo = x32_a(ov[k], hi); ov[k] = lo + hi; }
      if (lane < 32) {
        const u32x4 ogr = {pk_bf16(ov[0], ov[1]), tag, pk_bf16(ov[2], ov[3]), tag};
        ST128(ogr, rws, WS_OB + (grp * 8 + b) * 512 + lane * 16, STAUX);
        *(LAS u32x4*)(L + L_OM + lane * 16) = ogr;
      }
      MEM_FENCE();
      lds_inc(L, F_OMR, lane);
    }
  } else if (wave == 0) {
    __builtin_amdgcn_s_setprio(3);
    tag = epoch * 1664525u + 1013904223u;
    lds_wait_ge(flag + F_QPC, 4u);
    STAMP(2);
    __builtin_amdgcn_s_sleep(QSLEEP1);
    {
      const int po = WS_QP + (cb * 4 + rb) * QPSTR + lane * 16;
      u32x4 pg[3];
      int of[3] = {po + ((rb + 1) & 3) * 1024, po + ((rb + 2) & 3) * 1024, po + ((rb + 3) & 3) * 1024};
      gatherm<3>(pg, of, tag, rws);
      const f32x2 own = *(const LAS f32x2*)(L + L_QO + lane * 8);
      const float a0 = (own[0] + __uint_as_float(pg[0][0])) + (__uint_as_float(pg[1][0]) + __uint_as_float(pg[2][0]));
      const float a1 = (own[1] + __uint_as_float(pg[0][2])) + (__uint_as_float(pg[1][2]) + __uint_as_float(pg[2][2]));
      const float n0 = dpp<XOR1>(a0), n1 = dpp<XOR1>(a1);
      const unsigned pk = (lane & 1) ? pk_bf16(n1, a1) : pk_bf16(a0, n0);
      const int src = (lane & 15) * 4 + ((lane >> 4) & 1);
      const unsigned pa = bperm(pk, src), pb = bperm(pk, src + 2);
      if (lane < 32) ST128((u32x4{pa, tag, pb, tag}), rws, WS_Q + ((cb >> 3) * 8 + rb + 4 * (lane >> 4)) * QSTR + (cb & 7) * 256 + (lane & 15) * 16, STAUX);
    }
    STAMP(6);
    __builtin_amdgcn_s_sleep(QSLEEP2);
    {
      const int qoff = WS_Q + (kvh * 8 + b) * QSTR + lane * 16;
      u32x4 qq[2];
      int of[2] = {qoff, qoff + 1024};
      gatherm<2>(qq, of, tag, rws);
      *(LAS u32x2*)(L + L_Q + lane * 8) = u32x2{qq[0][0], qq[0][2]};
      *(LAS u32x2*)(L + L_Q + 512 + lane * 8) = u32x2{qq[1][0], qq[1][2]};
      LGKM0();
      if (lane == 0) flag[F_QRDY] = 1u;
    }
    STAMP(3);
    u32x4 xg = {0, 0, 0, 0}, gg = {0, 0, 0, 0};
    if (lane < 56) {
      xg = *(const GAS u32x4*)(x + b * DM + lane * 8);
      gg = *(const GAS u32x4*)(gw + lane * 8);
    }
    float z = 0.f;
#pragma unroll
    for (int t = 0; t < 4; ++t) z += rbf(bf_lo(xg[t]) * bf_lo(gg[t])) + rbf(bf_hi(xg[t]) * bf_hi(gg[t]));
    z = rbf(wave_sum(z));
    const float g = rbf(1.f / (1.f + __expf(-z)));
    const unsigned g1u = __float_as_uint(rbf(1.f - g));
    if (s == 0 && lane < 8) ST128((u32x4{g1u, tag, g1u, tag}), rws, WS_G + (kvh * 8 + b) * 128 + lane * 16, STAUX);
    lds_wait_ge(flag + F_SCNT, 8u);
    {
      const f32x2 ml = *(const LAS f32x2*)(L + L_ST + (lane & 15) * 8);
      float M = fmaxf(ml[0], dpp<XOR2>(ml[0]));
      M = fmaxf(M, dpp<0x124>(M)); M = fmaxf(M, dpp<ROR8>(M));
      const float al = __builtin_amdgcn_exp2f((ml[0] - M) * C2);
      float Ls = al * ml[1];
      Ls += dpp<XOR2>(Ls); Ls += dpp<0x124>(Ls); Ls += dpp<ROR8>(Ls);
      if (lane < 16) *(LAS float*)(L + L_FW + lane * 4) = al * (g / Ls);
      LGKM0();
      if (lane == 0) flag[F_FRDY] = 1u;
    }
    STAMP(4);
  } else if (wave != 7 && xc < 7) {
    const int j = wave - 5, t = xc * 8 + kvh;
    const unsigned lb = (unsigned)(uintptr_t)(L + L_T) + j * 14336;
    const auto r1 = RSRC(w1, DM * FF * 2);
    const int o = ((j * 224 + (lane >> 2)) * FF + t * 32 + (lane & 3) * 8) * 2;
#pragma unroll
    for (int i = 0; i < 14; ++i) {
      if (i >= WT) vmwait<WT - 1>();
      dma16(r1, o + i * 16 * FF * 2, lb + i * 1024);
    }
    {
      const auto rx = RSRC((u64)x, NB * DM * 2);
#pragma unroll
      for (int i = 0; i < 4; ++i) {
        vmwait<WT - 1>();
        dma16(rx, (j * 4 + i) * 1024 + lane * 16, (unsigned)(uintptr_t)(L + L_XA) + (j * 4 + i) * 1024);
      }
    }
    tag = get_tag(flag);
    vmwait<0>();
    lds_inc(L, F_TCNT, lane);
    lds_wait_ge(flag + F_TCNT, 2u);
    STAMP(6);
    LAS unsigned char* tb = L + L_T + ((lane >> 4) * 8 + ((lane >> 2) & 3)) * 64 + j * 32 + (lane & 3) * 8;
    f32x4 acc0 = {0, 0, 0, 0}, acc1 = {0, 0, 0, 0};
#pragma unroll
    for (int ks = 0; ks < 14; ++ks) {
      const u32x2 p0 = trd(tb + ks * 2048), p1 = trd(tb + ks * 2048 + 256);
      const u32x4 xa = *(const LAS u32x4*)(L + L_XA + (lane & 7) * (DM * 2) + ks * 64 + (lane >> 4) * 16);
      if (ks & 1) acc1 = mfma(u32x4{p0[0], p0[1], p1[0], p1[1]}, xa, acc1);
      else acc0 = mfma(u32x4{p0[0], p0[1], p1[0], p1[1]}, xa, acc0);
    }
    acc0 += acc1;
    float hv[4];
#pragma unroll
    for (int k = 0; k < 4; ++k) {
      const float v = rbf(acc0[k]);
      hv[k] = v / (1.f + __expf(-v));
    }
    if (!(lane & 8))
      ST128((u32x4{pk_bf16(hv[0], hv[1]), tag, pk_bf16(hv[2], hv[3]), tag}), rws, WS_HB + t * 1024 + j * 512 + (lane >> 4) * 128 + (lane & 7) * 16, STAUX);
  }
  const int cpair = (xc ^ 1) * 32 + jj;
  if (wave == 7 && b < 7) {
    const int fgrp = s < 3 ? kvh * 3 + s : (kvh < 4 ? 24 + kvh : -1);
    tag = get_tag(flag);
    const bool hi8 = lane & 8;
    u32x4 hb[3];
    {
      const unsigned lb = (unsigned)(uintptr_t)(L + L_WO);
      const auto r2 = RSRC(w2, FF * DM * 2);
      const auto ro = RSRC(wo, NQ * DM * 2);
      const int off2 = (fgrp * 64 + (lane >> 3)) * 896 + b * 128 + (lane & 7) * 16;
      const int offo = (kvh * 512 + s * 64 + (lane >> 3)) * 896 + b * 128 + (lane & 7) * 16;
      lds_wait_ge(flag + F_VISS, 8u);
      STAMP(2);
      if (fgrp >= 0) {
#pragma unroll
        for (int i = 0; i < 8; ++i) dma16(r2, off2 + i * 8 * 896, lb + 16384 + i * 1024);
      }
#pragma unroll
      for (int i = 0; i < 16; ++i) dma16(ro, offo + ((i >> 3) * 256 + (i & 7) * 8) * 896, lb + i * 1024);
      STAMP(3);
      if (fgrp >= 0) {
        int of[3] = {WS_HB + fgrp * 2048 + lane * 16, WS_HB + fgrp * 2048 + 1024 + lane * 16, WS_G + (kvh * 8 + (lane & 7)) * 128};
        gatherm<3>(hb, of, tag, rws);
      }
      vmwait<0>();
    }
    __builtin_amdgcn_s_setprio(3);
    const bool ownb = (lane & 7) == b;
    const int ooff = WS_OB + grp * 4096 + (lane & 7) * 512 + (lane >> 3) * 16;
    u32x4 ob[4];
#pragma unroll
    for (int i = 0; i < 4; ++i) ob[i] = LD128(rws, ownb ? OOB : ooff + i * 128, LDAUX);
    SCHED_FENCE();
    LAS unsigned char* tb = L + L_WO + ((lane >> 4) * 8 + ((lane >> 2) & 3)) * 128 + (lane & 3) * 8;
    f32x4 acc[4] = {{0, 0, 0, 0}, {0, 0, 0, 0}, {0, 0, 0, 0}, {0, 0, 0, 0}};
    if (fgrp >= 0) {
      const u32x4 hv = {hb[0][0], hb[0][2], hb[1][0], hb[1][2]};
      const float g1 = __uint_as_float(hb[2][0]);
#pragma unroll
      for (int ks = 0; ks < 2; ++ks) {
        const u32x4 bf = {hi8 ? 0u : hv[2 * ks], hi8 ? 0u : hv[2 * ks + 1], hi8 ? hv[2 * ks] : 0u, hi8 ? hv[2 * ks + 1] : 0u};
#pragma unroll
        for (int ct = 0; ct < 4; ++ct) {
          const u32x2 p0 = trd(tb + 16384 + ks * 4096 + ct * 32), p1 = trd(tb + 16384 + ks * 4096 + 512 + ct * 32);
          acc[ct] = mfma(u32x4{p0[0], p0[1], p1[0], p1[1]}, bf, acc[ct]);
        }
      }
#pragma unroll
      for (int ct = 0; ct < 4; ++ct) acc[ct] *= g1;
    }
    u32x4 wf[4][4];
#pragma unroll
    for (int ks = 0; ks < 4; ++ks)
#pragma unroll
      for (int ct = 0; ct < 4; ++ct) {
        const u32x2 p0 = trd(tb + ks * 4096 + ct * 32), p1 = trd(tb + ks * 4096 + 512 + ct * 32);
        wf[ks][ct] = u32x4{p0[0], p0[1], p1[0], p1[1]};
      }
    const int po = WS_PO + (b * 32 + (lane >> 3)) * PSTR + grp * 128 + (lane & 7) * 16;
    STAMP(4);
    lds_wait_ge(flag + F_OMR, 1u);
#pragma unroll
    for (int i = 0; i < 4; ++i) {
      const u32x4 v = *(const LAS u32x4*)(L + L_OM + i * 128 + (lane >> 3) * 16);
      ob[i] = ownb ? v : ob[i];
    }
    u64 ms[4] = {~0ull, ~0ull, ~0ull, ~0ull};
    if (missing4(ob, ms, tag)) {
#pragma nounroll
      for (unsigned n = 0; n < SPIN_CAP; ++n) {
        pass4<128>(ob, ms, ooff, rws);
        if (!missing4(ob, ms, tag)) break;
      }
    }
    STAMP(6);
    u32x4 bf[4];
#pragma unroll
    for (int ks = 0; ks < 4; ++ks) bf[ks] = u32x4{hi8 ? 0u : ob[ks][0], hi8 ? 0u : ob[ks][2], hi8 ? ob[ks][0] : 0u, hi8 ? ob[ks][2] : 0u};
#pragma unroll
    for (int ct = 0; ct < 4; ++ct)
#pragma unroll
      for (int ks = 0; ks < 4; ++ks) acc[ct] = mfma(wf[ks][ct], bf[ks], acc[ct]);
    {
#pragma unroll
      for (int ct = 0; ct < 4; ++ct) {
        const float k0 = hi8 ? acc[ct][2] : acc[ct][0], k1 = hi8 ? acc[ct][3] : acc[ct][1];
        const float s0 = hi8 ? acc[ct][0] : acc[ct][2], s1 = hi8 ? acc[ct][1] : acc[ct][3];
        const u32x4 pv = {__float_as_uint(k0 + dpp<ROR8>(s0)), tag, __float_as_uint(k1 + dpp<ROR8>(s1)), tag};
        ST128(pv, rws, po + ct * 8 * PSTR, STAUX);
      }
      STAMP(5);
    }
  } else if (wave == 0 && cpair < 224) {
    __builtin_amdgcn_s_setprio(3);
    lds_wait_ge(flag + F_OCNT, 8u);
    STAMP(5);
    __builtin_amdgcn_s_sleep(FSLEEP);
    const int poff = WS_PO + cpair * PSTR + lane * 16;
    u32x4 pg[4] = {{0, 0, 0, 0}, {0, 0, 0, 0}, {0, 0, 0, 0}, {0, 0, 0, 0}};
    u64 ms[4] = {~0ull, ~0ull, ~0ull, ~0ull};
    GAS unsigned* op = (GAS unsigned*)(out + (lane & 7) * DM + cpair * 2);
#pragma nounroll
    for (unsigned n = 0; n < SPIN_CAP; ++n) {
      pass4<1024>(pg, ms, poff, rws);
      if (!missing4(pg, ms, tag)) {
        float a0 = (__uint_as_float(pg[0][0]) + __uint_as_float(pg[1][0])) + (__uint_as_float(pg[2][0]) + __uint_as_float(pg[3][0]));
        float a1 = (__uint_as_float(pg[0][2]) + __uint_as_float(pg[1][2])) + (__uint_as_float(pg[2][2]) + __uint_as_float(pg[3][2]));
        a0 += dpp<ROR8>(a0); a1 += dpp<ROR8>(a1);
        float o, t;
        t = x16_a(a0, o); a0 = t + o; t = x16_a(a1, o); a1 = t + o;
        t = x32_a(a0, o); a0 = t + o; t = x32_a(a1, o); a1 = t + o;
        if (lane < 8) *op = pk_bf16(a0, a1);
        if (bid == 0 && lane == 0) __hip_atomic_store((GAS unsigned*)(ws + WS_EPOCH), tag, __ATOMIC_RELAXED, __HIP_MEMORY_SCOPE_AGENT);
        STAMP(1);
        break;
      }
    }
  }
}

extern "C" void kernel_launch(void* const* d_in, const int* in_sizes, int n_in, void* d_out, int out_size, void* d_ws, size_t ws_size,
                              hipStream_t stream) {
  fused_kernel<<<256, 1024, 0, stream>>>(d_in[0], d_in[1], d_in[2], d_in[3], d_in[4], d_in[6], d_in[7], d_in[8], d_out, d_ws);
}
```

```cpp
#include <hip/hip_runtime.h>
#include <hip/hip_bf16.h>
#include <stdint.h>

constexpr int CODE_BYTES = 17792;
constexpr int WK = 4, WV = 3, WT = 3;
constexpr int KTRIG = 22;
constexpr int VSPLIT = 10, VPER = 3;
#define QSLEEP 10
#define SSLEEP 24
#define FSLEEP 32

typedef unsigned u32x4 __attribute__((ext_vector_type(4)));
typedef unsigned u32x2 __attribute__((ext_vector_type(2)));
typedef float f32x4 __attribute__((ext_vector_type(4)));
typedef float f32x2 __attribute__((ext_vector_type(2)));
typedef short s16x4 __attribute__((ext_vector_type(4)));
typedef __bf16 bf16x2 __attribute__((ext_vector_type(2)));
typedef __bf16 bf16x8 __attribute__((ext_vector_type(8)));
typedef unsigned short u16;
typedef unsigned u32x16 __attribute__((ext_vector_type(16)));
typedef unsigned long long u64;
#define LAS __attribute__((address_space(3)))
#define GAS __attribute__((address_space(1)))

constexpr int NB = 8, DM = 448, HKV = 8, SK = 4096, DH = 256, WIN = 1024, FF = 1792, NQ = 4096;
constexpr int PSTR = 4096 + 128;
constexpr int QPSTR = 4096 + 256, QSTR = 2048 + 128;
constexpr int WS_EPOCH = 0;
constexpr int WS_G = 4096;
constexpr int WS_QP = 16384;
constexpr int WS_Q = WS_QP + 256 * QPSTR;
constexpr int WS_HB = WS_Q + 64 * QSTR;
constexpr int WS_S = WS_HB + 65536;
constexpr int WS_OB = WS_S + 524288;
constexpr int WS_PO = WS_OB + 131072;
constexpr int WS_END = WS_PO + 224 * PSTR;
constexpr int L_FLAG = 0, L_Q = 256, L_QO = L_Q + 1024, L_ST = L_QO + 512, L_FW = L_ST + 128, L_OM = L_FW + 64, L_SM = L_OM + 512, L_P = L_SM + 2304, L_OP = L_P + 4096, L_WO = L_OP + 4096, L_W2 = L_WO + 16384,
              L_X = L_W2 + 8192, L_WQ = L_X + 2048, L_T = L_WQ + 14336, L_XA = L_T + 28672, L_END = L_XA + 8192;
enum { F_TAGV = 0, F_TAG = 1, F_QRDY = 2, F_WQC = 3, F_QPC = 4, F_KISS = 5, F_OCNT = 6, F_TCNT = 7, F_VISS = 8, F_SCNT = 9, F_FRDY = 10, F_KCNT = 11, F_OMR = 12, F_PR = 16 };
constexpr int AUX_NT = 2, AUX_SC1 = 16;
constexpr float C2 = 0.0625f * 1.4426950408889634f;
constexpr int OOB = 0x7f000000;
constexpr unsigned SPIN_CAP = 1u << 18;

#define RSRC(p, n) __builtin_amdgcn_make_buffer_rsrc((void*)(p), 0, (n), 0x00020000)
#define LD128(r, off, aux) __builtin_amdgcn_raw_buffer_load_b128((r), (off), 0, (aux))
#define ST128(v, r, off, aux) __builtin_amdgcn_raw_buffer_store_b128((v), (r), (off), 0, (aux))
#define SCHED_FENCE() __builtin_amdgcn_sched_barrier(0)
#define MEM_FENCE() asm volatile("" ::: "memory")
template <int N> __device__ __forceinline__ void vmwait() { asm volatile("s_waitcnt vmcnt(%0)" ::"n"(N) : "memory"); }
#define LGKM0() asm volatile("s_waitcnt lgkmcnt(0)" ::: "memory")

__device__ __forceinline__ f32x4 mfma(u32x4 a, u32x4 b, f32x4 c) {
  return __builtin_amdgcn_mfma_f32_16x16x32_bf16(__builtin_bit_cast(bf16x8, a), __builtin_bit_cast(bf16x8, b), c, 0, 0, 0);
}
__device__ __forceinline__ u32x2 trd(LAS unsigned char* p) {
  return __builtin_bit_cast(u32x2, __builtin_amdgcn_ds_read_tr16_b64_v4i16((LAS s16x4*)p));
}
__device__ __forceinline__ unsigned pk_bf16(float lo, float hi) {
  f32x2 v = {lo, hi};
  return __builtin_bit_cast(unsigned, __builtin_convertvector(v, bf16x2));
}
__device__ __forceinline__ float bf_lo(unsigned u) { return __uint_as_float(u << 16); }
__device__ __forceinline__ float bf_hi(unsigned u) { return __uint_as_float(u & 0xffff0000u); }
__device__ __forceinline__ float rbf(float f) { return bf_lo(pk_bf16(f, f)); }
template <int CTRL> __device__ __forceinline__ float dpp(float x) {
  return __builtin_bit_cast(float, __builtin_amdgcn_mov_dpp(__builtin_bit_cast(int, x), CTRL, 0xf, 0xf, true));
}
constexpr int XOR1 = 0xB1, XOR2 = 0x4E, HMIRROR = 0x141, ROR8 = 0x128;
__device__ __forceinline__ float x16_a(float x, float& o) {
  auto s = __builtin_amdgcn_permlane16_swap(__float_as_uint(x), __float_as_uint(x), false, false);
  o = __uint_as_float(s[1]);
  return __uint_as_float(s[0]);
}
__device__ __forceinline__ float x32_a(float x, float& o) {
  auto s = __builtin_amdgcn_permlane32_swap(__float_as_uint(x), __float_as_uint(x), false, false);
  o = __uint_as_float(s[1]);
  return __uint_as_float(s[0]);
}
__device__ __forceinline__ float wave_max(float x) {
  x = fmaxf(x, dpp<XOR1>(x)); x = fmaxf(x, dpp<XOR2>(x)); x = fmaxf(x, dpp<HMIRROR>(x)); x = fmaxf(x, dpp<ROR8>(x));
  float o, a = x16_a(x, o); x = fmaxf(a, o);
  a = x32_a(x, o); return fmaxf(a, o);
}
__device__ __forceinline__ float wave_sum(float x) {
  x += dpp<XOR1>(x); x += dpp<XOR2>(x); x += dpp<HMIRROR>(x); x += dpp<ROR8>(x);
  float o, a = x16_a(x, o); x = a + o;
  a = x32_a(x, o); return a + o;
}
__device__ __forceinline__ void lds_wait_ge(volatile LAS unsigned* f, unsigned want) {
#pragma nounroll
  for (unsigned n = 0; n < (1u << 22); ++n) {
    if (__builtin_amdgcn_readfirstlane(*f) >= want) break;
    __builtin_amdgcn_s_sleep(1);
  }
  MEM_FENCE();
}
__device__ __forceinline__ unsigned bperm(unsigned v, int srclane) { return __builtin_amdgcn_ds_bpermute(srclane << 2, v); }
__device__ __forceinline__ void lds_inc(LAS unsigned char* L, int f, int lane) {
  if (lane == 0) asm volatile("ds_add_u32 %0, %1" ::"v"((unsigned)(uintptr_t)(L + f * 4)), "v"(1u) : "memory");
}
__device__ __forceinline__ unsigned get_tag(volatile LAS unsigned* flag) {
  lds_wait_ge(flag + 0, 1u);
  return __builtin_amdgcn_readfirstlane(flag[1]);
}
__device__ __forceinline__ void dma16(__amdgpu_buffer_rsrc_t r, int voff, unsigned ldsdst) {
  unsigned keep;
  asm volatile("s_mov_b32 %0, m0\n\ts_mov_b32 m0, %3\n\ts_nop 0\n\tbuffer_load_dwordx4 %1, %2, 0 offen nt lds\n\ts_mov_b32 m0, %0"
               : "=&s"(keep) : "v"(voff), "s"(r), "s"(ldsdst) : "memory");
}
__device__ __forceinline__ void dma16p(__amdgpu_buffer_rsrc_t r, int voff, unsigned ldsdst) {
  unsigned keep;
  asm volatile("s_mov_b32 %0, m0\n\ts_mov_b32 m0, %3\n\ts_nop 0\n\tbuffer_load_dwordx4 %1, %2, 0 offen lds\n\ts_mov_b32 m0, %0"
               : "=&s"(keep) : "v"(voff), "s"(r), "s"(ldsdst) : "memory");
}
template <int N>
__device__ __forceinline__ void gatherm(u32x4 (&a)[N], int (&off)[N], unsigned tag, __amdgpu_buffer_rsrc_t r) {
#pragma nounroll
  for (unsigned n = 0; n < SPIN_CAP; ++n) {
    MEM_FENCE();
    u32x4 v[N];
#pragma unroll
    for (int i = 0; i < N; ++i) v[i] = LD128(r, off[i], AUX_SC1);
    bool all = true;
#pragma unroll
    for (int i = 0; i < N; ++i) {
      const bool mine = off[i] != OOB, ok = v[i][1] == tag && v[i][3] == tag;
      a[i][0] = mine ? v[i][0] : a[i][0];
      a[i][2] = mine ? v[i][2] : a[i][2];
      off[i] = ok ? OOB : off[i];
      all = all && (ok || !mine);
    }
    if (__all(all)) break;
  }
}
template <int STEP>
__device__ __forceinline__ void pass4(u32x4 (&a)[4], const u64 (&m)[4], int off, __amdgpu_buffer_rsrc_t r) {
  asm volatile(
      "s_mov_b64 exec, %[m0]\n\tbuffer_load_dwordx4 %[a0], %[o], %[r], 0 offen sc1\n\t"
      "s_mov_b64 exec, %[m1]\n\tbuffer_load_dwordx4 %[a1], %[o], %[r], 0 offen offset:%c[s1] sc1\n\t"
      "s_mov_b64 exec, %[m2]\n\tbuffer_load_dwordx4 %[a2], %[o], %[r], 0 offen offset:%c[s2] sc1\n\t"
      "s_mov_b64 exec, %[m3]\n\tbuffer_load_dwordx4 %[a3], %[o], %[r], 0 offen offset:%c[s3] sc1\n\t"
      "s_mov_b64 exec, -1\n\ts_waitcnt vmcnt(0)"
      : [a0] "+v"(a[0]), [a1] "+v"(a[1]), [a2] "+v"(a[2]), [a3] "+v"(a[3])
      : [m0] "s"(m[0]), [m1] "s"(m[1]), [m2] "s"(m[2]), [m3] "s"(m[3]), [o] "v"(off), [r] "s"(r), [s1] "i"(STEP), [s2] "i"(2 * STEP), [s3] "i"(3 * STEP)
      : "memory");
}
__device__ __forceinline__ bool missing4(const u32x4 (&a)[4], u64 (&m)[4], unsigned tag) {
#pragma unroll
  for (int i = 0; i < 4; ++i) m[i] &= __builtin_amdgcn_ballot_w64(a[i][1] != tag) | __builtin_amdgcn_ballot_w64(a[i][3] != tag);
  return (m[0] | m[1] | m[2] | m[3]) != 0ull;
}
struct Sel { unsigned lo[4], hi[4]; };
__device__ __forceinline__ Sel make_sel(int lane) {
  Sel s;
  const int j = lane & 7;
#pragma unroll
  for (int t = 0; t < 4; ++t) {
    const bool on = (j >> 1) == t;
    s.lo[t] = on ? ((j & 1) ? 0x01000c0cu : 0x0c0c0100u) : 0x0c0c0c0cu;
    s.hi[t] = on ? ((j & 1) ? 0x03020c0cu : 0x0c0c0302u) : 0x0c0c0c0cu;
  }
  return s;
}
__device__ __forceinline__ u32x4 place(unsigned src, const unsigned (&sel)[4]) {
  u32x4 r;
#pragma unroll
  for (int t = 0; t < 4; ++t) r[t] = __builtin_amdgcn_perm(src, src, sel[t]);
  return r;
}

__global__ void __launch_bounds__(1024, 4) fused_kernel(const void*, const void*, const void*, const void*, const void*, const void*, const void*,
                                                     const void*, void*, void*) {
  u32x16 ka;
  u32x4 kb;
  asm volatile("s_load_dwordx16 %0, %2, 0x0\n\ts_load_dwordx4 %1, %2, 0x40" : "=&s"(ka), "=&s"(kb) : "s"(__builtin_amdgcn_kernarg_segment_ptr()) : "memory");
  __shared__ __attribute__((aligned(16))) unsigned char smem[L_END];
  LAS unsigned char* L = (LAS unsigned char*)smem;
  volatile LAS unsigned* flag = (volatile LAS unsigned*)(L + L_FLAG);
  const int bid = blockIdx.x, xc = bid & 7, jj = bid >> 3, kvh = jj & 7, s = jj >> 3, b = (xc - 2 * s) & 7;
  const int tid = threadIdx.x, lane = tid & 63, wave = __builtin_amdgcn_readfirstlane(tid >> 6);
  {
    unsigned long long pc0;
    asm volatile("s_getpc_b64 %0" : "=s"(pc0));
    const auto rc = RSRC(pc0 & ~255ull, CODE_BYTES);
    unsigned keep;
    asm volatile("ds_write_b32 %[fa], %[z]\n\t"
                 "s_cmp_lg_u32 %[w], 7\n\ts_cbranch_scc1 3f\n\t"
                 "s_mov_b32 %[k], m0\n\ts_mov_b32 m0, %[lc]\n\ts_nop 0\n\tbuffer_load_dwordx4 %[oc], %[rc], 0 offen lds\n\ts_mov_b32 m0, %[k]\n"
                 "3:"
                 : [k] "=&s"(keep)
                 : [fa] "v"((unsigned)(uintptr_t)L + (tid & 31) * 4), [z] "v"(0u), [w] "s"(wave), [oc] "v"((bid >> 3) * 1024 + lane * 16), [rc] "s"(rc),
                   [lc] "s"((unsigned)(uintptr_t)(L + L_WO))
                 : "memory", "scc");
  }
  const int cb = jj + 32 * (xc >> 2), rb = (xc + 1) & 3;
  const bool isx = wave == 8;
  int o0 = (lane >> (isx ? 4 : 3)) * (isx ? DM * 2 : NQ * 2) + (lane & (isx ? 15 : 7)) * 16 + (isx ? rb * 224 : ((rb * 112 + (wave - 9) * 16) * NQ + cb * 64) * 2);
  int o1 = o0 + (isx ? 4 * DM * 2 : 8 * NQ * 2);
  const int grp = kvh * 4 + s;
  const int ulo = s * 36, uhi = s == 3 ? 128 : ulo + 36;
  const int nkw = s == 3 ? 4 : 6;
  const int urel = s == 3 && wave == 4 ? 16 : (wave - 1) * 6;
  int klo = (lane & 7) * 512 + ((lane >> 3) & 1) * 64 + (lane >> 4) * 16 + (ulo + urel) * 4096;
  unsigned kvoff = (unsigned)(((b * HKV + kvh) * SK + (SK - WIN)) * DH * 2);
  unsigned lbq = (unsigned)(uintptr_t)(L + L_X) + (wave - 8) * 2048, nrq = isx ? NB * DM * 2 : DM * NQ * 2;
  asm volatile("s_waitcnt lgkmcnt(0)" : "+s"(ka), "+s"(kb), "+v"(o0), "+v"(o1), "+v"(klo), "+s"(kvoff), "+s"(lbq), "+s"(nrq)::"memory");
#define ARG(v, i) ((u64)v[2 * (i)] | ((u64)v[2 * (i) + 1] << 32))
  const GAS u16* x = (const GAS u16*)ARG(ka, 0);
  const u64 kc = ARG(ka, 1), vc = ARG(ka, 2), wo = ARG(ka, 4), w1 = ARG(ka, 5), w2 = ARG(ka, 6), ws = ARG(kb, 1);
  const GAS u16* gw = (const GAS u16*)ARG(ka, 7);
  GAS u16* out = (GAS u16*)ARG(kb, 0);
  unsigned epochv = 0;
  {
    const auto rq = RSRC((u64)(isx ? ka[0] : ka[6]) | ((u64)(isx ? ka[1] : ka[7]) << 32), nrq);
    unsigned keep;
    asm volatile(
        "s_cmp_lt_u32 %[w], 8\n\ts_cbranch_scc1 1f\n\t"
        "s_mov_b32 %[k], m0\n\ts_mov_b32 m0, %[lb]\n\ts_nop 0\n\tbuffer_load_dwordx4 %[o0], %[r], 0 offen nt lds\n\t"
        "s_add_u32 m0, %[lb], 0x400\n\ts_nop 0\n\tbuffer_load_dwordx4 %[o1], %[r], 0 offen nt lds\n\ts_mov_b32 m0, %[k]\n"
        "1:\n\ts_cmp_lg_u32 %[w], 0\n\ts_cbranch_scc1 2f\n\tglobal_load_dword %[e], %[e], %[ws] sc1\n\ts_barrier\n\ts_waitcnt vmcnt(0)\n\ts_branch 4f\n"
        "2:\n\ts_barrier\n"
        "4:"
        : [e] "+v"(epochv), [k] "=&s"(keep)
        : [w] "s"(wave), [lb] "s"(lbq), [o0] "v"(o0), [o1] "v"(o1), [r] "s"(rq), [ws] "s"(ws)
        : "memory", "scc");
  }
  const unsigned epoch = __builtin_amdgcn_readfirstlane(epochv);
  if (wave == 0) {
    if (lane == 0) *(volatile LAS u32x2*)flag = u32x2{1u, epoch * 1664525u + 1013904223u};
  }
  const auto rws = RSRC(ws, WS_END);
  unsigned tag = 0;

  if (__builtin_expect(wave >= 1 && wave <= nkw, 1)) {
    const int kw = wave - 1;
    const int nu = s == 3 && wave >= 3 ? 4 : 6;
    const int u0 = ulo + urel;
    const int brot = wave + jj + xc;
    const auto rk = RSRC(kc + kvoff, WIN * DH * 2);
    u32x4 r[24];
    const unsigned hsh = ((unsigned)(bid * 8 + wave) * 2654435761u) >> 16;
    const int urot = hsh & 1, pr = nu == 6 ? (hsh >> 1) % 3u : (hsh >> 1) & 1;
    const int up[3] = {pr * 2, nu == 6 ? (pr == 2 ? 0 : pr * 2 + 2) : 2 - pr * 2, nu == 6 ? (pr == 0 ? 4 : pr * 2 - 2) : 4};
    const int lo5 = nu == 6 ? klo : OOB;
#pragma unroll
    for (int i = 0; i < 24; ++i) {
      if (i >= WK) vmwait<WK - 1>();
      r[i] = __builtin_amdgcn_raw_buffer_load_b128(rk, i < 16 ? klo : lo5, (up[i >> 3] + (((i >> 2) & 1) ^ urot)) * 4096 + ((brot + i) & 3) * 128, AUX_NT);
      SCHED_FENCE();
      if (i == KTRIG) lds_inc(L, F_KISS, lane);
    }
    __builtin_amdgcn_s_setprio(2);
    lds_wait_ge(flag + F_QRDY, 1u);
    tag = __builtin_amdgcn_readfirstlane(flag[F_TAG]);
    u32x4 qa[4];
#pragma unroll
    for (int t = 0; t < 4; ++t) qa[t] = *(const LAS u32x4*)(L + L_Q + (lane & 1) * 512 + ((brot + t) & 3) * 128 + ((lane >> 1) & 1) * 64 + (lane >> 4) * 16);
    float s0 = 0.f, s1 = 0.f;
#pragma unroll
    for (int u = 0; u < 6; ++u) {
      f32x4 c = {0, 0, 0, 0};
#pragma unroll
      for (int t = 0; t < 4; ++t) c = mfma(qa[t], r[4 * u + t], c);
      float v0 = (lane & 8) ? c[2] : c[0], v1 = (lane & 8) ? c[3] : c[1];
      v0 += dpp<ROR8>(v0); v1 += dpp<ROR8>(v1);
      if ((lane >> 3) == up[u >> 1] + ((u & 1) ^ urot)) { s0 = v0; s1 = v1; }
    }
    const unsigned pk = pk_bf16(s0, s1);
    const unsigned pa = bperm(pk, (2 * lane) & 63), pb = bperm(pk, (2 * lane + 1) & 63);
    if (lane < 4 * nu) {
      ST128((u32x4{pa, tag, pb, tag}), rws, WS_S + (b * 8 + kvh) * 8192 + u0 * 64 + lane * 16, AUX_SC1);
      *(LAS u32x4*)(L + L_SM + urel * 64 + lane * 16) = u32x4{pa, tag, pb, tag};
    }
    MEM_FENCE();
    lds_inc(L, F_KCNT, lane);
    if (wave <= 4) {
      u32x4 sg[2];
      int of[2];
      bool own[2];
#pragma unroll
      for (int i = 0; i < 2; ++i) {
        const int myu = (kw + 4 * i) * 16 + (lane >> 2);
        own[i] = myu >= ulo && myu < uhi;
        of[i] = own[i] ? OOB : WS_S + (b * 8 + kvh) * 8192 + (kw + 4 * i) * 1024 + lane * 16;
      }
      __builtin_amdgcn_s_sleep(SSLEEP);
      gatherm<2>(sg, of, tag, rws);
      lds_wait_ge(flag + F_KCNT, (unsigned)nkw);
#pragma unroll
      for (int i = 0; i < 2; ++i) {
        const int c = kw + 4 * i;
        const u32x4 mv = *(const LAS u32x4*)(L + L_SM + (own[i] ? (c * 16 - ulo) * 64 + lane * 16 : 0));
        const unsigned sa = own[i] ? mv[0] : sg[i][0], sb = own[i] ? mv[2] : sg[i][2];
        const float m0 = wave_max(fmaxf(bf_lo(sa), bf_lo(sb))), m1 = wave_max(fmaxf(bf_hi(sa), bf_hi(sb)));
        const float n0 = m0 == -INFINITY ? 0.f : m0, n1 = m1 == -INFINITY ? 0.f : m1;
        const float ea = __builtin_amdgcn_exp2f((bf_lo(sa) - n0) * C2), eb = __builtin_amdgcn_exp2f((bf_lo(sb) - n0) * C2);
        const float ec = __builtin_amdgcn_exp2f((bf_hi(sa) - n1) * C2), ed = __builtin_amdgcn_exp2f((bf_hi(sb) - n1) * C2);
        *(LAS unsigned*)(L + L_P + c * 512 + lane * 4) = pk_bf16(ea, eb);
        *(LAS unsigned*)(L + L_P + c * 512 + 256 + lane * 4) = pk_bf16(ec, ed);
        const float l0 = wave_sum(ea + eb), l1 = wave_sum(ec + ed);
        if (lane == 0) *(LAS f32x4*)(L + L_ST + c * 16) = f32x4{m0, l0, m1, l1};
      }
      MEM_FENCE();
      lds_inc(L, F_PR + kw, lane);
      lds_inc(L, F_SCNT, lane);
    }
  } else if (wave >= 8) {
    const int w = wave - 8;
    vmwait<0>();
    lds_inc(L, F_WQC, lane);
    tag = get_tag(flag);
    if (w >= 4) {
      __builtin_amdgcn_s_setprio(3);
      const int ct = w - 4;
      const int xrow = ((lane & 7) >> 1) + 4 * (lane & 1);
      LAS unsigned char* xp = L + L_X + xrow * 256 + (lane >> 4) * 16;
      LAS unsigned char* xp2 = L + L_X + xrow * 256 + 192 + (lane >> 4) * 8;
      LAS unsigned char* tb = L + L_WQ + ((lane >> 4) * 8 + ((lane >> 2) & 3)) * 128 + (lane & 3) * 8 + ct * 32;
      LAS unsigned char* tb2 = L + L_WQ + (96 + (lane >> 4) * 4 + ((lane >> 2) & 3)) * 128 + (lane & 3) * 8 + ct * 32;
      const int dest = ((lane >> 4) & 1) * 2 + (lane >> 5);
      const int qo = WS_QP + (cb * 4 + dest) * QPSTR + rb * 1024 + ct * 256 + (lane & 15) * 16;
      lds_wait_ge(flag + F_WQC, 8u);
      const u32x2 pt = trd(tb2), xb = *(const LAS u32x2*)xp2;
      f32x4 acc = mfma(u32x4{xb[0], xb[1], 0u, 0u}, u32x4{pt[0], pt[1], 0u, 0u}, f32x4{0, 0, 0, 0});
#pragma unroll
      for (int ks = 0; ks < 3; ++ks) {
        const u32x2 p0 = trd(tb + ks * 4096), p1 = trd(tb + ks * 4096 + 512);
        acc = mfma(*(const LAS u32x4*)(xp + ks * 64), u32x4{p0[0], p0[1], p1[0], p1[1]}, acc);
      }
      const float v0 = lane >= 32 ? acc[2] : acc[0], v1 = lane >= 32 ? acc[3] : acc[1];
      if (dest != rb) ST128((u32x4{__float_as_uint(v0), tag, __float_as_uint(v1), tag}), rws, qo, AUX_SC1);
      else *(LAS f32x2*)(L + L_QO + (ct * 16 + (lane & 15)) * 8) = f32x2{v0, v1};
      LGKM0();
      lds_inc(L, F_QPC, lane);
      __builtin_amdgcn_s_setprio(0);
    }
    const auto rv = RSRC(vc + kvoff, WIN * DH * 2);
    u32x4 r[16];
    const int trot = w + jj + xc;
    const int voff = (w * 128 + (lane >> 4) * 8 + ((lane >> 3) & 1)) * 512 + s * 128 + (lane & 7) * 16;
    const Sel sel = make_sel(lane);
    lds_wait_ge(flag + F_KISS, (unsigned)nkw);
#define V_ISSUE(i) do { if ((i) >= WV) vmwait<WV - 1>(); \
      r[i] = __builtin_amdgcn_raw_buffer_load_b128(rv, voff + ((i) & 3) * 1024, ((((i) >> 2) + trot) & 3) * 16384, AUX_NT); SCHED_FENCE(); \
      if ((i) == 15) lds_inc(L, F_VISS, lane); } while (0)
#define V_USE(i) do { a0 = mfma(place(pp[(i) >> 2][(i) & 3], sel.lo), r[i], a0); a1 = mfma(place(pp[(i) >> 2][(i) & 3], sel.hi), r[i], a1); } while (0)
#pragma unroll
    for (int i = 0; i <= VSPLIT; ++i) V_ISSUE(i);
    u32x4 pp[4];
    f32x4 a0 = {0, 0, 0, 0}, a1 = {0, 0, 0, 0};
#define P_READ() _Pragma("unroll") for (int t = 0; t < 4; ++t) \
      pp[t] = *(const LAS u32x4*)(L + L_P + w * 512 + ((lane >> 3) & 1) * 256 + (((t + trot) & 3) * 32 + (lane >> 4) * 8) * 2)
    float f;
    unsigned wad = (unsigned)(uintptr_t)(L + L_OP) + w * 512 + ((lane >> 5) * 64 + (lane & 7) * 8 + ((lane >> 4) & 1) * 4) * 4;
    unsigned rad = (unsigned)(uintptr_t)(L + L_OP) + (lane >> 5) * 2048 + (lane & 31) * 16;
    int oad = WS_OB + (grp * 8 + b) * 512 + lane * 16;
#define F_GET() do { lds_wait_ge(flag + F_FRDY, 1u); f = *(const LAS float*)(L + L_FW + w * 8 + (lane >> 5) * 4); \
      asm volatile("" : "+v"(f), "+v"(wad), "+v"(rad), "+v"(oad)); } while (0)
    if (__builtin_expect(__builtin_amdgcn_readfirstlane(flag[F_PR + (w & 3)]) != 0u, 1)) {
      MEM_FENCE();
      P_READ();
#pragma unroll
      for (int i = VSPLIT + 1; i < 16; ++i) {
        V_ISSUE(i);
#pragma unroll
        for (int k = 0; k < 16; ++k)
          if (k >= (i - VSPLIT - 1) * VPER && k < (i - VSPLIT) * VPER && k <= i - WV) V_USE(k);
        SCHED_FENCE();
      }
#pragma unroll
      for (int k = 0; k < 16 - WV; ++k) {
        bool done = false;
#pragma unroll
        for (int i = VSPLIT + 1; i < 16; ++i) done = done || (k >= (i - VSPLIT - 1) * VPER && k < (i - VSPLIT) * VPER && k <= i - WV);
        if (!done) { V_USE(k); if (k & 1) SCHED_FENCE(); }
      }
      SCHED_FENCE();
      F_GET();
      u32x4 la[WV], lb[WV];
#pragma unroll
      for (int k = 0; k < WV; ++k) {
        la[k] = place(pp[(16 - WV + k) >> 2][(16 - WV + k) & 3], sel.lo);
        lb[k] = place(pp[(16 - WV + k) >> 2][(16 - WV + k) & 3], sel.hi);
        asm volatile("" : "+v"(la[k]), "+v"(lb[k]));
      }
      SCHED_FENCE();
#pragma unroll
      for (int k = 0; k < WV; ++k) { a0 = mfma(la[k], r[16 - WV + k], a0); a1 = mfma(lb[k], r[16 - WV + k], a1); }
    } else {
#pragma unroll
      for (int i = VSPLIT + 1; i < 16; ++i) V_ISSUE(i);
      lds_wait_ge(flag + F_PR + (w & 3), 1u);
      P_READ();
#pragma unroll
      for (int k = 0; k < 16; ++k) { V_USE(k); if (k & 1) SCHED_FENCE(); }
      F_GET();
    }
    f32x4 t = (lane & 8) ? a1 : a0;
#pragma unroll
    for (int k = 0; k < 4; ++k) t[k] = (t[k] + dpp<ROR8>(t[k])) * f;
    unsigned old;
    f32x4 o0v, o1v, o2v, o3v;
    asm volatile("ds_write_b128 %[wa], %[t]\n\tds_add_rtn_u32 %[old], %[ca], %[one]\n\t"
                 "ds_read_b128 %[p0], %[ra]\n\tds_read_b128 %[p1], %[ra] offset:512\n\tds_read_b128 %[p2], %[ra] offset:1024\n\tds_read_b128 %[p3], %[ra] offset:1536\n\t"
                 "s_waitcnt lgkmcnt(0)"
                 : [old] "=&v"(old), [p0] "=&v"(o0v), [p1] "=&v"(o1v), [p2] "=&v"(o2v), [p3] "=&v"(o3v)
                 : [wa] "v"(wad), [t] "v"(t), [ca] "v"((unsigned)(uintptr_t)(L + (lane == 0 ? F_OCNT * 4 : 128 + (lane & 31) * 4))), [one] "v"(1u), [ra] "v"(rad)
                 : "memory");
    if (__builtin_amdgcn_readfirstlane(old) == 7u) {
      f32x4 ov = (o0v + o1v) + (o2v + o3v);
#pragma unroll
      for (int k = 0; k < 4; ++k) { float hi, lo = x32_a(ov[k], hi); ov[k] = lo + hi; }
      if (lane < 32) {
        const u32x4 ogr = {pk_bf16(ov[0], ov[1]), tag, pk_bf16(ov[2], ov[3]), tag};
        ST128(ogr, rws, oad, AUX_SC1);
        *(LAS u32x4*)(L + L_OM + lane * 16) = ogr;
      }
      MEM_FENCE();
      lds_inc(L, F_OMR, lane);
    }
  } else if (wave == 0) {
    __builtin_amdgcn_s_setprio(3);
    tag = epoch * 1664525u + 1013904223u;
    lds_wait_ge(flag + F_QPC, 4u);
    __builtin_amdgcn_s_sleep(QSLEEP);
    {
      const int po = WS_QP + (cb * 4 + rb) * QPSTR + lane * 16;
      u32x4 pg[3];
      int of[3] = {po + ((rb + 1) & 3) * 1024, po + ((rb + 2) & 3) * 1024, po + ((rb + 3) & 3) * 1024};
      gatherm<3>(pg, of, tag, rws);
      const f32x2 own = *(const LAS f32x2*)(L + L_QO + lane * 8);
      const float a0 = (own[0] + __uint_as_float(pg[0][0])) + (__uint_as_float(pg[1][0]) + __uint_as_float(pg[2][0]));
      const float a1 = (own[1] + __uint_as_float(pg[0][2])) + (__uint_as_float(pg[1][2]) + __uint_as_float(pg[2][2]));
      const float n0 = dpp<XOR1>(a0), n1 = dpp<XOR1>(a1);
      const unsigned pk = (lane & 1) ? pk_bf16(n1, a1) : pk_bf16(a0, n0);
      const int src = (lane & 15) * 4 + ((lane >> 4) & 1);
      const unsigned pa = bperm(pk, src), pb = bperm(pk, src + 2);
      if (lane < 32) ST128((u32x4{pa, tag, pb, tag}), rws, WS_Q + ((cb >> 3) * 8 + rb + 4 * (lane >> 4)) * QSTR + (cb & 7) * 256 + (lane & 15) * 16, AUX_SC1);
    }
    __builtin_amdgcn_s_sleep(QSLEEP);
    {
      const int qoff = WS_Q + (kvh * 8 + b) * QSTR + lane * 16;
      u32x4 qq[2];
      int of[2] = {qoff, qoff + 1024};
      gatherm<2>(qq, of, tag, rws);
      *(LAS u32x2*)(L + L_Q + lane * 8) = u32x2{qq[0][0], qq[0][2]};
      *(LAS u32x2*)(L + L_Q + 512 + lane * 8) = u32x2{qq[1][0], qq[1][2]};
      LGKM0();
      if (lane == 0) flag[F_QRDY] = 1u;
    }
    u32x4 xg = {0, 0, 0, 0}, gg = {0, 0, 0, 0};
    if (lane < 56) {
      xg = *(const GAS u32x4*)(x + b * DM + lane * 8);
      gg = *(const GAS u32x4*)(gw + lane * 8);
    }
    float z = 0.f;
#pragma unroll
    for (int t = 0; t < 4; ++t) z += rbf(bf_lo(xg[t]) * bf_lo(gg[t])) + rbf(bf_hi(xg[t]) * bf_hi(gg[t]));
    z = rbf(wave_sum(z));
    const float g = rbf(1.f / (1.f + __expf(-z)));
    const unsigned g1u = __float_as_uint(rbf(1.f - g));
    if (s == 0 && lane < 8) ST128((u32x4{g1u, tag, g1u, tag}), rws, WS_G + (kvh * 8 + b) * 128 + lane * 16, AUX_SC1);
    lds_wait_ge(flag + F_SCNT, 4u);
    {
      const f32x2 ml = *(const LAS f32x2*)(L + L_ST + (lane & 15) * 8);
      float M = fmaxf(ml[0], dpp<XOR2>(ml[0]));
      M = fmaxf(M, dpp<0x124>(M)); M = fmaxf(M, dpp<ROR8>(M));
      const float al = __builtin_amdgcn_exp2f((ml[0] - M) * C2);
      float Ls = al * ml[1];
      Ls += dpp<XOR2>(Ls); Ls += dpp<0x124>(Ls); Ls += dpp<ROR8>(Ls);
      if (lane < 16) *(LAS float*)(L + L_FW + lane * 4) = al * (g / Ls);
      LGKM0();
      if (lane == 0) flag[F_FRDY] = 1u;
    }
  } else if (wave != 7 && xc < 7) {
    const int j = wave - 5, t = xc * 8 + kvh;
    const unsigned lb = (unsigned)(uintptr_t)(L + L_T) + j * 14336;
    const auto r1 = RSRC(w1, DM * FF * 2);
    const int o = ((j * 224 + (lane >> 2)) * FF + t * 32 + (lane & 3) * 8) * 2;
#pragma unroll
    for (int i = 0; i < 14; ++i) {
      if (i >= WT) vmwait<WT - 1>();
      dma16(r1, o + i * 16 * FF * 2, lb + i * 1024);
    }
    {
      const auto rx = RSRC((u64)x, NB * DM * 2);
#pragma unroll
      for (int i = 0; i < 4; ++i) {
        vmwait<WT - 1>();
        dma16p(rx, (j * 4 + i) * 1024 + lane * 16, (unsigned)(uintptr_t)(L + L_XA) + (j * 4 + i) * 1024);
      }
    }
    tag = get_tag(flag);
    vmwait<0>();
    lds_inc(L, F_TCNT, lane);
    lds_wait_ge(flag + F_TCNT, 2u);
    LAS unsigned char* tb = L + L_T + ((lane >> 4) * 8 + ((lane >> 2) & 3)) * 64 + j * 32 + (lane & 3) * 8;
    f32x4 acc0 = {0, 0, 0, 0}, acc1 = {0, 0, 0, 0};
#pragma unroll
    for (int ks = 0; ks < 14; ++ks) {
      const u32x2 p0 = trd(tb + ks * 2048), p1 = trd(tb + ks * 2048 + 256);
      const u32x4 xa = *(const LAS u32x4*)(L + L_XA + (lane & 7) * (DM * 2) + ks * 64 + (lane >> 4) * 16);
      if (ks & 1) acc1 = mfma(u32x4{p0[0], p0[1], p1[0], p1[1]}, xa, acc1);
      else acc0 = mfma(u32x4{p0[0], p0[1], p1[0], p1[1]}, xa, acc0);
    }
    acc0 += acc1;
    float hv[4];
#pragma unroll
    for (int k = 0; k < 4; ++k) {
      const float v = rbf(acc0[k]);
      hv[k] = v / (1.f + __expf(-v));
    }
    if (!(lane & 8))
      ST128((u32x4{pk_bf16(hv[0], hv[1]), tag, pk_bf16(hv[2], hv[3]), tag}), rws, WS_HB + t * 1024 + j * 512 + (lane >> 4) * 128 + (lane & 7) * 16, AUX_SC1);
  }
  const int cpair = (xc ^ 1) * 32 + jj;
  if (wave == 7 && b < 7) {
    const int fgrp = s < 3 ? kvh * 3 + s : (kvh < 4 ? 24 + kvh : -1);
    tag = get_tag(flag);
    const bool hi8 = lane & 8;
    u32x4 hb[3];
    {
      const unsigned lb = (unsigned)(uintptr_t)(L + L_WO);
      const auto r2 = RSRC(w2, FF * DM * 2);
      const auto ro = RSRC(wo, NQ * DM * 2);
      const int off2 = (fgrp * 64 + (lane >> 3)) * 896 + b * 128 + (lane & 7) * 16;
      const int offo = (kvh * 512 + s * 64 + (lane >> 3)) * 896 + b * 128 + (lane & 7) * 16;
      lds_wait_ge(flag + F_VISS, 8u);
      if (fgrp >= 0) {
#pragma unroll
        for (int i = 0; i < 8; ++i) dma16(r2, off2 + i * 8 * 896, lb + 16384 + i * 1024);
      }
#pragma unroll
      for (int i = 0; i < 16; ++i) dma16(ro, offo + ((i >> 3) * 256 + (i & 7) * 8) * 896, lb + i * 1024);
      if (fgrp >= 0) {
        int of[3] = {WS_HB + fgrp * 2048 + lane * 16, WS_HB + fgrp * 2048 + 1024 + lane * 16, WS_G + (kvh * 8 + (lane & 7)) * 128};
        gatherm<3>(hb, of, tag, rws);
      }
      vmwait<0>();
    }
    __builtin_amdgcn_s_setprio(3);
    const bool ownb = (lane & 7) == b;
    const int ooff = WS_OB + grp * 4096 + (lane & 7) * 512 + (lane >> 3) * 16;
    u32x4 ob[4];
#pragma unroll
    for (int i = 0; i < 4; ++i) ob[i] = LD128(rws, ownb ? OOB : ooff + i * 128, AUX_SC1);
    SCHED_FENCE();
    LAS unsigned char* tb = L + L_WO + ((lane >> 4) * 8 + ((lane >> 2) & 3)) * 128 + (lane & 3) * 8;
    f32x4 acc[4] = {{0, 0, 0, 0}, {0, 0, 0, 0}, {0, 0, 0, 0}, {0, 0, 0, 0}};
    if (fgrp >= 0) {
      const u32x4 hv = {hb[0][0], hb[0][2], hb[1][0], hb[1][2]};
      const float g1 = __uint_as_float(hb[2][0]);
#pragma unroll
      for (int ks = 0; ks < 2; ++ks) {
        const u32x4 bf = {hi8 ? 0u : hv[2 * ks], hi8 ? 0u : hv[2 * ks + 1], hi8 ? hv[2 * ks] : 0u, hi8 ? hv[2 * ks + 1] : 0u};
#pragma unroll
        for (int ct = 0; ct < 4; ++ct) {
          const u32x2 p0 = trd(tb + 16384 + ks * 4096 + ct * 32), p1 = trd(tb + 16384 + ks * 4096 + 512 + ct * 32);
          acc[ct] = mfma(u32x4{p0[0], p0[1], p1[0], p1[1]}, bf, acc[ct]);
        }
      }
#pragma unroll
      for (int ct = 0; ct < 4; ++ct) acc[ct] *= g1;
    }
    u32x4 wf[4][4];
#pragma unroll
    for (int ks = 0; ks < 4; ++ks)
#pragma unroll
      for (int ct = 0; ct < 4; ++ct) {
        const u32x2 p0 = trd(tb + ks * 4096 + ct * 32), p1 = trd(tb + ks * 4096 + 512 + ct * 32);
        wf[ks][ct] = u32x4{p0[0], p0[1], p1[0], p1[1]};
      }
    int po = WS_PO + (b * 32 + (lane >> 3)) * PSTR + grp * 128 + (lane & 7) * 16;
    asm volatile("" : "+v"(po));
    lds_wait_ge(flag + F_OMR, 1u);
#pragma unroll
    for (int i = 0; i < 4; ++i) {
      const u32x4 v = *(const LAS u32x4*)(L + L_OM + i * 128 + (lane >> 3) * 16);
      ob[i] = ownb ? v : ob[i];
    }
    u64 ms[4] = {~0ull, ~0ull, ~0ull, ~0ull};
    if (missing4(ob, ms, tag)) {
#pragma nounroll
      for (unsigned n = 0; n < SPIN_CAP; ++n) {
        pass4<128>(ob, ms, ooff, rws);
        if (!missing4(ob, ms, tag)) break;
      }
    }
    u32x4 bf[4];
#pragma unroll
    for (int ks = 0; ks < 4; ++ks) bf[ks] = u32x4{hi8 ? 0u : ob[ks][0], hi8 ? 0u : ob[ks][2], hi8 ? ob[ks][0] : 0u, hi8 ? ob[ks][2] : 0u};
#pragma unroll
    for (int ct = 0; ct < 4; ++ct)
#pragma unroll
      for (int ks = 0; ks < 4; ++ks) acc[ct] = mfma(wf[ks][ct], bf[ks], acc[ct]);
    {
#pragma unroll
      for (int ct = 0; ct < 4; ++ct) {
        const float k0 = hi8 ? acc[ct][2] : acc[ct][0], k1 = hi8 ? acc[ct][3] : acc[ct][1];
        const float s0 = hi8 ? acc[ct][0] : acc[ct][2], s1 = hi8 ? acc[ct][1] : acc[ct][3];
        const u32x4 pv = {__float_as_uint(k0 + dpp<ROR8>(s0)), tag, __float_as_uint(k1 + dpp<ROR8>(s1)), tag};
        ST128(pv, rws, po + ct * 8 * PSTR, AUX_SC1);
      }
    }
  } else if (wave == 0 && cpair < 224) {
    __builtin_amdgcn_s_setprio(3);
    lds_wait_ge(flag + F_OCNT, 8u);
    __builtin_amdgcn_s_sleep(FSLEEP);
    const int poff = WS_PO + cpair * PSTR + lane * 16;
    u32x4 pg[4] = {{0, 0, 0, 0}, {0, 0, 0, 0}, {0, 0, 0, 0}, {0, 0, 0, 0}};
    u64 ms[4] = {~0ull, ~0ull, ~0ull, ~0ull};
    GAS unsigned* op = (GAS unsigned*)(out + (lane & 7) * DM + cpair * 2);
    asm volatile("" : "+v"(op));
#pragma nounroll
    for (unsigned n = 0; n < SPIN_CAP; ++n) {
      pass4<1024>(pg, ms, poff, rws);
      if (!missing4(pg, ms, tag)) {
        float a0 = (__uint_as_float(pg[0][0]) + __uint_as_float(pg[1][0])) + (__uint_as_float(pg[2][0]) + __uint_as_float(pg[3][0]));
        float a1 = (__uint_as_float(pg[0][2]) + __uint_as_float(pg[1][2])) + (__uint_as_float(pg[2][2]) + __uint_as_float(pg[3][2]));
        a0 += dpp<ROR8>(a0); a1 += dpp<ROR8>(a1);
        float o, t;
        t = x16_a(a0, o); a0 = t + o; t = x16_a(a1, o); a1 = t + o;
        t = x32_a(a0, o); a0 = t + o; t = x32_a(a1, o); a1 = t + o;
        if (lane < 8) *op = pk_bf16(a0, a1);
        if (bid == 0 && lane == 0) __hip_atomic_store((GAS unsigned*)(ws + WS_EPOCH), tag, __ATOMIC_RELAXED, __HIP_MEMORY_SCOPE_AGENT);
        break;
      }
    }
  }
}

extern "C" void kernel_launch(void* const* d_in, const int* in_sizes, int n_in, void* d_out, int out_size, void* d_ws, size_t ws_size,
                              hipStream_t stream) {
  fused_kernel<<<256, 1024, 0, stream>>>(d_in[0], d_in[1], d_in[2], d_in[3], d_in[4], d_in[6], d_in[7], d_in[8], d_out, d_ws);
}
```

```cpp
#include <hip/hip_runtime.h>
#include <hip/hip_bf16.h>
#include <stdint.h>

constexpr int CODE_BYTES = 18176;
constexpr int WK0 = 5, WK = 4, WV = 3, WT = 3;
constexpr int KTRIG = 22;
constexpr int VSPLIT = 10, VPER = 3;
#define QSLEEP 10
#define SSLEEP 24
#define FSLEEP 32

typedef unsigned u32x4 __attribute__((ext_vector_type(4)));
typedef unsigned u32x2 __attribute__((ext_vector_type(2)));
typedef float f32x4 __attribute__((ext_vector_type(4)));
typedef float f32x2 __attribute__((ext_vector_type(2)));
typedef short s16x4 __attribute__((ext_vector_type(4)));
typedef __bf16 bf16x2 __attribute__((ext_vector_type(2)));
typedef __bf16 bf16x8 __attribute__((ext_vector_type(8)));
typedef unsigned short u16;
typedef unsigned u32x16 __attribute__((ext_vector_type(16)));
typedef unsigned long long u64;
#define LAS __attribute__((address_space(3)))
#define GAS __attribute__((address_space(1)))

constexpr int NB = 8, DM = 448, HKV = 8, SK = 4096, DH = 256, WIN = 1024, FF = 1792, NQ = 4096;
constexpr int PSTR = 4096 + 128;
constexpr int QPSTR = 4096 + 256, QSTR = 2048 + 128;
constexpr int WS_EPOCH = 0;
constexpr int WS_G = 4096;
constexpr int WS_QP = 16384;
constexpr int WS_Q = WS_QP + 256 * QPSTR;
constexpr int WS_HB = WS_Q + 64 * QSTR;
constexpr int WS_S = WS_HB + 65536;
constexpr int WS_OB = WS_S + 524288;
constexpr int WS_PO = WS_OB + 131072;
constexpr int WS_END = WS_PO + 224 * PSTR;
constexpr int L_FLAG = 0, L_Q = 256, L_QO = L_Q + 1024, L_ST = L_QO + 512, L_FW = L_ST + 128, L_OM = L_FW + 64, L_SM = L_OM + 512, L_P = L_SM + 2304, L_OP = L_P + 4096, L_WO = L_OP + 4096, L_W2 = L_WO + 16384,
              L_X = L_W2 + 8192, L_WQ = L_X + 2048, L_T = L_WQ + 14336, L_XA = L_T + 28672, L_END = L_XA + 8192;
enum { F_TAGV = 0, F_TAG = 1, F_QRDY = 2, F_WQC = 3, F_QPC = 4, F_KISS = 5, F_OCNT = 6, F_TCNT = 7, F_VISS = 8, F_SCNT = 9, F_FRDY = 10, F_KCNT = 11, F_OMR = 12, F_PR = 16 };
constexpr int AUX_NT = 2, AUX_SC1 = 16;
constexpr float C2 = 0.0625f * 1.4426950408889634f;
constexpr int OOB = 0x7f000000;
constexpr unsigned SPIN_CAP = 1u << 18;

#define RSRC(p, n) __builtin_amdgcn_make_buffer_rsrc((void*)(p), 0, (n), 0x00020000)
#define LD128(r, off, aux) __builtin_amdgcn_raw_buffer_load_b128((r), (off), 0, (aux))
#define ST128(v, r, off, aux) __builtin_amdgcn_raw_buffer_store_b128((v), (r), (off), 0, (aux))
#define SCHED_FENCE() __builtin_amdgcn_sched_barrier(0)
#define MEM_FENCE() asm volatile("" ::: "memory")
template <int N> __device__ __forceinline__ void vmwait() { asm volatile("s_waitcnt vmcnt(%0)" ::"n"(N) : "memory"); }
#define LGKM0() asm volatile("s_waitcnt lgkmcnt(0)" ::: "memory")

__device__ __forceinline__ f32x4 mfma(u32x4 a, u32x4 b, f32x4 c) {
  return __builtin_amdgcn_mfma_f32_16x16x32_bf16(__builtin_bit_cast(bf16x8, a), __builtin_bit_cast(bf16x8, b), c, 0, 0, 0);
}
__device__ __forceinline__ u32x2 trd(LAS unsigned char* p) {
  return __builtin_bit_cast(u32x2, __builtin_amdgcn_ds_read_tr16_b64_v4i16((LAS s16x4*)p));
}
__device__ __forceinline__ unsigned pk_bf16(float lo, float hi) {
  f32x2 v = {lo, hi};
  return __builtin_bit_cast(unsigned, __builtin_convertvector(v, bf16x2));
}
__device__ __forceinline__ float bf_lo(unsigned u) { return __uint_as_float(u << 16); }
__device__ __forceinline__ float bf_hi(unsigned u) { return __uint_as_float(u & 0xffff0000u); }
__device__ __forceinline__ float rbf(float f) { return bf_lo(pk_bf16(f, f)); }
template <int CTRL> __device__ __forceinline__ float dpp(float x) {
  return __builtin_bit_cast(float, __builtin_amdgcn_mov_dpp(__builtin_bit_cast(int, x), CTRL, 0xf, 0xf, true));
}
constexpr int XOR1 = 0xB1, XOR2 = 0x4E, HMIRROR = 0x141, ROR8 = 0x128;
__device__ __forceinline__ float x16_a(float x, float& o) {
  auto s = __builtin_amdgcn_permlane16_swap(__float_as_uint(x), __float_as_uint(x), false, false);
  o = __uint_as_float(s[1]);
  return __uint_as_float(s[0]);
}
__device__ __forceinline__ float x32_a(float x, float& o) {
  auto s = __builtin_amdgcn_permlane32_swap(__float_as_uint(x), __float_as_uint(x), false, false);
  o = __uint_as_float(s[1]);
  return __uint_as_float(s[0]);
}
__device__ __forceinline__ float wave_max(float x) {
  x = fmaxf(x, dpp<XOR1>(x)); x = fmaxf(x, dpp<XOR2>(x)); x = fmaxf(x, dpp<HMIRROR>(x)); x = fmaxf(x, dpp<ROR8>(x));
  float o, a = x16_a(x, o); x = fmaxf(a, o);
  a = x32_a(x, o); return fmaxf(a, o);
}
__device__ __forceinline__ float wave_sum(float x) {
  x += dpp<XOR1>(x); x += dpp<XOR2>(x); x += dpp<HMIRROR>(x); x += dpp<ROR8>(x);
  float o, a = x16_a(x, o); x = a + o;
  a = x32_a(x, o); return a + o;
}
__device__ __forceinline__ void lds_wait_ge(volatile LAS unsigned* f, unsigned want) {
#pragma nounroll
  for (unsigned n = 0; n < (1u << 22); ++n) {
    if (__builtin_amdgcn_readfirstlane(*f) >= want) break;
    __builtin_amdgcn_s_sleep(1);
  }
  MEM_FENCE();
}
__device__ __forceinline__ unsigned bperm(unsigned v, int srclane) { return __builtin_amdgcn_ds_bpermute(srclane << 2, v); }
__device__ __forceinline__ void lds_inc(LAS unsigned char* L, int f, int lane) {
  if (lane == 0) asm volatile("ds_add_u32 %0, %1" ::"v"((unsigned)(uintptr_t)(L + f * 4)), "v"(1u) : "memory");
}
__device__ __forceinline__ unsigned get_tag(volatile LAS unsigned* flag) {
  lds_wait_ge(flag + 0, 1u);
  return __builtin_amdgcn_readfirstlane(flag[1]);
}
__device__ __forceinline__ void dma16(__amdgpu_buffer_rsrc_t r, int voff, unsigned ldsdst) {
  unsigned keep;
  asm volatile("s_mov_b32 %0, m0\n\ts_mov_b32 m0, %3\n\ts_nop 0\n\tbuffer_load_dwordx4 %1, %2, 0 offen nt lds\n\ts_mov_b32 m0, %0"
               : "=&s"(keep) : "v"(voff), "s"(r), "s"(ldsdst) : "memory");
}
__device__ __forceinline__ void dma16p(__amdgpu_buffer_rsrc_t r, int voff, unsigned ldsdst) {
  unsigned keep;
  asm volatile("s_mov_b32 %0, m0\n\ts_mov_b32 m0, %3\n\ts_nop 0\n\tbuffer_load_dwordx4 %1, %2, 0 offen lds\n\ts_mov_b32 m0, %0"
               : "=&s"(keep) : "v"(voff), "s"(r), "s"(ldsdst) : "memory");
}
template <int N>
__device__ __forceinline__ void gatherm(u32x4 (&a)[N], int (&off)[N], unsigned tag, __amdgpu_buffer_rsrc_t r) {
#pragma nounroll
  for (unsigned n = 0; n < SPIN_CAP; ++n) {
    MEM_FENCE();
    u32x4 v[N];
#pragma unroll
    for (int i = 0; i < N; ++i) v[i] = LD128(r, off[i], AUX_SC1);
    bool all = true;
#pragma unroll
    for (int i = 0; i < N; ++i) {
      const bool mine = off[i] != OOB, ok = v[i][1] == tag && v[i][3] == tag;
      a[i][0] = mine ? v[i][0] : a[i][0];
      a[i][2] = mine ? v[i][2] : a[i][2];
      off[i] = ok ? OOB : off[i];
      all = all && (ok || !mine);
    }
    if (__all(all)) break;
  }
}
template <int STEP>
__device__ __forceinline__ void pass4(u32x4 (&a)[4], const u64 (&m)[4], int off, __amdgpu_buffer_rsrc_t r) {
  asm volatile(
      "s_mov_b64 exec, %[m0]\n\tbuffer_load_dwordx4 %[a0], %[o], %[r], 0 offen sc1\n\t"
      "s_mov_b64 exec, %[m1]\n\tbuffer_load_dwordx4 %[a1], %[o], %[r], 0 offen offset:%c[s1] sc1\n\t"
      "s_mov_b64 exec, %[m2]\n\tbuffer_load_dwordx4 %[a2], %[o], %[r], 0 offen offset:%c[s2] sc1\n\t"
      "s_mov_b64 exec, %[m3]\n\tbuffer_load_dwordx4 %[a3], %[o], %[r], 0 offen offset:%c[s3] sc1\n\t"
      "s_mov_b64 exec, -1\n\ts_waitcnt vmcnt(0)"
      : [a0] "+v"(a[0]), [a1] "+v"(a[1]), [a2] "+v"(a[2]), [a3] "+v"(a[3])
      : [m0] "s"(m[0]), [m1] "s"(m[1]), [m2] "s"(m[2]), [m3] "s"(m[3]), [o] "v"(off), [r] "s"(r), [s1] "i"(STEP), [s2] "i"(2 * STEP), [s3] "i"(3 * STEP)
      : "memory");
}
__device__ __forceinline__ bool missing4(const u32x4 (&a)[4], u64 (&m)[4], unsigned tag) {
#pragma unroll
  for (int i = 0; i < 4; ++i) m[i] &= __builtin_amdgcn_ballot_w64(a[i][1] != tag) | __builtin_amdgcn_ballot_w64(a[i][3] != tag);
  return (m[0] | m[1] | m[2] | m[3]) != 0ull;
}
struct Sel { unsigned lo[4], hi[4]; };
__device__ __forceinline__ Sel make_sel(int lane) {
  Sel s;
  const int j = lane & 7;
#pragma unroll
  for (int t = 0; t < 4; ++t) {
    const bool on = (j >> 1) == t;
    s.lo[t] = on ? ((j & 1) ? 0x01000c0cu : 0x0c0c0100u) : 0x0c0c0c0cu;
    s.hi[t] = on ? ((j & 1) ? 0x03020c0cu : 0x0c0c0302u) : 0x0c0c0c0cu;
  }
  return s;
}
__device__ __forceinline__ u32x4 place(unsigned src, const unsigned (&sel)[4]) {
  u32x4 r;
#pragma unroll
  for (int t = 0; t < 4; ++t) r[t] = __builtin_amdgcn_perm(src, src, sel[t]);
  return r;
}

#define KA0 "s36"
#define KA1 "s37"
#define KA6 "s42"
#define KA7 "s43"
#define KB23 "s[54:55]"
#define RT "s[56:59]"
#define RT01 "s[56:57]"
#define RT0 "s56"
#define RT1 "s57"
#define RT2 "s58"
#define RT3 "s59"
__global__ void __launch_bounds__(1024, 4) fused_kernel(const void*, const void*, const void*, const void*, const void*, const void*, const void*,
                                                     const void*, void*, void*) {
  __shared__ __attribute__((aligned(16))) unsigned char smem[L_END];
  LAS unsigned char* L = (LAS unsigned char*)smem;
  volatile LAS unsigned* flag = (volatile LAS unsigned*)(L + L_FLAG);
  const int bid = blockIdx.x, tid = threadIdx.x, lane = tid & 63;
  u32x16 ka;
  u32x4 kb, rt;
  unsigned epochv = 0, wave, t0, t1, t2, v0, v1;
  asm volatile(
      "8:\n\ts_load_dwordx16 %[ka], %[kp], 0x0\n\ts_load_dwordx4 %[kb], %[kp], 0x40\n\t"
      "v_readfirstlane_b32 %[w], %[tid]\n\ts_lshr_b32 %[w], %[w], 6\n\t"
      "s_cmp_lt_u32 %[w], 8\n\ts_cbranch_scc1 1f\n\t"
      "s_add_u32 %[t0], %[bid], 1\n\ts_and_b32 %[t0], %[t0], 3\n\t"
      "s_lshl_b32 %[t2], %[w], 11\n\ts_add_u32 m0, %[t2], %[lx]\n\t"
      "s_cmp_eq_u32 %[w], 8\n\ts_cbranch_scc1 5f\n\t"
      "v_lshrrev_b32 %[v0], 3, %[lane]\n\tv_and_b32 %[v1], 7, %[lane]\n\tv_lshlrev_b32 %[v0], 13, %[v0]\n\tv_lshl_or_b32 %[v0], %[v1], 4, %[v0]\n\t"
      "s_mul_i32 %[t0], %[t0], 0xe0000\n\ts_lshl_b32 %[t1], %[w], 17\n\ts_add_u32 %[t0], %[t0], %[t1]\n\t"
      "s_lshl_b32 %[t1], %[bid], 4\n\ts_and_b32 %[t1], %[t1], 0xf80\n\ts_add_u32 %[t0], %[t0], %[t1]\n\t"
      "s_lshl_b32 %[t1], %[bid], 10\n\ts_and_b32 %[t1], %[t1], 0x1000\n\ts_add_u32 %[t0], %[t0], %[t1]\n\t"
      "s_sub_u32 %[t0], %[t0], 0x120000\n\ts_add_u32 %[t1], %[t0], 0x10000\n\t"
      "s_mov_b32 " RT2 ", 0x380000\n\ts_mov_b32 " RT3 ", 0x20000\n\t"
      "s_waitcnt lgkmcnt(0)\n\ts_mov_b32 " RT0 ", " KA6 "\n\ts_and_b32 " RT1 ", " KA7 ", 0xffff\n\t"
      "s_branch 6f\n"
      "5:\n\t"
      "v_lshrrev_b32 %[v0], 4, %[lane]\n\tv_and_b32 %[v1], 15, %[lane]\n\tv_min_u32 %[v1], 13, %[v1]\n\tv_mul_u32_u24 %[v0], 0x380, %[v0]\n\tv_lshl_add_u32 %[v0], %[v1], 4, %[v0]\n\t"
      "s_mul_i32 %[t0], %[t0], 0xe0\n\tv_add_u32 %[v0], %[t0], %[v0]\n\ts_mov_b32 %[t0], 0\n\ts_movk_i32 %[t1], 0xe00\n\t"
      "s_movk_i32 " RT2 ", 0x1c00\n\ts_mov_b32 " RT3 ", 0x20000\n\t"
      "s_waitcnt lgkmcnt(0)\n\ts_mov_b32 " RT0 ", " KA0 "\n\ts_and_b32 " RT1 ", " KA1 ", 0xffff\n"
      "6:\n\t"
      "buffer_load_dwordx4 %[v0], " RT ", %[t0] offen nt lds\n\t"
      "s_add_u32 m0, m0, 0x400\n\ts_nop 0\n\tbuffer_load_dwordx4 %[v0], " RT ", %[t1] offen nt lds\n\t"
      "s_barrier\n\ts_branch 4f\n"
      "1:\n\ts_cmp_eq_u32 %[w], 0\n\ts_cbranch_scc1 2f\n\t"
      "s_cmp_lt_u32 %[w], 5\n\ts_cbranch_scc1 4f\n\t"
      "s_cmp_eq_u32 %[w], 7\n\ts_cbranch_scc1 3f\n\t"
      "s_cmp_lt_u32 %[bid], 192\n\ts_cbranch_scc1 4f\n\t"
      "s_waitcnt lgkmcnt(0)\n\ts_barrier\n\ts_branch 4f\n"
      "3:\n\t"
      "v_lshlrev_b32 %[v0], 2, %[lane]\n\tds_write_b32 %[v0], %[e]\n\t"
      "s_getpc_b64 " RT01 "\n7:\n\ts_sub_u32 " RT0 ", " RT0 ", 7b-8b\n\ts_subb_u32 " RT1 ", " RT1 ", 0\n\ts_and_b32 " RT0 ", " RT0 ", 0xffffff00\n\ts_and_b32 " RT1 ", " RT1 ", 0xffff\n\t"
      "s_mov_b32 " RT2 ", %[cl]\n\ts_mov_b32 " RT3 ", 0x20000\n\ts_mov_b32 m0, %[lc]\n\t"
      "s_lshr_b32 %[t0], %[bid], 3\n\ts_lshl_b32 %[t0], %[t0], 10\n\tv_lshlrev_b32 %[v0], 4, %[lane]\n\tv_add_u32 %[v0], %[t0], %[v0]\n\t"
      "buffer_load_dwordx4 %[v0], " RT ", 0 offen lds\n\t"
      "s_waitcnt lgkmcnt(0)\n\ts_barrier\n\ts_branch 4f\n"
      "2:\n\ts_waitcnt lgkmcnt(0)\n\tglobal_load_dword %[e], %[e], " KB23 " sc1\n\ts_barrier\n\ts_waitcnt vmcnt(0)\n"
      "4:"
      : [ka] "={s[36:51]}"(ka), [kb] "={s[52:55]}"(kb), [rt] "={s[56:59]}"(rt), [w] "=&s"(wave), [t0] "=&s"(t0), [t1] "=&s"(t1), [t2] "=&s"(t2),
        [v0] "=&v"(v0), [v1] "=&v"(v1), [e] "+v"(epochv)
      : [kp] "{s[0:1]}"(__builtin_amdgcn_kernarg_segment_ptr()), [bid] "{s2}"(bid), [tid] "v"(tid), [lane] "v"(lane), [lx] "i"(L_X - 8 * 2048),
        [lc] "i"(L_WO), [cl] "i"(CODE_BYTES)
      : "memory", "scc");
#define ARG(v, i) ((u64)v[2 * (i)] | ((u64)v[2 * (i) + 1] << 32))
  const int xc = bid & 7, jj = bid >> 3, kvh = jj & 7, s = jj >> 3, b = (xc - 2 * s) & 7;
  const int nkw = s == 3 ? 4 : 6;
  unsigned tag = 0;

  if (__builtin_expect(wave - 1u < (unsigned)nkw, 1)) {
    const bool four = s == 3 && wave >= 3;
    const int urel = four && wave == 4 ? 16 : (wave - 1) * 6;
    const unsigned hsh = (((unsigned)bid * 8u + wave) * 2654435761u) >> 24;
    unsigned ord = (unsigned)((four ? 0x3210321032ull : 0x543210543210ull) >> (((hsh * (four ? 4u : 6u)) >> 8) * 4u));
    int klo = (lane & 7) * 512 + ((lane >> 3) & 1) * 64 + (lane >> 4) * 16 + (s * 36 + urel) * 4096;
    unsigned kvoff = (unsigned)(((b * HKV + kvh) * SK + (SK - WIN)) * DH * 2);
    unsigned brot = (wave + jj + xc) * 128u;
    u32x16 kak = ka;
    u32x4 kbk = kb;
    asm volatile("s_waitcnt lgkmcnt(0)\n\ts_barrier" : "+{s[36:51]}"(kak), "+{s[52:55]}"(kbk), "+v"(klo), "+s"(kvoff), "+s"(ord), "+s"(brot)::"memory");
    const auto rk = RSRC(ARG(kak, 1) + kvoff, WIN * DH * 2);
    u32x4 r[24];
#pragma unroll
    for (int i = 0; i < 24; ++i) {
      if (i >= WK0) vmwait<WK - 1>();
      r[i] = __builtin_amdgcn_raw_buffer_load_b128(rk, i < 16 || !four ? klo : OOB, ((ord >> (i & ~3)) & 15u) * 4096 + ((brot + i * 128) & 0x180), AUX_NT);
      SCHED_FENCE();
      if (i == KTRIG) lds_inc(L, F_KISS, lane);
    }
    const u64 ws = ARG(kbk, 1);
    const auto rws = RSRC(ws, WS_END);
    const int kw = wave - 1, nu = four ? 4 : 6, ulo = s * 36, uhi = s == 3 ? 128 : ulo + 36, u0 = ulo + urel;
    __builtin_amdgcn_s_setprio(2);
    lds_wait_ge(flag + F_QRDY, 1u);
    tag = __builtin_amdgcn_readfirstlane(flag[F_TAG]);
    u32x4 qa[4];
#pragma unroll
    for (int t = 0; t < 4; ++t) qa[t] = *(const LAS u32x4*)(L + L_Q + (lane & 1) * 512 + ((brot + t * 128) & 0x180) + ((lane >> 1) & 1) * 64 + (lane >> 4) * 16);
    float s0 = 0.f, s1 = 0.f;
#pragma unroll
    for (int u = 0; u < 6; ++u) {
      f32x4 c = {0, 0, 0, 0};
#pragma unroll
      for (int t = 0; t < 4; ++t) c = mfma(qa[t], r[4 * u + t], c);
      float c0 = (lane & 8) ? c[2] : c[0], c1 = (lane & 8) ? c[3] : c[1];
      c0 += dpp<ROR8>(c0); c1 += dpp<ROR8>(c1);
      if ((unsigned)(lane >> 3) == ((ord >> (4 * u)) & 15u) && (u < 4 || !four)) { s0 = c0; s1 = c1; }
    }
    const unsigned pk = pk_bf16(s0, s1);
    const unsigned pa = bperm(pk, (2 * lane) & 63), pb = bperm(pk, (2 * lane + 1) & 63);
    if (lane < 4 * nu) {
      ST128((u32x4{pa, tag, pb, tag}), rws, WS_S + (b * 8 + kvh) * 8192 + u0 * 64 + lane * 16, AUX_SC1);
      *(LAS u32x4*)(L + L_SM + urel * 64 + lane * 16) = u32x4{pa, tag, pb, tag};
    }
    MEM_FENCE();
    lds_inc(L, F_KCNT, lane);
    if (wave <= 4) {
      u32x4 sg[2];
      int of[2];
      bool own[2];
#pragma unroll
      for (int i = 0; i < 2; ++i) {
        const int myu = (kw + 4 * i) * 16 + (lane >> 2);
        own[i] = myu >= ulo && myu < uhi;
        of[i] = own[i] ? OOB : WS_S + (b * 8 + kvh) * 8192 + (kw + 4 * i) * 1024 + lane * 16;
      }
      __builtin_amdgcn_s_sleep(SSLEEP);
      gatherm<2>(sg, of, tag, rws);
      lds_wait_ge(flag + F_KCNT, (unsigned)nkw);
#pragma unroll
      for (int i = 0; i < 2; ++i) {
        const int c = kw + 4 * i;
        const u32x4 mv = *(const LAS u32x4*)(L + L_SM + (own[i] ? (c * 16 - ulo) * 64 + lane * 16 : 0));
        const unsigned sa = own[i] ? mv[0] : sg[i][0], sb = own[i] ? mv[2] : sg[i][2];
        const float m0 = wave_max(fmaxf(bf_lo(sa), bf_lo(sb))), m1 = wave_max(fmaxf(bf_hi(sa), bf_hi(sb)));
        const float n0 = m0 == -INFINITY ? 0.f : m0, n1 = m1 == -INFINITY ? 0.f : m1;
        const float ea = __builtin_amdgcn_exp2f((bf_lo(sa) - n0) * C2), eb = __builtin_amdgcn_exp2f((bf_lo(sb) - n0) * C2);
        const float ec = __builtin_amdgcn_exp2f((bf_hi(sa) - n1) * C2), ed = __builtin_amdgcn_exp2f((bf_hi(sb) - n1) * C2);
        *(LAS unsigned*)(L + L_P + c * 512 + lane * 4) = pk_bf16(ea, eb);
        *(LAS unsigned*)(L + L_P + c * 512 + 256 + lane * 4) = pk_bf16(ec, ed);
        const float l0 = wave_sum(ea + eb), l1 = wave_sum(ec + ed);
        if (lane == 0) *(LAS f32x4*)(L + L_ST + c * 16) = f32x4{m0, l0, m1, l1};
      }
      MEM_FENCE();
      lds_inc(L, F_PR + kw, lane);
      lds_inc(L, F_SCNT, lane);
    }
    return;
  }
  const GAS u16* x = (const GAS u16*)ARG(ka, 0);
  const u64 vc = ARG(ka, 2), wo = ARG(ka, 4), w1 = ARG(ka, 5), w2 = ARG(ka, 6), ws = ARG(kb, 1);
  const GAS u16* gw = (const GAS u16*)ARG(ka, 7);
  GAS u16* out = (GAS u16*)ARG(kb, 0);
  const auto rws = RSRC(ws, WS_END);
  const int cb = jj + 32 * (xc >> 2), rb = (xc + 1) & 3;
  const int grp = kvh * 4 + s;
  if (wave >= 8) {
    const unsigned kvoff = (unsigned)(((b * HKV + kvh) * SK + (SK - WIN)) * DH * 2);
    const int w = wave - 8;
    vmwait<0>();
    lds_inc(L, F_WQC, lane);
    tag = get_tag(flag);
    if (w >= 4) {
      __builtin_amdgcn_s_setprio(3);
      const int ct = w - 4;
      const int xrow = ((lane & 7) >> 1) + 4 * (lane & 1);
      LAS unsigned char* xp = L + L_X + xrow * 256 + (lane >> 4) * 16;
      LAS unsigned char* xp2 = L + L_X + xrow * 256 + 192 + (lane >> 4) * 8;
      LAS unsigned char* tb = L + L_WQ + ((lane >> 4) * 8 + ((lane >> 2) & 3)) * 128 + (lane & 3) * 8 + ct * 32;
      LAS unsigned char* tb2 = L + L_WQ + (96 + (lane >> 4) * 4 + ((lane >> 2) & 3)) * 128 + (lane & 3) * 8 + ct * 32;
      const int dest = ((lane >> 4) & 1) * 2 + (lane >> 5);
      const int qo = WS_QP + (cb * 4 + dest) * QPSTR + rb * 1024 + ct * 256 + (lane & 15) * 16;
      lds_wait_ge(flag + F_WQC, 8u);
      const u32x2 pt = trd(tb2), xb = *(const LAS u32x2*)xp2;
      f32x4 acc = mfma(u32x4{xb[0], xb[1], 0u, 0u}, u32x4{pt[0], pt[1], 0u, 0u}, f32x4{0, 0, 0, 0});
#pragma unroll
      for (int ks = 0; ks < 3; ++ks) {
        const u32x2 p0 = trd(tb + ks * 4096), p1 = trd(tb + ks * 4096 + 512);
        acc = mfma(*(const LAS u32x4*)(xp + ks * 64), u32x4{p0[0], p0[1], p1[0], p1[1]}, acc);
      }
      const float q0 = lane >= 32 ? acc[2] : acc[0], q1 = lane >= 32 ? acc[3] : acc[1];
      if (dest != rb) ST128((u32x4{__float_as_uint(q0), tag, __float_as_uint(q1), tag}), rws, qo, AUX_SC1);
      else *(LAS f32x2*)(L + L_QO + (ct * 16 + (lane & 15)) * 8) = f32x2{q0, q1};
      LGKM0();
      lds_inc(L, F_QPC, lane);
      __builtin_amdgcn_s_setprio(0);
    }
    const auto rv = RSRC(vc + kvoff, WIN * DH * 2);
    u32x4 r[16];
    const int trot = w + jj + xc;
    const int voff = (w * 128 + (lane >> 4) * 8 + ((lane >> 3) & 1)) * 512 + s * 128 + (lane & 7) * 16;
    const Sel sel = make_sel(lane);
    lds_wait_ge(flag + F_KISS, (unsigned)nkw);
#define V_ISSUE(i) do { if ((i) >= WV) vmwait<WV - 1>(); \
      r[i] = __builtin_amdgcn_raw_buffer_load_b128(rv, voff + ((i) & 3) * 1024, ((((i) >> 2) + trot) & 3) * 16384, AUX_NT); SCHED_FENCE(); \
      if ((i) == 15) lds_inc(L, F_VISS, lane); } while (0)
#define V_USE(i) do { a0 = mfma(place(pp[(i) >> 2][(i) & 3], sel.lo), r[i], a0); a1 = mfma(place(pp[(i) >> 2][(i) & 3], sel.hi), r[i], a1); } while (0)
#pragma unroll
    for (int i = 0; i <= VSPLIT; ++i) V_ISSUE(i);
    u32x4 pp[4];
    f32x4 a0 = {0, 0, 0, 0}, a1 = {0, 0, 0, 0};
#define P_READ() _Pragma("unroll") for (int t = 0; t < 4; ++t) \
      pp[t] = *(const LAS u32x4*)(L + L_P + w * 512 + ((lane >> 3) & 1) * 256 + (((t + trot) & 3) * 32 + (lane >> 4) * 8) * 2)
    float f;
    unsigned wad = (unsigned)(uintptr_t)(L + L_OP) + w * 512 + ((lane >> 5) * 64 + (lane & 7) * 8 + ((lane >> 4) & 1) * 4) * 4;
    unsigned rad = (unsigned)(uintptr_t)(L + L_OP) + (lane >> 5) * 2048 + (lane & 31) * 16;
    int oad = WS_OB + (grp * 8 + b) * 512 + lane * 16;
#define F_GET() do { lds_wait_ge(flag + F_FRDY, 1u); f = *(const LAS float*)(L + L_FW + w * 8 + (lane >> 5) * 4); \
      asm volatile("" : "+v"(f), "+v"(wad), "+v"(rad), "+v"(oad)); } while (0)
    if (__builtin_expect(__builtin_amdgcn_readfirstlane(flag[F_PR + (w & 3)]) != 0u, 1)) {
      MEM_FENCE();
      P_READ();
#pragma unroll
      for (int i = VSPLIT + 1; i < 16; ++i) {
        V_ISSUE(i);
#pragma unroll
        for (int k = 0; k < 16; ++k)
          if (k >= (i - VSPLIT - 1) * VPER && k < (i - VSPLIT) * VPER && k <= i - WV) V_USE(k);
        SCHED_FENCE();
      }
#pragma unroll
      for (int k = 0; k < 16 - WV; ++k) {
        bool done = false;
#pragma unroll
        for (int i = VSPLIT + 1; i < 16; ++i) done = done || (k >= (i - VSPLIT - 1) * VPER && k < (i - VSPLIT) * VPER && k <= i - WV);
        if (!done) { V_USE(k); if (k & 1) SCHED_FENCE(); }
      }
      SCHED_FENCE();
      F_GET();
      u32x4 la[WV], lb[WV];
#pragma unroll
      for (int k = 0; k < WV; ++k) {
        la[k] = place(pp[(16 - WV + k) >> 2][(16 - WV + k) & 3], sel.lo);
        lb[k] = place(pp[(16 - WV + k) >> 2][(16 - WV + k) & 3], sel.hi);
        asm volatile("" : "+v"(la[k]), "+v"(lb[k]));
      }
      SCHED_FENCE();
#pragma unroll
      for (int k = 0; k < WV; ++k) { a0 = mfma(la[k], r[16 - WV + k], a0); a1 = mfma(lb[k], r[16 - WV + k], a1); }
    } else {
#pragma unroll
      for (int i = VSPLIT + 1; i < 16; ++i) V_ISSUE(i);
      lds_wait_ge(flag + F_PR + (w & 3), 1u);
      P_READ();
#pragma unroll
      for (int k = 0; k < 16; ++k) { V_USE(k); if (k & 1) SCHED_FENCE(); }
      F_GET();
    }
    f32x4 t = (lane & 8) ? a1 : a0;
#pragma unroll
    for (int k = 0; k < 4; ++k) t[k] = (t[k] + dpp<ROR8>(t[k])) * f;
    unsigned old;
    f32x4 o0v, o1v, o2v, o3v;
    asm volatile("ds_write_b128 %[wa], %[t]\n\tds_add_rtn_u32 %[old], %[ca], %[one]\n\t"
                 "ds_read_b128 %[p0], %[ra]\n\tds_read_b128 %[p1], %[ra] offset:512\n\tds_read_b128 %[p2], %[ra] offset:1024\n\tds_read_b128 %[p3], %[ra] offset:1536\n\t"
                 "s_waitcnt lgkmcnt(0)"
                 : [old] "=&v"(old), [p0] "=&v"(o0v), [p1] "=&v"(o1v), [p2] "=&v"(o2v), [p3] "=&v"(o3v)
                 : [wa] "v"(wad), [t] "v"(t), [ca] "v"((unsigned)(uintptr_t)(L + (lane == 0 ? F_OCNT * 4 : 128 + (lane & 31) * 4))), [one] "v"(1u), [ra] "v"(rad)
                 : "memory");
    if (__builtin_amdgcn_readfirstlane(old) == 7u) {
      f32x4 ov = (o0v + o1v) + (o2v + o3v);
#pragma unroll
      for (int k = 0; k < 4; ++k) { float hi, lo = x32_a(ov[k], hi); ov[k] = lo + hi; }
      if (lane < 32) {
        const u32x4 ogr = {pk_bf16(ov[0], ov[1]), tag, pk_bf16(ov[2], ov[3]), tag};
        ST128(ogr, rws, oad, AUX_SC1);
        *(LAS u32x4*)(L + L_OM + lane * 16) = ogr;
      }
      MEM_FENCE();
      lds_inc(L, F_OMR, lane);
    }
  } else if (wave == 0) {
    __builtin_amdgcn_s_setprio(3);
    tag = __builtin_amdgcn_readfirstlane(epochv) * 1664525u + 1013904223u;
    if (lane == 0) *(volatile LAS u32x2*)flag = u32x2{1u, tag};
    lds_wait_ge(flag + F_QPC, 4u);
    __builtin_amdgcn_s_sleep(QSLEEP);
    {
      const int po = WS_QP + (cb * 4 + rb) * QPSTR + lane * 16;
      u32x4 pg[3];
      int of[3] = {po + ((rb + 1) & 3) * 1024, po + ((rb + 2) & 3) * 1024, po + ((rb + 3) & 3) * 1024};
      gatherm<3>(pg, of, tag, rws);
      const f32x2 own = *(const LAS f32x2*)(L + L_QO + lane * 8);
      const float a0 = (own[0] + __uint_as_float(pg[0][0])) + (__uint_as_float(pg[1][0]) + __uint_as_float(pg[2][0]));
      const float a1 = (own[1] + __uint_as_float(pg[0][2])) + (__uint_as_float(pg[1][2]) + __uint_as_float(pg[2][2]));
      const float n0 = dpp<XOR1>(a0), n1 = dpp<XOR1>(a1);
      const unsigned pk = (lane & 1) ? pk_bf16(n1, a1) : pk_bf16(a0, n0);
      const int src = (lane & 15) * 4 + ((lane >> 4) & 1);
      const unsigned pa = bperm(pk, src), pb = bperm(pk, src + 2);
      if (lane < 32) ST128((u32x4{pa, tag, pb, tag}), rws, WS_Q + ((cb >> 3) * 8 + rb + 4 * (lane >> 4)) * QSTR + (cb & 7) * 256 + (lane & 15) * 16, AUX_SC1);
    }
    __builtin_amdgcn_s_sleep(QSLEEP);
    {
      const int qoff = WS_Q + (kvh * 8 + b) * QSTR + lane * 16;
      u32x4 qq[2];
      int of[2] = {qoff, qoff + 1024};
      gatherm<2>(qq, of, tag, rws);
      *(LAS u32x2*)(L + L_Q + lane * 8) = u32x2{qq[0][0], qq[0][2]};
      *(LAS u32x2*)(L + L_Q + 512 + lane * 8) = u32x2{qq[1][0], qq[1][2]};
      LGKM0();
      if (lane == 0) flag[F_QRDY] = 1u;
    }
    u32x4 xg = {0, 0, 0, 0}, gg = {0, 0, 0, 0};
    if (lane < 56) {
      xg = *(const GAS u32x4*)(x + b * DM + lane * 8);
      gg = *(const GAS u32x4*)(gw + lane * 8);
    }
    float z = 0.f;
#pragma unroll
    for (int t = 0; t < 4; ++t) z += rbf(bf_lo(xg[t]) * bf_lo(gg[t])) + rbf(bf_hi(xg[t]) * bf_hi(gg[t]));
    z = rbf(wave_sum(z));
    const float g = rbf(1.f / (1.f + __expf(-z)));
    const unsigned g1u = __float_as_uint(rbf(1.f - g));
    if (s == 0 && lane < 8) ST128((u32x4{g1u, tag, g1u, tag}), rws, WS_G + (kvh * 8 + b) * 128 + lane * 16, AUX_SC1);
    lds_wait_ge(flag + F_SCNT, 4u);
    {
      const f32x2 ml = *(const LAS f32x2*)(L + L_ST + (lane & 15) * 8);
      float M = fmaxf(ml[0], dpp<XOR2>(ml[0]));
      M = fmaxf(M, dpp<0x124>(M)); M = fmaxf(M, dpp<ROR8>(M));
      const float al = __builtin_amdgcn_exp2f((ml[0] - M) * C2);
      float Ls = al * ml[1];
      Ls += dpp<XOR2>(Ls); Ls += dpp<0x124>(Ls); Ls += dpp<ROR8>(Ls);
      if (lane < 16) *(LAS float*)(L + L_FW + lane * 4) = al * (g / Ls);
      LGKM0();
      if (lane == 0) flag[F_FRDY] = 1u;
    }
  } else if (wave != 7 && xc < 7) {
    const int j = wave - 5, t = xc * 8 + kvh;
    const unsigned lb = (unsigned)(uintptr_t)(L + L_T) + j * 14336;
    const auto r1 = RSRC(w1, DM * FF * 2);
    const int o = ((j * 224 + (lane >> 2)) * FF + t * 32 + (lane & 3) * 8) * 2;
#pragma unroll
    for (int i = 0; i < 14; ++i) {
      if (i >= WT) vmwait<WT - 1>();
      dma16(r1, o + i * 16 * FF * 2, lb + i * 1024);
    }
    {
      const auto rx = RSRC((u64)x, NB * DM * 2);
#pragma unroll
      for (int i = 0; i < 4; ++i) {
        vmwait<WT - 1>();
        dma16p(rx, (j * 4 + i) * 1024 + lane * 16, (unsigned)(uintptr_t)(L + L_XA) + (j * 4 + i) * 1024);
      }
    }
    tag = get_tag(flag);
    vmwait<0>();
    lds_inc(L, F_TCNT, lane);
    lds_wait_ge(flag + F_TCNT, 2u);
    LAS unsigned char* tb = L + L_T + ((lane >> 4) * 8 + ((lane >> 2) & 3)) * 64 + j * 32 + (lane & 3) * 8;
    f32x4 acc0 = {0, 0, 0, 0}, acc1 = {0, 0, 0, 0};
#pragma unroll
    for (int ks = 0; ks < 14; ++ks) {
      const u32x2 p0 = trd(tb + ks * 2048), p1 = trd(tb + ks * 2048 + 256);
      const u32x4 xa = *(const LAS u32x4*)(L + L_XA + (lane & 7) * (DM * 2) + ks * 64 + (lane >> 4) * 16);
      if (ks & 1) acc1 = mfma(u32x4{p0[0], p0[1], p1[0], p1[1]}, xa, acc1);
      else acc0 = mfma(u32x4{p0[0], p0[1], p1[0], p1[1]}, xa, acc0);
    }
    acc0 += acc1;
    float hv[4];
#pragma unroll
    for (int k = 0; k < 4; ++k) {
      const float v = rbf(acc0[k]);
      hv[k] = v / (1.f + __expf(-v));
    }
    if (!(lane & 8))
      ST128((u32x4{pk_bf16(hv[0], hv[1]), tag, pk_bf16(hv[2], hv[3]), tag}), rws, WS_HB + t * 1024 + j * 512 + (lane >> 4) * 128 + (lane & 7) * 16, AUX_SC1);
  }
  const int cpair = (xc ^ 1) * 32 + jj;
  if (wave == 7 && b < 7) {
    const int fgrp = s < 3 ? kvh * 3 + s : (kvh < 4 ? 24 + kvh : -1);
    tag = get_tag(flag);
    const bool hi8 = lane & 8;
    u32x4 hb[3];
    {
      const unsigned lb = (unsigned)(uintptr_t)(L + L_WO);
      const auto r2 = RSRC(w2, FF * DM * 2);
      const auto ro = RSRC(wo, NQ * DM * 2);
      const int off2 = (fgrp * 64 + (lane >> 3)) * 896 + b * 128 + (lane & 7) * 16;
      const int offo = (kvh * 512 + s * 64 + (lane >> 3)) * 896 + b * 128 + (lane & 7) * 16;
      lds_wait_ge(flag + F_VISS, 8u);
      if (fgrp >= 0) {
#pragma unroll
        for (int i = 0; i < 8; ++i) dma16(r2, off2 + i * 8 * 896, lb + 16384 + i * 1024);
      }
#pragma unroll
      for (int i = 0; i < 16; ++i) dma16(ro, offo + ((i >> 3) * 256 + (i & 7) * 8) * 896, lb + i * 1024);
      if (fgrp >= 0) {
        int of[3] = {WS_HB + fgrp * 2048 + lane * 16, WS_HB + fgrp * 2048 + 1024 + lane * 16, WS_G + (kvh * 8 + (lane & 7)) * 128};
        gatherm<3>(hb, of, tag, rws);
      }
      vmwait<0>();
    }
    __builtin_amdgcn_s_setprio(3);
    const bool ownb = (lane & 7) == b;
    const int ooff = WS_OB + grp * 4096 + (lane & 7) * 512 + (lane >> 3) * 16;
    u32x4 ob[4];
#pragma unroll
    for (int i = 0; i < 4; ++i) ob[i] = LD128(rws, ownb ? OOB : ooff + i * 128, AUX_SC1);
    SCHED_FENCE();
    LAS unsigned char* tb = L + L_WO + ((lane >> 4) * 8 + ((lane >> 2) & 3)) * 128 + (lane & 3) * 8;
    f32x4 acc[4] = {{0, 0, 0, 0}, {0, 0, 0, 0}, {0, 0, 0, 0}, {0, 0, 0, 0}};
    if (fgrp >= 0) {
      const u32x4 hv = {hb[0][0], hb[0][2], hb[1][0], hb[1][2]};
      const float g1 = __uint_as_float(hb[2][0]);
#pragma unroll
      for (int ks = 0; ks < 2; ++ks) {
        const u32x4 bf = {hi8 ? 0u : hv[2 * ks], hi8 ? 0u : hv[2 * ks + 1], hi8 ? hv[2 * ks] : 0u, hi8 ? hv[2 * ks + 1] : 0u};
#pragma unroll
        for (int ct = 0; ct < 4; ++ct) {
          const u32x2 p0 = trd(tb + 16384 + ks * 4096 + ct * 32), p1 = trd(tb + 16384 + ks * 4096 + 512 + ct * 32);
          acc[ct] = mfma(u32x4{p0[0], p0[1], p1[0], p1[1]}, bf, acc[ct]);
        }
      }
#pragma unroll
      for (int ct = 0; ct < 4; ++ct) acc[ct] *= g1;
    }
    u32x4 wf[4][4];
#pragma unroll
    for (int ks = 0; ks < 4; ++ks)
#pragma unroll
      for (int ct = 0; ct < 4; ++ct) {
        const u32x2 p0 = trd(tb + ks * 4096 + ct * 32), p1 = trd(tb + ks * 4096 + 512 + ct * 32);
        wf[ks][ct] = u32x4{p0[0], p0[1], p1[0], p1[1]};
      }
    int po = WS_PO + (b * 32 + (lane >> 3)) * PSTR + grp * 128 + (lane & 7) * 16;
    asm volatile("" : "+v"(po));
    lds_wait_ge(flag + F_OMR, 1u);
#pragma unroll
    for (int i = 0; i < 4; ++i) {
      const u32x4 v = *(const LAS u32x4*)(L + L_OM + i * 128 + (lane >> 3) * 16);
      ob[i] = ownb ? v : ob[i];
    }
    u64 ms[4] = {~0ull, ~0ull, ~0ull, ~0ull};
    if (missing4(ob, ms, tag)) {
#pragma nounroll
      for (unsigned n = 0; n < SPIN_CAP; ++n) {
        pass4<128>(ob, ms, ooff, rws);
        if (!missing4(ob, ms, tag)) break;
      }
    }
    u32x4 bf[4];
#pragma unroll
    for (int ks = 0; ks < 4; ++ks) bf[ks] = u32x4{hi8 ? 0u : ob[ks][0], hi8 ? 0u : ob[ks][2], hi8 ? ob[ks][0] : 0u, hi8 ? ob[ks][2] : 0u};
#pragma unroll
    for (int ct = 0; ct < 4; ++ct)
#pragma unroll
      for (int ks = 0; ks < 4; ++ks) acc[ct] = mfma(wf[ks][ct], bf[ks], acc[ct]);
    {
#pragma unroll
      for (int ct = 0; ct < 4; ++ct) {
        const float k0 = hi8 ? acc[ct][2] : acc[ct][0], k1 = hi8 ? acc[ct][3] : acc[ct][1];
        const float s0 = hi8 ? acc[ct][0] : acc[ct][2], s1 = hi8 ? acc[ct][1] : acc[ct][3];
        const u32x4 pv = {__float_as_uint(k0 + dpp<ROR8>(s0)), tag, __float_as_uint(k1 + dpp<ROR8>(s1)), tag};
        ST128(pv, rws, po + ct * 8 * PSTR, AUX_SC1);
      }
    }
  } else if (wave == 0 && cpair < 224) {
    __builtin_amdgcn_s_setprio(3);
    lds_wait_ge(flag + F_OCNT, 8u);
    __builtin_amdgcn_s_sleep(FSLEEP);
    const int poff = WS_PO + cpair * PSTR + lane * 16;
    u32x4 pg[4] = {{0, 0, 0, 0}, {0, 0, 0, 0}, {0, 0, 0, 0}, {0, 0, 0, 0}};
    u64 ms[4] = {~0ull, ~0ull, ~0ull, ~0ull};
    GAS unsigned* op = (GAS unsigned*)(out + (lane & 7) * DM + cpair * 2);
    asm volatile("" : "+v"(op));
#pragma nounroll
    for (unsigned n = 0; n < SPIN_CAP; ++n) {
      pass4<1024>(pg, ms, poff, rws);
      if (!missing4(pg, ms, tag)) {
        float a0 = (__uint_as_float(pg[0][0]) + __uint_as_float(pg[1][0])) + (__uint_as_float(pg[2][0]) + __uint_as_float(pg[3][0]));
        float a1 = (__uint_as_float(pg[0][2]) + __uint_as_float(pg[1][2])) + (__uint_as_float(pg[2][2]) + __uint_as_float(pg[3][2]));
        a0 += dpp<ROR8>(a0); a1 += dpp<ROR8>(a1);
        float o, t;
        t = x16_a(a0, o); a0 = t + o; t = x16_a(a1, o); a1 = t + o;
        t = x32_a(a0, o); a0 = t + o; t = x32_a(a1, o); a1 = t + o;
        if (lane < 8) *op = pk_bf16(a0, a1);
        if (bid == 0 && lane == 0) __hip_atomic_store((GAS unsigned*)(ws + WS_EPOCH), tag, __ATOMIC_RELAXED, __HIP_MEMORY_SCOPE_AGENT);
        break;
      }
    }
  }
}

extern "C" void kernel_launch(void* const* d_in, const int* in_sizes, int n_in, void* d_out, int out_size, void* d_ws, size_t ws_size,
                              hipStream_t stream) {
  fused_kernel<<<256, 1024, 0, stream>>>(d_in[0], d_in[1], d_in[2], d_in[3], d_in[4], d_in[6], d_in[7], d_in[8], d_out, d_ws);
}
```

```cpp
#include <hip/hip_runtime.h>
#include <hip/hip_bf16.h>
#include <stdint.h>

constexpr int CODE_BYTES = 19200;
constexpr int WK0 = 5, WK = 4, WV = 3, WT = 3;
constexpr int KTRIG = 22;
constexpr int VSPLIT = 10, VPER = 3;
#define QSLEEP 10
#define SSLEEP 24
#define FSLEEP 10
constexpr int NPAIR = 6;

typedef unsigned u32x4 __attribute__((ext_vector_type(4)));
typedef unsigned u32x2 __attribute__((ext_vector_type(2)));
typedef float f32x4 __attribute__((ext_vector_type(4)));
typedef float f32x2 __attribute__((ext_vector_type(2)));
typedef short s16x4 __attribute__((ext_vector_type(4)));
typedef __bf16 bf16x2 __attribute__((ext_vector_type(2)));
typedef __bf16 bf16x8 __attribute__((ext_vector_type(8)));
typedef unsigned short u16;
typedef unsigned u32x16 __attribute__((ext_vector_type(16)));
typedef unsigned long long u64;
#define LAS __attribute__((address_space(3)))
#define GAS __attribute__((address_space(1)))

constexpr int NB = 8, DM = 448, HKV = 8, SK = 4096, DH = 256, WIN = 1024, FF = 1792, NQ = 4096;
constexpr int QPSTR = 4096 + 256, QSTR = 2048 + 128;
constexpr int WS_G = 4096;
constexpr int WS_QP = 16384;
constexpr int WS_Q = WS_QP + 256 * QPSTR;
constexpr int WS_HB = WS_Q + 64 * QSTR;
constexpr int WS_S = WS_HB + 65536;
constexpr int WS_PR = (WS_S + 524288 + 16383) & ~16383;
constexpr int WS_OB = 2 << 20;
constexpr int WS_PO = WS_OB + 16 * 16384;
constexpr int WS_END = WS_PO + 112 * 16384;
static_assert(WS_PR + 8 * 16384 <= WS_OB && WS_END == 4 << 20, "two 2 MB pages");
__device__ __forceinline__ int inbox(int k) { return (k >> 1) * 16384 + (k & 1) * 4096; }
constexpr int L_FLAG = 0, L_Q = 256, L_QO = L_Q + 1024, L_ST = L_QO + 512, L_FW = L_ST + 128, L_OM = L_FW + 64, L_SM = L_OM + 512, L_P = L_SM + 2304, L_OP = L_P + 4096, L_WO = L_OP + 4096, L_W2 = L_WO + 16384,
              L_X = L_W2 + 8192, L_WQ = L_X + 2048, L_T = L_WQ + 14336, L_XA = L_T + 28672, L_END = L_XA + 8192;
enum { F_TAGV = 0, F_TAG = 1, F_QRDY = 2, F_WQC = 3, F_QPC = 4, F_KISS = 5, F_OCNT = 6, F_TCNT = 7, F_VISS = 8, F_SCNT = 9, F_FRDY = 10, F_KCNT = 11, F_OMR = 12, F_G1V = 14, F_G1 = 15, F_PR = 16, F_NRV = 20, F_NR = 21 };
constexpr int AUX_NT = 2, AUX_SC1 = 16;
constexpr float C2 = 0.0625f * 1.4426950408889634f;
constexpr int OOB = 0x7f000000;
constexpr unsigned SPIN_CAP = 1u << 18;

#define RSRC(p, n) __builtin_amdgcn_make_buffer_rsrc((void*)(p), 0, (n), 0x00020000)
#define LD128(r, off, aux) __builtin_amdgcn_raw_buffer_load_b128((r), (off), 0, (aux))
#define ST128(v, r, off, aux) __builtin_amdgcn_raw_buffer_store_b128((v), (r), (off), 0, (aux))
#define SCHED_FENCE() __builtin_amdgcn_sched_barrier(0)
#define MEM_FENCE() asm volatile("" ::: "memory")
template <int N> __device__ __forceinline__ void vmwait() { asm volatile("s_waitcnt vmcnt(%0)" ::"n"(N) : "memory"); }
#define LGKM0() asm volatile("s_waitcnt lgkmcnt(0)" ::: "memory")

__device__ __forceinline__ f32x4 mfma(u32x4 a, u32x4 b, f32x4 c) {
  return __builtin_amdgcn_mfma_f32_16x16x32_bf16(__builtin_bit_cast(bf16x8, a), __builtin_bit_cast(bf16x8, b), c, 0, 0, 0);
}
__device__ __forceinline__ u32x2 trd(LAS unsigned char* p) {
  return __builtin_bit_cast(u32x2, __builtin_amdgcn_ds_read_tr16_b64_v4i16((LAS s16x4*)p));
}
__device__ __forceinline__ unsigned pk_bf16(float lo, float hi) {
  f32x2 v = {lo, hi};
  return __builtin_bit_cast(unsigned, __builtin_convertvector(v, bf16x2));
}
__device__ __forceinline__ float bf_lo(unsigned u) { return __uint_as_float(u << 16); }
__device__ __forceinline__ float bf_hi(unsigned u) { return __uint_as_float(u & 0xffff0000u); }
__device__ __forceinline__ float rbf(float f) { return bf_lo(pk_bf16(f, f)); }
template <int CTRL> __device__ __forceinline__ float dpp(float x) {
  return __builtin_bit_cast(float, __builtin_amdgcn_mov_dpp(__builtin_bit_cast(int, x), CTRL, 0xf, 0xf, true));
}
constexpr int XOR1 = 0xB1, XOR2 = 0x4E, HMIRROR = 0x141, ROR8 = 0x128;
__device__ __forceinline__ float x16_a(float x, float& o) {
  auto s = __builtin_amdgcn_permlane16_swap(__float_as_uint(x), __float_as_uint(x), false, false);
  o = __uint_as_float(s[1]);
  return __uint_as_float(s[0]);
}
__device__ __forceinline__ float x32_a(float x, float& o) {
  auto s = __builtin_amdgcn_permlane32_swap(__float_as_uint(x), __float_as_uint(x), false, false);
  o = __uint_as_float(s[1]);
  return __uint_as_float(s[0]);
}
__device__ __forceinline__ float wave_max(float x) {
  x = fmaxf(x, dpp<XOR1>(x)); x = fmaxf(x, dpp<XOR2>(x)); x = fmaxf(x, dpp<HMIRROR>(x)); x = fmaxf(x, dpp<ROR8>(x));
  float o, a = x16_a(x, o); x = fmaxf(a, o);
  a = x32_a(x, o); return fmaxf(a, o);
}
__device__ __forceinline__ float wave_sum(float x) {
  x += dpp<XOR1>(x); x += dpp<XOR2>(x); x += dpp<HMIRROR>(x); x += dpp<ROR8>(x);
  float o, a = x16_a(x, o); x = a + o;
  a = x32_a(x, o); return a + o;
}
__device__ __forceinline__ void lds_wait_ge(volatile LAS unsigned* f, unsigned want) {
#pragma nounroll
  for (unsigned n = 0; n < (1u << 22); ++n) {
    if (__builtin_amdgcn_readfirstlane(*f) >= want) break;
    __builtin_amdgcn_s_sleep(1);
  }
  MEM_FENCE();
}
__device__ __forceinline__ unsigned bperm(unsigned v, int srclane) { return __builtin_amdgcn_ds_bpermute(srclane << 2, v); }
__device__ __forceinline__ void lds_inc(LAS unsigned char* L, int f, int lane) {
  if (lane == 0) asm volatile("ds_add_u32 %0, %1" ::"v"((unsigned)(uintptr_t)(L + f * 4)), "v"(1u) : "memory");
}
__device__ __forceinline__ unsigned get_tag(volatile LAS unsigned* flag) {
  lds_wait_ge(flag + 0, 1u);
  return __builtin_amdgcn_readfirstlane(flag[1]);
}
__device__ __forceinline__ void dma16(__amdgpu_buffer_rsrc_t r, int voff, unsigned ldsdst) {
  unsigned keep;
  asm volatile("s_mov_b32 %0, m0\n\ts_mov_b32 m0, %3\n\ts_nop 0\n\tbuffer_load_dwordx4 %1, %2, 0 offen nt lds\n\ts_mov_b32 m0, %0"
               : "=&s"(keep) : "v"(voff), "s"(r), "s"(ldsdst) : "memory");
}
__device__ __forceinline__ void dma16p(__amdgpu_buffer_rsrc_t r, int voff, unsigned ldsdst) {
  unsigned keep;
  asm volatile("s_mov_b32 %0, m0\n\ts_mov_b32 m0, %3\n\ts_nop 0\n\tbuffer_load_dwordx4 %1, %2, 0 offen lds\n\ts_mov_b32 m0, %0"
               : "=&s"(keep) : "v"(voff), "s"(r), "s"(ldsdst) : "memory");
}
template <int N>
__device__ __forceinline__ void gatherm(u32x4 (&a)[N], int (&off)[N], unsigned tag, __amdgpu_buffer_rsrc_t r) {
#pragma nounroll
  for (unsigned n = 0; n < SPIN_CAP; ++n) {
    MEM_FENCE();
    u32x4 v[N];
#pragma unroll
    for (int i = 0; i < N; ++i) v[i] = LD128(r, off[i], AUX_SC1);
    bool all = true;
#pragma unroll
    for (int i = 0; i < N; ++i) {
      const bool mine = off[i] != OOB, ok = v[i][1] == tag && v[i][3] == tag;
      a[i][0] = mine ? v[i][0] : a[i][0];
      a[i][2] = mine ? v[i][2] : a[i][2];
      off[i] = ok ? OOB : off[i];
      all = all && (ok || !mine);
    }
    if (__all(all)) break;
  }
}
template <int STEP>
__device__ __forceinline__ void pass4(u32x4 (&a)[4], const u64 (&m)[4], int off, __amdgpu_buffer_rsrc_t r) {
  asm volatile(
      "s_mov_b64 exec, %[m0]\n\tbuffer_load_dwordx4 %[a0], %[o], %[r], 0 offen sc1\n\t"
      "s_mov_b64 exec, %[m1]\n\tbuffer_load_dwordx4 %[a1], %[o], %[r], 0 offen offset:%c[s1] sc1\n\t"
      "s_mov_b64 exec, %[m2]\n\tbuffer_load_dwordx4 %[a2], %[o], %[r], 0 offen offset:%c[s2] sc1\n\t"
      "s_mov_b64 exec, %[m3]\n\tbuffer_load_dwordx4 %[a3], %[o], %[r], 0 offen offset:%c[s3] sc1\n\t"
      "s_mov_b64 exec, -1\n\ts_waitcnt vmcnt(0)"
      : [a0] "+v"(a[0]), [a1] "+v"(a[1]), [a2] "+v"(a[2]), [a3] "+v"(a[3])
      : [m0] "s"(m[0]), [m1] "s"(m[1]), [m2] "s"(m[2]), [m3] "s"(m[3]), [o] "v"(off), [r] "s"(r), [s1] "i"(STEP), [s2] "i"(2 * STEP), [s3] "i"(3 * STEP)
      : "memory");
}
__device__ __forceinline__ bool missing4(const u32x4 (&a)[4], u64 (&m)[4], unsigned tag) {
#pragma unroll
  for (int i = 0; i < 4; ++i) m[i] &= __builtin_amdgcn_ballot_w64(a[i][1] != tag) | __builtin_amdgcn_ballot_w64(a[i][3] != tag);
  return (m[0] | m[1] | m[2] | m[3]) != 0ull;
}
struct Sel { unsigned lo[4], hi[4]; };
__device__ __forceinline__ Sel make_sel(int lane) {
  Sel s;
  const int j = lane & 7;
#pragma unroll
  for (int t = 0; t < 4; ++t) {
    const bool on = (j >> 1) == t;
    s.lo[t] = on ? ((j & 1) ? 0x01000c0cu : 0x0c0c0100u) : 0x0c0c0c0cu;
    s.hi[t] = on ? ((j & 1) ? 0x03020c0cu : 0x0c0c0302u) : 0x0c0c0c0cu;
  }
  return s;
}
__device__ __forceinline__ u32x4 place(unsigned src, const unsigned (&sel)[4]) {
  u32x4 r;
#pragma unroll
  for (int t = 0; t < 4; ++t) r[t] = __builtin_amdgcn_perm(src, src, sel[t]);
  return r;
}

#define KA0 "s36"
#define KA1 "s37"
#define KA6 "s42"
#define KA7 "s43"
#define KB23 "s[54:55]"
#define RT "s[56:59]"
#define RT01 "s[56:57]"
#define RT0 "s56"
#define RT1 "s57"
#define RT2 "s58"
#define RT3 "s59"
__global__ void __launch_bounds__(1024, 4) fused_kernel(const void*, const void*, const void*, const void*, const void*, const void*, const void*,
                                                     const void*, void*, void*) {
  __shared__ __attribute__((aligned(16))) unsigned char smem[L_END];
  LAS unsigned char* L = (LAS unsigned char*)smem;
  volatile LAS unsigned* flag = (volatile LAS unsigned*)(L + L_FLAG);
  const int bid = blockIdx.x, tid = threadIdx.x, lane = tid & 63;
  u32x16 ka;
  u32x4 kb, rt;
  unsigned epochv = 0, wave, t0, t1, t2, v0, v1;
  asm volatile(
      "8:\n\ts_load_dwordx16 %[ka], %[kp], 0x0\n\ts_load_dwordx4 %[kb], %[kp], 0x40\n\t"
      "v_readfirstlane_b32 %[w], %[tid]\n\ts_lshr_b32 %[w], %[w], 6\n\t"
      "s_cmp_lt_u32 %[w], 8\n\ts_cbranch_scc1 1f\n\t"
      "s_add_u32 %[t0], %[bid], 1\n\ts_and_b32 %[t0], %[t0], 3\n\t"
      "s_lshl_b32 %[t2], %[w], 11\n\ts_add_u32 m0, %[t2], %[lx]\n\t"
      "s_cmp_eq_u32 %[w], 8\n\ts_cbranch_scc1 5f\n\t"
      "v_lshrrev_b32 %[v0], 3, %[lane]\n\tv_and_b32 %[v1], 7, %[lane]\n\tv_lshlrev_b32 %[v0], 13, %[v0]\n\tv_lshl_or_b32 %[v0], %[v1], 4, %[v0]\n\t"
      "s_mul_i32 %[t0], %[t0], 0xe0000\n\ts_lshl_b32 %[t1], %[w], 17\n\ts_add_u32 %[t0], %[t0], %[t1]\n\t"
      "s_lshl_b32 %[t1], %[bid], 4\n\ts_and_b32 %[t1], %[t1], 0xf80\n\ts_add_u32 %[t0], %[t0], %[t1]\n\t"
      "s_lshl_b32 %[t1], %[bid], 10\n\ts_and_b32 %[t1], %[t1], 0x1000\n\ts_add_u32 %[t0], %[t0], %[t1]\n\t"
      "s_sub_u32 %[t0], %[t0], 0x120000\n\ts_add_u32 %[t1], %[t0], 0x10000\n\t"
      "s_mov_b32 " RT2 ", 0x380000\n\ts_mov_b32 " RT3 ", 0x20000\n\t"
      "s_waitcnt lgkmcnt(0)\n\ts_mov_b32 " RT0 ", " KA6 "\n\ts_and_b32 " RT1 ", " KA7 ", 0xffff\n\t"
      "s_branch 6f\n"
      "5:\n\t"
      "v_lshrrev_b32 %[v0], 4, %[lane]\n\tv_and_b32 %[v1], 15, %[lane]\n\tv_min_u32 %[v1], 13, %[v1]\n\tv_mul_u32_u24 %[v0], 0x380, %[v0]\n\tv_lshl_add_u32 %[v0], %[v1], 4, %[v0]\n\t"
      "s_mul_i32 %[t0], %[t0], 0xe0\n\tv_add_u32 %[v0], %[t0], %[v0]\n\ts_mov_b32 %[t0], 0\n\ts_movk_i32 %[t1], 0xe00\n\t"
      "s_movk_i32 " RT2 ", 0x1c00\n\ts_mov_b32 " RT3 ", 0x20000\n\t"
      "s_waitcnt lgkmcnt(0)\n\ts_mov_b32 " RT0 ", " KA0 "\n\ts_and_b32 " RT1 ", " KA1 ", 0xffff\n"
      "6:\n\t"
      "buffer_load_dwordx4 %[v0], " RT ", %[t0] offen nt lds\n\t"
      "s_add_u32 m0, m0, 0x400\n\ts_nop 0\n\tbuffer_load_dwordx4 %[v0], " RT ", %[t1] offen nt lds\n\t"
      "s_barrier\n\ts_branch 4f\n"
      "1:\n\ts_cmp_eq_u32 %[w], 0\n\ts_cbranch_scc1 2f\n\t"
      "s_cmp_lt_u32 %[w], 5\n\ts_cbranch_scc1 4f\n\t"
      "s_cmp_eq_u32 %[w], 7\n\ts_cbranch_scc1 3f\n\t"
      "s_cmp_lt_u32 %[bid], 192\n\ts_cbranch_scc1 4f\n\t"
      "s_waitcnt lgkmcnt(0)\n\ts_barrier\n\ts_branch 4f\n"
      "3:\n\t"
      "v_lshlrev_b32 %[v0], 2, %[lane]\n\tds_write_b32 %[v0], %[e]\n\t"
      "s_getpc_b64 " RT01 "\n7:\n\ts_sub_u32 " RT0 ", " RT0 ", 7b-8b\n\ts_subb_u32 " RT1 ", " RT1 ", 0\n\ts_and_b32 " RT0 ", " RT0 ", 0xffffff00\n\ts_and_b32 " RT1 ", " RT1 ", 0xffff\n\t"
      "s_mov_b32 " RT2 ", %[cl]\n\ts_mov_b32 " RT3 ", 0x20000\n\ts_mov_b32 m0, %[lc]\n\t"
      "s_lshr_b32 %[t0], %[bid], 3\n\ts_lshl_b32 %[t0], %[t0], 10\n\tv_lshlrev_b32 %[v0], 4, %[lane]\n\tv_add_u32 %[v0], %[t0], %[v0]\n\t"
      "buffer_load_dwordx4 %[v0], " RT ", 0 offen lds\n\t"
      "s_waitcnt lgkmcnt(0)\n\ts_barrier\n\ts_branch 4f\n"
      "2:\n\ts_waitcnt lgkmcnt(0)\n\tglobal_load_dword %[e], %[e], " KB23 " sc1\n\ts_barrier\n\ts_waitcnt vmcnt(0)\n"
      "4:"
      : [ka] "={s[36:51]}"(ka), [kb] "={s[52:55]}"(kb), [rt] "={s[56:59]}"(rt), [w] "=&s"(wave), [t0] "=&s"(t0), [t1] "=&s"(t1), [t2] "=&s"(t2),
        [v0] "=&v"(v0), [v1] "=&v"(v1), [e] "+v"(epochv)
      : [kp] "{s[0:1]}"(__builtin_amdgcn_kernarg_segment_ptr()), [bid] "{s2}"(bid), [tid] "v"(tid), [lane] "v"(lane), [lx] "i"(L_X - 8 * 2048),
        [lc] "i"(L_WO), [cl] "i"(CODE_BYTES)
      : "memory", "scc");
#define WINDOW(p) (((p) + 0x3fffffull) & ~0x3fffffull)
#define ARG(v, i) ((u64)v[2 * (i)] | ((u64)v[2 * (i) + 1] << 32))
  const int xc = bid & 7, jj = bid >> 3, kvh = jj & 7, s = jj >> 3, b = (xc - 2 * s) & 7;
  const int nkw = s == 3 ? 4 : 6;
  unsigned tag = 0;

  if (__builtin_expect(wave - 1u < (unsigned)nkw, 1)) {
    const bool four = s == 3 && wave >= 3;
    const int urel = four && wave == 4 ? 16 : (wave - 1) * 6;
    const unsigned hsh = (((unsigned)bid * 8u + wave) * 2654435761u) >> 24;
    unsigned ord = (unsigned)((four ? 0x3210321032ull : 0x543210543210ull) >> (((hsh * (four ? 4u : 6u)) >> 8) * 4u));
    int klo = (lane & 7) * 512 + ((lane >> 3) & 1) * 64 + (lane >> 4) * 16 + (s * 36 + urel) * 4096;
    unsigned kvoff = (unsigned)(((b * HKV + kvh) * SK + (SK - WIN)) * DH * 2);
    unsigned brot = (wave + jj + xc) * 128u;
    u32x16 kak = ka;
    u32x4 kbk = kb;
    asm volatile("s_waitcnt lgkmcnt(0)\n\ts_barrier" : "+{s[36:51]}"(kak), "+{s[52:55]}"(kbk), "+v"(klo), "+s"(kvoff), "+s"(ord), "+s"(brot)::"memory");
    const auto rk = RSRC(ARG(kak, 1) + kvoff, WIN * DH * 2);
    u32x4 r[24];
#pragma unroll
    for (int i = 0; i < 24; ++i) {
      if (i >= WK0) vmwait<WK - 1>();
      r[i] = __builtin_amdgcn_raw_buffer_load_b128(rk, i < 16 || !four ? klo : OOB, ((ord >> (i & ~3)) & 15u) * 4096 + ((brot + i * 128) & 0x180), AUX_NT);
      SCHED_FENCE();
      if (i == KTRIG) lds_inc(L, F_KISS, lane);
    }
    const u64 ws = WINDOW(ARG(kbk, 1));
    const auto rws = RSRC(ws, WS_END);
    const int kw = wave - 1, nu = four ? 4 : 6, ulo = s * 36, uhi = s == 3 ? 128 : ulo + 36, u0 = ulo + urel;
    __builtin_amdgcn_s_setprio(2);
    lds_wait_ge(flag + F_QRDY, 1u);
    tag = __builtin_amdgcn_readfirstlane(flag[F_TAG]);
    u32x4 qa[4];
#pragma unroll
    for (int t = 0; t < 4; ++t) qa[t] = *(const LAS u32x4*)(L + L_Q + (lane & 1) * 512 + ((brot + t * 128) & 0x180) + ((lane >> 1) & 1) * 64 + (lane >> 4) * 16);
    float s0 = 0.f, s1 = 0.f;
#pragma unroll
    for (int u = 0; u < 6; ++u) {
      f32x4 c = {0, 0, 0, 0};
#pragma unroll
      for (int t = 0; t < 4; ++t) c = mfma(qa[t], r[4 * u + t], c);
      float c0 = (lane & 8) ? c[2] : c[0], c1 = (lane & 8) ? c[3] : c[1];
      c0 += dpp<ROR8>(c0); c1 += dpp<ROR8>(c1);
      if ((unsigned)(lane >> 3) == ((ord >> (4 * u)) & 15u) && (u < 4 || !four)) { s0 = c0; s1 = c1; }
    }
    const unsigned pk = pk_bf16(s0, s1);
    const unsigned pa = bperm(pk, (2 * lane) & 63), pb = bperm(pk, (2 * lane + 1) & 63);
    if (lane < 4 * nu) {
      ST128((u32x4{pa, tag, pb, tag}), rws, WS_S + (b * 8 + kvh) * 8192 + u0 * 64 + lane * 16, AUX_SC1);
      *(LAS u32x4*)(L + L_SM + urel * 64 + lane * 16) = u32x4{pa, tag, pb, tag};
    }
    MEM_FENCE();
    lds_inc(L, F_KCNT, lane);
    if (wave <= 4) {
      u32x4 sg[2];
      int of[2];
      bool own[2];
#pragma unroll
      for (int i = 0; i < 2; ++i) {
        const int myu = (kw + 4 * i) * 16 + (lane >> 2);
        own[i] = myu >= ulo && myu < uhi;
        of[i] = own[i] ? OOB : WS_S + (b * 8 + kvh) * 8192 + (kw + 4 * i) * 1024 + lane * 16;
      }
      __builtin_amdgcn_s_sleep(SSLEEP);
      gatherm<2>(sg, of, tag, rws);
      lds_wait_ge(flag + F_KCNT, (unsigned)nkw);
#pragma unroll
      for (int i = 0; i < 2; ++i) {
        const int c = kw + 4 * i;
        const u32x4 mv = *(const LAS u32x4*)(L + L_SM + (own[i] ? (c * 16 - ulo) * 64 + lane * 16 : 0));
        const unsigned sa = own[i] ? mv[0] : sg[i][0], sb = own[i] ? mv[2] : sg[i][2];
        const float m0 = wave_max(fmaxf(bf_lo(sa), bf_lo(sb))), m1 = wave_max(fmaxf(bf_hi(sa), bf_hi(sb)));
        const float n0 = m0 == -INFINITY ? 0.f : m0, n1 = m1 == -INFINITY ? 0.f : m1;
        const float ea = __builtin_amdgcn_exp2f((bf_lo(sa) - n0) * C2), eb = __builtin_amdgcn_exp2f((bf_lo(sb) - n0) * C2);
        const float ec = __builtin_amdgcn_exp2f((bf_hi(sa) - n1) * C2), ed = __builtin_amdgcn_exp2f((bf_hi(sb) - n1) * C2);
        *(LAS unsigned*)(L + L_P + c * 512 + lane * 4) = pk_bf16(ea, eb);
        *(LAS unsigned*)(L + L_P + c * 512 + 256 + lane * 4) = pk_bf16(ec, ed);
        const float l0 = wave_sum(ea + eb), l1 = wave_sum(ec + ed);
        if (lane == 0) *(LAS f32x4*)(L + L_ST + c * 16) = f32x4{m0, l0, m1, l1};
      }
      MEM_FENCE();
      lds_inc(L, F_PR + kw, lane);
      lds_inc(L, F_SCNT, lane);
    }
    return;
  }
  const GAS u16* x = (const GAS u16*)ARG(ka, 0);
  const u64 vc = ARG(ka, 2), wo = ARG(ka, 4), w1 = ARG(ka, 5), w2 = ARG(ka, 6), ws = WINDOW(ARG(kb, 1));
  const GAS u16* gw = (const GAS u16*)ARG(ka, 7);
  GAS u16* out = (GAS u16*)ARG(kb, 0);
  const auto rws = RSRC(ws, WS_END);
  const int cb = jj + 32 * (xc >> 2), rb = (xc + 1) & 3;
  const int grp = kvh * 4 + s;
  if (wave >= 8) {
    const unsigned kvoff = (unsigned)(((b * HKV + kvh) * SK + (SK - WIN)) * DH * 2);
    const int w = wave - 8;
    vmwait<0>();
    lds_inc(L, F_WQC, lane);
    tag = get_tag(flag);
    if (w >= 4) {
      __builtin_amdgcn_s_setprio(3);
      const int ct = w - 4;
      const int xrow = ((lane & 7) >> 1) + 4 * (lane & 1);
      LAS unsigned char* xp = L + L_X + xrow * 256 + (lane >> 4) * 16;
      LAS unsigned char* xp2 = L + L_X + xrow * 256 + 192 + (lane >> 4) * 8;
      LAS unsigned char* tb = L + L_WQ + ((lane >> 4) * 8 + ((lane >> 2) & 3)) * 128 + (lane & 3) * 8 + ct * 32;
      LAS unsigned char* tb2 = L + L_WQ + (96 + (lane >> 4) * 4 + ((lane >> 2) & 3)) * 128 + (lane & 3) * 8 + ct * 32;
      const int dest = ((lane >> 4) & 1) * 2 + (lane >> 5);
      const int qo = WS_QP + (cb * 4 + dest) * QPSTR + rb * 1024 + ct * 256 + (lane & 15) * 16;
      lds_wait_ge(flag + F_WQC, 8u);
      const u32x2 pt = trd(tb2), xb = *(const LAS u32x2*)xp2;
      f32x4 acc = mfma(u32x4{xb[0], xb[1], 0u, 0u}, u32x4{pt[0], pt[1], 0u, 0u}, f32x4{0, 0, 0, 0});
#pragma unroll
      for (int ks = 0; ks < 3; ++ks) {
        const u32x2 p0 = trd(tb + ks * 4096), p1 = trd(tb + ks * 4096 + 512);
        acc = mfma(*(const LAS u32x4*)(xp + ks * 64), u32x4{p0[0], p0[1], p1[0], p1[1]}, acc);
      }
      const float q0 = lane >= 32 ? acc[2] : acc[0], q1 = lane >= 32 ? acc[3] : acc[1];
      if (dest != rb) ST128((u32x4{__float_as_uint(q0), tag, __float_as_uint(q1), tag}), rws, qo, AUX_SC1);
      else *(LAS f32x2*)(L + L_QO + (ct * 16 + (lane & 15)) * 8) = f32x2{q0, q1};
      LGKM0();
      lds_inc(L, F_QPC, lane);
      __builtin_amdgcn_s_setprio(0);
    }
    const auto rv = RSRC(vc + kvoff, WIN * DH * 2);
    u32x4 r[16];
    const int trot = w + jj + xc;
    const int voff = (w * 128 + (lane >> 4) * 8 + ((lane >> 3) & 1)) * 512 + s * 128 + (lane & 7) * 16;
    const Sel sel = make_sel(lane);
    lds_wait_ge(flag + F_KISS, (unsigned)nkw);
#define V_ISSUE(i) do { if ((i) >= WV) vmwait<WV - 1>(); \
      r[i] = __builtin_amdgcn_raw_buffer_load_b128(rv, voff + ((i) & 3) * 1024, ((((i) >> 2) + trot) & 3) * 16384, AUX_NT); SCHED_FENCE(); \
      if ((i) == 15) lds_inc(L, F_VISS, lane); } while (0)
#define V_USE(i) do { a0 = mfma(place(pp[(i) >> 2][(i) & 3], sel.lo), r[i], a0); a1 = mfma(place(pp[(i) >> 2][(i) & 3], sel.hi), r[i], a1); } while (0)
#pragma unroll
    for (int i = 0; i <= VSPLIT; ++i) V_ISSUE(i);
    u32x4 pp[4];
    f32x4 a0 = {0, 0, 0, 0}, a1 = {0, 0, 0, 0};
#define P_READ() _Pragma("unroll") for (int t = 0; t < 4; ++t) \
      pp[t] = *(const LAS u32x4*)(L + L_P + w * 512 + ((lane >> 3) & 1) * 256 + (((t + trot) & 3) * 32 + (lane >> 4) * 8) * 2)
    float f;
    unsigned wad = (unsigned)(uintptr_t)(L + L_OP) + w * 512 + ((lane >> 5) * 64 + (lane & 7) * 8 + ((lane >> 4) & 1) * 4) * 4;
    unsigned rad = (unsigned)(uintptr_t)(L + L_OP) + (lane >> 5) * 2048 + (lane & 31) * 16;
    int oad = WS_OB + inbox(grp) + b * 512 + lane * 16;
#define F_GET() do { lds_wait_ge(flag + F_FRDY, 1u); f = *(const LAS float*)(L + L_FW + w * 8 + (lane >> 5) * 4); \
      asm volatile("" : "+v"(f), "+v"(wad), "+v"(rad), "+v"(oad)); } while (0)
    if (__builtin_expect(__builtin_amdgcn_readfirstlane(flag[F_PR + (w & 3)]) != 0u, 1)) {
      MEM_FENCE();
      P_READ();
#pragma unroll
      for (int i = VSPLIT + 1; i < 16; ++i) {
        V_ISSUE(i);
#pragma unroll
        for (int k = 0; k < 16; ++k)
          if (k >= (i - VSPLIT - 1) * VPER && k < (i - VSPLIT) * VPER && k <= i - WV) V_USE(k);
        SCHED_FENCE();
      }
#pragma unroll
      for (int k = 0; k < 16 - WV; ++k) {
        bool done = false;
#pragma unroll
        for (int i = VSPLIT + 1; i < 16; ++i) done = done || (k >= (i - VSPLIT - 1) * VPER && k < (i - VSPLIT) * VPER && k <= i - WV);
        if (!done) { V_USE(k); if (k & 1) SCHED_FENCE(); }
      }
      SCHED_FENCE();
      F_GET();
      u32x4 la[WV], lb[WV];
#pragma unroll
      for (int k = 0; k < WV; ++k) {
        la[k] = place(pp[(16 - WV + k) >> 2][(16 - WV + k) & 3], sel.lo);
        lb[k] = place(pp[(16 - WV + k) >> 2][(16 - WV + k) & 3], sel.hi);
        asm volatile("" : "+v"(la[k]), "+v"(lb[k]));
      }
      SCHED_FENCE();
#pragma unroll
      for (int k = 0; k < WV; ++k) { a0 = mfma(la[k], r[16 - WV + k], a0); a1 = mfma(lb[k], r[16 - WV + k], a1); }
    } else {
#pragma unroll
      for (int i = VSPLIT + 1; i < 16; ++i) V_ISSUE(i);
      lds_wait_ge(flag + F_PR + (w & 3), 1u);
      P_READ();
#pragma unroll
      for (int k = 0; k < 16; ++k) { V_USE(k); if (k & 1) SCHED_FENCE(); }
      F_GET();
    }
    f32x4 t = (lane & 8) ? a1 : a0;
#pragma unroll
    for (int k = 0; k < 4; ++k) t[k] = (t[k] + dpp<ROR8>(t[k])) * f;
    unsigned old;
    f32x4 o0v, o1v, o2v, o3v;
    asm volatile("ds_write_b128 %[wa], %[t]\n\tds_add_rtn_u32 %[old], %[ca], %[one]\n\t"
                 "ds_read_b128 %[p0], %[ra]\n\tds_read_b128 %[p1], %[ra] offset:512\n\tds_read_b128 %[p2], %[ra] offset:1024\n\tds_read_b128 %[p3], %[ra] offset:1536\n\t"
                 "s_waitcnt lgkmcnt(0)"
                 : [old] "=&v"(old), [p0] "=&v"(o0v), [p1] "=&v"(o1v), [p2] "=&v"(o2v), [p3] "=&v"(o3v)
                 : [wa] "v"(wad), [t] "v"(t), [ca] "v"((unsigned)(uintptr_t)(L + (lane == 0 ? F_OCNT * 4 : 128 + (lane & 31) * 4))), [one] "v"(1u), [ra] "v"(rad)
                 : "memory");
    if (__builtin_amdgcn_readfirstlane(old) == 7u) {
      f32x4 ov = (o0v + o1v) + (o2v + o3v);
#pragma unroll
      for (int k = 0; k < 4; ++k) { float hi, lo = x32_a(ov[k], hi); ov[k] = lo + hi; }
      if (lane < 32) {
        const u32x4 ogr = {pk_bf16(ov[0], ov[1]), tag, pk_bf16(ov[2], ov[3]), tag};
        ST128(ogr, rws, oad, AUX_SC1);
        ST128(ogr, rws, oad + 8192, AUX_SC1);
        *(LAS u32x4*)(L + L_OM + lane * 16) = ogr;
      }
      MEM_FENCE();
      lds_inc(L, F_OMR, lane);
    }
  } else if (wave == 0) {
    __builtin_amdgcn_s_setprio(3);
    tag = __builtin_amdgcn_readfirstlane(epochv) * 1664525u + 1013904223u;
    if (lane == 0) *(volatile LAS u32x2*)flag = u32x2{1u, tag};
    lds_wait_ge(flag + F_QPC, 4u);
    __builtin_amdgcn_s_sleep(QSLEEP);
    {
      const int po = WS_QP + (cb * 4 + rb) * QPSTR + lane * 16;
      u32x4 pg[3];
      int of[3] = {po + ((rb + 1) & 3) * 1024, po + ((rb + 2) & 3) * 1024, po + ((rb + 3) & 3) * 1024};
      gatherm<3>(pg, of, tag, rws);
      const f32x2 own = *(const LAS f32x2*)(L + L_QO + lane * 8);
      const float a0 = (own[0] + __uint_as_float(pg[0][0])) + (__uint_as_float(pg[1][0]) + __uint_as_float(pg[2][0]));
      const float a1 = (own[1] + __uint_as_float(pg[0][2])) + (__uint_as_float(pg[1][2]) + __uint_as_float(pg[2][2]));
      const float n0 = dpp<XOR1>(a0), n1 = dpp<XOR1>(a1);
      const unsigned pk = (lane & 1) ? pk_bf16(n1, a1) : pk_bf16(a0, n0);
      const int src = (lane & 15) * 4 + ((lane >> 4) & 1);
      const unsigned pa = bperm(pk, src), pb = bperm(pk, src + 2);
      if (lane < 32) ST128((u32x4{pa, tag, pb, tag}), rws, WS_Q + ((cb >> 3) * 8 + rb + 4 * (lane >> 4)) * QSTR + (cb & 7) * 256 + (lane & 15) * 16, AUX_SC1);
    }
    __builtin_amdgcn_s_sleep(QSLEEP);
    {
      const int qoff = WS_Q + (kvh * 8 + b) * QSTR + lane * 16;
      u32x4 qq[2];
      int of[2] = {qoff, qoff + 1024};
      gatherm<2>(qq, of, tag, rws);
      *(LAS u32x2*)(L + L_Q + lane * 8) = u32x2{qq[0][0], qq[0][2]};
      *(LAS u32x2*)(L + L_Q + 512 + lane * 8) = u32x2{qq[1][0], qq[1][2]};
      LGKM0();
      if (lane == 0) flag[F_QRDY] = 1u;
    }
    u32x4 xg = {0, 0, 0, 0}, gg = {0, 0, 0, 0};
    if (lane < 56) {
      xg = *(const GAS u32x4*)(x + b * DM + lane * 8);
      gg = *(const GAS u32x4*)(gw + lane * 8);
    }
    float z = 0.f;
#pragma unroll
    for (int t = 0; t < 4; ++t) z += rbf(bf_lo(xg[t]) * bf_lo(gg[t])) + rbf(bf_hi(xg[t]) * bf_hi(gg[t]));
    z = rbf(wave_sum(z));
    const float g = rbf(1.f / (1.f + __expf(-z)));
    const unsigned g1u = __float_as_uint(rbf(1.f - g));
    if (lane == 0) *(volatile LAS u32x2*)(flag + F_G1V) = u32x2{1u, g1u};
    lds_wait_ge(flag + F_SCNT, 4u);
    {
      const f32x2 ml = *(const LAS f32x2*)(L + L_ST + (lane & 15) * 8);
      float M = fmaxf(ml[0], dpp<XOR2>(ml[0]));
      M = fmaxf(M, dpp<0x124>(M)); M = fmaxf(M, dpp<ROR8>(M));
      const float al = __builtin_amdgcn_exp2f((ml[0] - M) * C2);
      float Ls = al * ml[1];
      Ls += dpp<XOR2>(Ls); Ls += dpp<0x124>(Ls); Ls += dpp<ROR8>(Ls);
      if (lane < 16) *(LAS float*)(L + L_FW + lane * 4) = al * (g / Ls);
      LGKM0();
      if (lane == 0) flag[F_FRDY] = 1u;
    }
  } else if (wave != 7 && xc < 7) {
    const int j = wave - 5, t = xc * 8 + kvh;
    const unsigned lb = (unsigned)(uintptr_t)(L + L_T) + j * 14336;
    const auto r1 = RSRC(w1, DM * FF * 2);
    const int o = ((j * 224 + (lane >> 2)) * FF + t * 32 + (lane & 3) * 8) * 2;
#pragma unroll
    for (int i = 0; i < 14; ++i) {
      if (i >= WT) vmwait<WT - 1>();
      dma16(r1, o + i * 16 * FF * 2, lb + i * 1024);
    }
    {
      const auto rx = RSRC((u64)x, NB * DM * 2);
#pragma unroll
      for (int i = 0; i < 4; ++i) {
        vmwait<WT - 1>();
        dma16p(rx, (j * 4 + i) * 1024 + lane * 16, (unsigned)(uintptr_t)(L + L_XA) + (j * 4 + i) * 1024);
      }
    }
    tag = get_tag(flag);
    vmwait<0>();
    lds_inc(L, F_TCNT, lane);
    lds_wait_ge(flag + F_TCNT, 2u);
    LAS unsigned char* tb = L + L_T + ((lane >> 4) * 8 + ((lane >> 2) & 3)) * 64 + j * 32 + (lane & 3) * 8;
    f32x4 acc0 = {0, 0, 0, 0}, acc1 = {0, 0, 0, 0};
#pragma unroll
    for (int ks = 0; ks < 14; ++ks) {
      const u32x2 p0 = trd(tb + ks * 2048), p1 = trd(tb + ks * 2048 + 256);
      const u32x4 xa = *(const LAS u32x4*)(L + L_XA + (lane & 7) * (DM * 2) + ks * 64 + (lane >> 4) * 16);
      if (ks & 1) acc1 = mfma(u32x4{p0[0], p0[1], p1[0], p1[1]}, xa, acc1);
      else acc0 = mfma(u32x4{p0[0], p0[1], p1[0], p1[1]}, xa, acc0);
    }
    acc0 += acc1;
    float hv[4];
#pragma unroll
    for (int k = 0; k < 4; ++k) {
      const float v = rbf(acc0[k]);
      hv[k] = v / (1.f + __expf(-v));
    }
    if (!(lane & 8))
      ST128((u32x4{pk_bf16(hv[0], hv[1]), tag, pk_bf16(hv[2], hv[3]), tag}), rws, WS_HB + t * 1024 + j * 512 + (lane >> 4) * 128 + (lane & 7) * 16, AUX_SC1);
  }
  const int cpair = (xc ^ 1) * 32 + jj;
  if (wave == 7) {
    const int fgrp = b == 7 ? -1 : s < 3 ? kvh * 3 + s : (kvh < 4 ? 24 + kvh : -1);
    tag = get_tag(flag);
    const bool hi8 = lane & 8;
    const unsigned upper = (__builtin_amdgcn_s_getreg(20 | (3 << 11)) >> 2) & 1u;
    if (s == 0) {
      const u64 pa = ws + WS_PR + kvh * 16384 + b * 1024;
      int vote = 0;
#pragma unroll
      for (int i = 0; i < NPAIR; ++i) {
        u64 t0, t1, t2;
        unsigned d;
        asm volatile("s_memtime %0\n\ts_waitcnt lgkmcnt(0)\n\ts_load_dword %3, %4, %5\n\ts_waitcnt lgkmcnt(0)\n\ts_memtime %1\n\ts_waitcnt lgkmcnt(0)\n\t"
                     "s_load_dword %3, %4, %6\n\ts_waitcnt lgkmcnt(0)\n\ts_memtime %2\n\ts_waitcnt lgkmcnt(0)"
                     : "=&s"(t0), "=&s"(t1), "=&s"(t2), "=&s"(d) : "s"(pa), "i"(i * 128), "i"(i * 128 + 8192) : "memory");
        vote += (unsigned)(t1 - t0) > (unsigned)(t2 - t1) ? 1 : -1;
      }
      lds_wait_ge(flag + F_G1V, 1u);
      if (lane < 8) ST128((u32x4{flag[F_G1], tag, (unsigned)(upper ? -vote : vote), tag}), rws, WS_G + (kvh * 8 + b) * 128 + lane * 16, AUX_SC1);
    }
    u32x4 hb[3];
    int nearc;
    {
      const unsigned lb = (unsigned)(uintptr_t)(L + L_WO);
      const auto r2 = RSRC(w2, FF * DM * 2);
      const auto ro = RSRC(wo, NQ * DM * 2);
      const int off2 = (fgrp * 64 + (lane >> 3)) * 896 + b * 128 + (lane & 7) * 16;
      const int offo = (kvh * 512 + s * 64 + (lane >> 3)) * 896 + b * 128 + (lane & 7) * 16;
      lds_wait_ge(flag + F_SCNT, 2u);
      {
        int of[3] = {fgrp >= 0 ? WS_HB + fgrp * 2048 + lane * 16 : OOB, fgrp >= 0 ? WS_HB + fgrp * 2048 + 1024 + lane * 16 : OOB, WS_G + (kvh * 8 + (lane & 7)) * 128};
        gatherm<3>(hb, of, tag, rws);
        float vs = (float)(int)hb[2][2];
        vs += dpp<XOR1>(vs); vs += dpp<XOR2>(vs); vs += dpp<HMIRROR>(vs);
        nearc = (((__builtin_amdgcn_readfirstlane(__float_as_uint(vs)) >> 31) ^ upper) & 1u) * 8192;
        if (lane == 0) *(volatile LAS u32x2*)(flag + F_NRV) = u32x2{1u, (unsigned)nearc};
      }
      if (b == 7) return;
      lds_wait_ge(flag + F_VISS, 8u);
      if (fgrp >= 0) {
#pragma unroll
        for (int i = 0; i < 8; ++i) dma16(r2, off2 + i * 8 * 896, lb + 16384 + i * 1024);
      }
#pragma unroll
      for (int i = 0; i < 16; ++i) dma16(ro, offo + ((i >> 3) * 256 + (i & 7) * 8) * 896, lb + i * 1024);
    }
    __builtin_amdgcn_s_setprio(3);
    const bool ownb = (lane & 7) == b;
    const int ooff = WS_OB + nearc + inbox(grp) + (lane & 7) * 512 + (lane >> 3) * 16;
    u32x4 ob[4];
#pragma unroll
    for (int i = 0; i < 4; ++i) ob[i] = LD128(rws, ownb ? OOB : ooff + i * 128, AUX_SC1);
    SCHED_FENCE();
    LAS unsigned char* tb = L + L_WO + ((lane >> 4) * 8 + ((lane >> 2) & 3)) * 128 + (lane & 3) * 8;
    f32x4 acc[4] = {{0, 0, 0, 0}, {0, 0, 0, 0}, {0, 0, 0, 0}, {0, 0, 0, 0}};
    if (fgrp >= 0) {
      vmwait<20>();
      const u32x4 hv = {hb[0][0], hb[0][2], hb[1][0], hb[1][2]};
      const float g1 = __uint_as_float(hb[2][0]);
#pragma unroll
      for (int ks = 0; ks < 2; ++ks) {
        const u32x4 bf = {hi8 ? 0u : hv[2 * ks], hi8 ? 0u : hv[2 * ks + 1], hi8 ? hv[2 * ks] : 0u, hi8 ? hv[2 * ks + 1] : 0u};
#pragma unroll
        for (int ct = 0; ct < 4; ++ct) {
          const u32x2 p0 = trd(tb + 16384 + ks * 4096 + ct * 32), p1 = trd(tb + 16384 + ks * 4096 + 512 + ct * 32);
          acc[ct] = mfma(u32x4{p0[0], p0[1], p1[0], p1[1]}, bf, acc[ct]);
        }
      }
#pragma unroll
      for (int ct = 0; ct < 4; ++ct) acc[ct] *= g1;
    }
    u32x4 wf[4][4];
#pragma unroll
    for (int ks = 0; ks < 4; ++ks) {
      if (ks == 0) vmwait<16>(); else if (ks == 1) vmwait<12>(); else if (ks == 2) vmwait<8>(); else vmwait<4>();
#pragma unroll
      for (int ct = 0; ct < 4; ++ct) {
        const u32x2 p0 = trd(tb + ks * 4096 + ct * 32), p1 = trd(tb + ks * 4096 + 512 + ct * 32);
        wf[ks][ct] = u32x4{p0[0], p0[1], p1[0], p1[1]};
      }
    }
    int po = WS_PO + (nearc ^ (((xc ^ b) >> 2) & 1) * 8192) + inbox(b * 32 + (lane >> 3)) + grp * 128 + (lane & 7) * 16;
    asm volatile("" : "+v"(po));
    lds_wait_ge(flag + F_OMR, 1u);
#pragma unroll
    for (int i = 0; i < 4; ++i) {
      const u32x4 v = *(const LAS u32x4*)(L + L_OM + i * 128 + (lane >> 3) * 16);
      ob[i] = ownb ? v : ob[i];
    }
    u64 ms[4] = {~0ull, ~0ull, ~0ull, ~0ull};
    if (missing4(ob, ms, tag)) {
#pragma nounroll
      for (unsigned n = 0; n < SPIN_CAP; ++n) {
        pass4<128>(ob, ms, ooff, rws);
        if (!missing4(ob, ms, tag)) break;
      }
    }
    u32x4 bf[4];
#pragma unroll
    for (int ks = 0; ks < 4; ++ks) bf[ks] = u32x4{hi8 ? 0u : ob[ks][0], hi8 ? 0u : ob[ks][2], hi8 ? ob[ks][0] : 0u, hi8 ? ob[ks][2] : 0u};
#pragma unroll
    for (int ct = 0; ct < 4; ++ct)
#pragma unroll
      for (int ks = 0; ks < 4; ++ks) acc[ct] = mfma(wf[ks][ct], bf[ks], acc[ct]);
    u32x4 pv[4];
#pragma unroll
    for (int ct = 0; ct < 4; ++ct) {
      const float k0 = hi8 ? acc[ct][2] : acc[ct][0], k1 = hi8 ? acc[ct][3] : acc[ct][1];
      const float s0 = hi8 ? acc[ct][0] : acc[ct][2], s1 = hi8 ? acc[ct][1] : acc[ct][3];
      pv[ct] = u32x4{__float_as_uint(k0 + dpp<ROR8>(s0)), tag, __float_as_uint(k1 + dpp<ROR8>(s1)), tag};
      __builtin_amdgcn_raw_buffer_store_b128(pv[ct], rws, po, ct * 4 * 16384, AUX_SC1);
    }
    po ^= 8192;
#pragma unroll
    for (int ct = 0; ct < 4; ++ct) __builtin_amdgcn_raw_buffer_store_b128(pv[ct], rws, po, ct * 4 * 16384, AUX_SC1);
  } else if (wave == 0 && cpair < 224) {
    __builtin_amdgcn_s_setprio(3);
    lds_wait_ge(flag + F_OCNT, 8u);
    __builtin_amdgcn_s_sleep(FSLEEP);
    lds_wait_ge(flag + F_NRV, 1u);
    const int poff = WS_PO + __builtin_amdgcn_readfirstlane(flag[F_NR]) + inbox(cpair) + lane * 16;
    u32x4 pg[4] = {{0, 0, 0, 0}, {0, 0, 0, 0}, {0, 0, 0, 0}, {0, 0, 0, 0}};
    u64 ms[4] = {~0ull, ~0ull, ~0ull, ~0ull};
    GAS unsigned* op = (GAS unsigned*)(out + (lane & 7) * DM + cpair * 2);
    asm volatile("" : "+v"(op));
#pragma nounroll
    for (unsigned n = 0; n < SPIN_CAP; ++n) {
      pass4<1024>(pg, ms, poff, rws);
      if (!missing4(pg, ms, tag)) {
        float a0 = (__uint_as_float(pg[0][0]) + __uint_as_float(pg[1][0])) + (__uint_as_float(pg[2][0]) + __uint_as_float(pg[3][0]));
        float a1 = (__uint_as_float(pg[0][2]) + __uint_as_float(pg[1][2])) + (__uint_as_float(pg[2][2]) + __uint_as_float(pg[3][2]));
        a0 += dpp<ROR8>(a0); a1 += dpp<ROR8>(a1);
        float o, t;
        t = x16_a(a0, o); a0 = t + o; t = x16_a(a1, o); a1 = t + o;
        t = x32_a(a0, o); a0 = t + o; t = x32_a(a1, o); a1 = t + o;
        if (lane < 8) *op = pk_bf16(a0, a1);
        if (bid == 0 && lane == 0) __hip_atomic_store((GAS unsigned*)ARG(kb, 1), tag, __ATOMIC_RELAXED, __HIP_MEMORY_SCOPE_AGENT);
        break;
      }
    }
  }
}

extern "C" void kernel_launch(void* const* d_in, const int* in_sizes, int n_in, void* d_out, int out_size, void* d_ws, size_t ws_size,
                              hipStream_t stream) {
  fused_kernel<<<256, 1024, 0, stream>>>(d_in[0], d_in[1], d_in[2], d_in[3], d_in[4], d_in[6], d_in[7], d_in[8], d_out, d_ws);
}
```

```cpp
#include <hip/hip_runtime.h>
#include <hip/hip_bf16.h>
#include <stdint.h>

#define STAMPS 0
#define CODEPF 1
#define CODE_BYTES 19200
#define VAUX 2
#define KAUX 2
#define KLATE 0
#define W1P 0
#define U1 58
#define WNT " nt"
#define WK2 WK
#define WV2 WV
#define WVE WV
#define QSLEEP1 10
#define QSLEEP2 10
#define FSLEEP 10
#define WK 4
#define WV 3
#define WT 3
#define WK0 5
#define HFLAG F_SCNT
#define HTRIG 2u
#define NPAIR 6
#define FLIP 1
#define S3W 0
#define VPER 3
#define SSLEEP 24
#define VSPLIT 10
#define VTRIG 15
#define KTRIG 22

typedef unsigned u32x4 __attribute__((ext_vector_type(4)));
typedef unsigned u32x2 __attribute__((ext_vector_type(2)));
typedef float f32x4 __attribute__((ext_vector_type(4)));
typedef float f32x2 __attribute__((ext_vector_type(2)));
typedef short s16x4 __attribute__((ext_vector_type(4)));
typedef short s16x8 __attribute__((ext_vector_type(8)));
typedef __bf16 bf16x2 __attribute__((ext_vector_type(2)));
typedef __bf16 bf16x8 __attribute__((ext_vector_type(8)));
typedef unsigned short u16;
typedef unsigned u32x16 __attribute__((ext_vector_type(16)));
typedef unsigned long long u64;
#define LAS __attribute__((address_space(3)))
#define GAS __attribute__((address_space(1)))

constexpr int NB = 8, DM = 448, HKV = 8, SK = 4096, DH = 256, WIN = 1024, FF = 1792, NQ = 4096;
constexpr int PSTR = 4096 + 128;
constexpr int QPSTR = 4096 + 256, QSTR = 2048 + 128, HSTR = 256;
constexpr int WS_G = 4096;
constexpr int WS_QP = 16384;
constexpr int WS_Q = WS_QP + 256 * QPSTR;
constexpr int WS_HB = WS_Q + 64 * QSTR;
constexpr int WS_S = WS_HB + 65536;
constexpr int WS_PR = (WS_S + 524288 + 16383) & ~16383;
constexpr int WS_OB = 2 << 20;
constexpr int WS_PO = WS_OB + 16 * 16384;
constexpr int WS_END = WS_PO + 112 * 16384;
static_assert(WS_PR + 8 * 16384 <= WS_OB && WS_END == 4 << 20, "two 2 MB pages");
__device__ __forceinline__ int inbox(int k) { return (k >> 1) * 16384 + (k & 1) * 4096; }
constexpr int WS_STAMP = 8 << 20;
constexpr int L_FLAG = 0, L_Q = 256, L_QO = L_Q + 1024, L_ST = L_QO + 512, L_FW = L_ST + 128, L_OM = L_FW + 64, L_SM = L_OM + 512, L_P = L_SM + 2304, L_OP = L_P + 4096, L_WO = L_OP + 4096, L_W2 = L_WO + 16384,
              L_X = L_W2 + 8192, L_WQ = L_X + 2048, L_T = L_WQ + 14336, L_XA = L_T + 28672, L_END = L_XA + 8192;
enum { F_TAGV = 0, F_TAG = 1, F_QRDY = 2, F_WQC = 3, F_QPC = 4, F_KISS = 5, F_OCNT = 6, F_TCNT = 7, F_VISS = 8, F_SCNT = 9, F_FRDY = 10, F_KCNT = 11, F_OMR = 12, F_G1V = 14, F_G1 = 15, F_PR = 16, F_NRV = 20, F_NR = 21 };
#define LDAUX 16
#define STAUX 16
#define WS_AT_END 0
constexpr int AUX_NT = 2, AUX_SC1 = 16;
constexpr float C2 = 0.0625f * 1.4426950408889634f;
constexpr int OOB = 0x7f000000;
constexpr unsigned SPIN_CAP = 1u << 18;

#define RSRC(p, n) __builtin_amdgcn_make_buffer_rsrc((void*)(p), 0, (n), 0x00020000)
#define LD128(r, off, aux) __builtin_amdgcn_raw_buffer_load_b128((r), (off), 0, (aux))
#define ST128(v, r, off, aux) __builtin_amdgcn_raw_buffer_store_b128((v), (r), (off), 0, (aux))
#define SCHED_FENCE() __builtin_amdgcn_sched_barrier(0)
#define MEM_FENCE() asm volatile("" ::: "memory")
template <int N> __device__ __forceinline__ void vmwait() { asm volatile("s_waitcnt vmcnt(%0)" ::"n"(N) : "memory"); }
#define LGKM0() asm volatile("s_waitcnt lgkmcnt(0)" ::: "memory")

__device__ __forceinline__ f32x4 mfma(u32x4 a, u32x4 b, f32x4 c) {
  return __builtin_amdgcn_mfma_f32_16x16x32_bf16(__builtin_bit_cast(bf16x8, a), __builtin_bit_cast(bf16x8, b), c, 0, 0, 0);
}
__device__ __forceinline__ u32x2 trd(LAS unsigned char* p) {
  return __builtin_bit_cast(u32x2, __builtin_amdgcn_ds_read_tr16_b64_v4i16((LAS s16x4*)p));
}
__device__ __forceinline__ unsigned pk_bf16(float lo, float hi) {
  f32x2 v = {lo, hi};
  return __builtin_bit_cast(unsigned, __builtin_convertvector(v, bf16x2));
}
__device__ __forceinline__ float bf_lo(unsigned u) { return __uint_as_float(u << 16); }
__device__ __forceinline__ float bf_hi(unsigned u) { return __uint_as_float(u & 0xffff0000u); }
__device__ __forceinline__ float rbf(float f) { return bf_lo(pk_bf16(f, f)); }
template <int CTRL> __device__ __forceinline__ float dpp(float x) {
  return __builtin_bit_cast(float, __builtin_amdgcn_mov_dpp(__builtin_bit_cast(int, x), CTRL, 0xf, 0xf, true));
}
constexpr int XOR1 = 0xB1, XOR2 = 0x4E, HMIRROR = 0x141, ROR8 = 0x128;
__device__ __forceinline__ float x16_a(float x, float& o) {
  auto s = __builtin_amdgcn_permlane16_swap(__float_as_uint(x), __float_as_uint(x), false, false);
  o = __uint_as_float(s[1]);
  return __uint_as_float(s[0]);
}
__device__ __forceinline__ float x32_a(float x, float& o) {
  auto s = __builtin_amdgcn_permlane32_swap(__float_as_uint(x), __float_as_uint(x), false, false);
  o = __uint_as_float(s[1]);
  return __uint_as_float(s[0]);
}
__device__ __forceinline__ float wave_max(float x) {
  x = fmaxf(x, dpp<XOR1>(x)); x = fmaxf(x, dpp<XOR2>(x)); x = fmaxf(x, dpp<HMIRROR>(x)); x = fmaxf(x, dpp<ROR8>(x));
  float o, a = x16_a(x, o); x = fmaxf(a, o);
  a = x32_a(x, o); return fmaxf(a, o);
}
__device__ __forceinline__ float wave_sum(float x) {
  x += dpp<XOR1>(x); x += dpp<XOR2>(x); x += dpp<HMIRROR>(x); x += dpp<ROR8>(x);
  float o, a = x16_a(x, o); x = a + o;
  a = x32_a(x, o); return a + o;
}
__device__ __forceinline__ void lds_wait_ge(volatile LAS unsigned* f, unsigned want) {
#pragma nounroll
  for (unsigned n = 0; n < (1u << 22); ++n) {
    if (__builtin_amdgcn_readfirstlane(*f) >= want) break;
    __builtin_amdgcn_s_sleep(1);
  }
  MEM_FENCE();
}
__device__ __forceinline__ unsigned bperm(unsigned v, int srclane) { return __builtin_amdgcn_ds_bpermute(srclane << 2, v); }
__device__ __forceinline__ void lds_inc(LAS unsigned char* L, int f, int lane) {
  if (lane == 0) asm volatile("ds_add_u32 %0, %1" ::"v"((unsigned)(uintptr_t)(L + f * 4)), "v"(1u) : "memory");
}
__device__ __forceinline__ unsigned lds_inc_ret(LAS unsigned char* L, int f, int lane) {
  unsigned old = 0;
  if (lane == 0) asm volatile("ds_add_rtn_u32 %0, %1, %2\n\ts_waitcnt lgkmcnt(0)" : "=&v"(old) : "v"((unsigned)(uintptr_t)(L + f * 4)), "v"(1u) : "memory");
  return __builtin_amdgcn_readfirstlane(old);
}
__device__ __forceinline__ unsigned get_tag(volatile LAS unsigned* flag) {
  lds_wait_ge(flag + 0, 1u);
  return __builtin_amdgcn_readfirstlane(flag[1]);
}
__device__ __forceinline__ void dma16(__amdgpu_buffer_rsrc_t r, int voff, unsigned ldsdst) {
  unsigned keep;
  asm volatile("s_mov_b32 %0, m0\n\ts_mov_b32 m0, %3\n\ts_nop 0\n\tbuffer_load_dwordx4 %1, %2, 0 offen" WNT " lds\n\ts_mov_b32 m0, %0"
               : "=&s"(keep) : "v"(voff), "s"(r), "s"(ldsdst) : "memory");
}
__device__ __forceinline__ void dma16p(__amdgpu_buffer_rsrc_t r, int voff, unsigned ldsdst) {
  unsigned keep;
  asm volatile("s_mov_b32 %0, m0\n\ts_mov_b32 m0, %3\n\ts_nop 0\n\tbuffer_load_dwordx4 %1, %2, 0 offen lds\n\ts_mov_b32 m0, %0"
               : "=&s"(keep) : "v"(voff), "s"(r), "s"(ldsdst) : "memory");
}
template <int N>
__device__ __forceinline__ void gatherm(u32x4 (&a)[N], int (&off)[N], unsigned tag, __amdgpu_buffer_rsrc_t r) {
#pragma nounroll
  for (unsigned n = 0; n < SPIN_CAP; ++n) {
    MEM_FENCE();
    u32x4 v[N];
#pragma unroll
    for (int i = 0; i < N; ++i) v[i] = LD128(r, off[i], LDAUX);
    bool all = true;
#pragma unroll
    for (int i = 0; i < N; ++i) {
      const bool mine = off[i] != OOB, ok = v[i][1] == tag && v[i][3] == tag;
      a[i][0] = mine ? v[i][0] : a[i][0];
      a[i][2] = mine ? v[i][2] : a[i][2];
      off[i] = ok ? OOB : off[i];
      all = all && (ok || !mine);
    }
    if (__all(all)) break;
  }
}
template <int STEP>
__device__ __forceinline__ void pass4(u32x4 (&a)[4], const u64 (&m)[4], int off, __amdgpu_buffer_rsrc_t r) {
  asm volatile(
      "s_mov_b64 exec, %[m0]\n\tbuffer_load_dwordx4 %[a0], %[o], %[r], 0 offen sc1\n\t"
      "s_mov_b64 exec, %[m1]\n\tbuffer_load_dwordx4 %[a1], %[o], %[r], 0 offen offset:%c[s1] sc1\n\t"
      "s_mov_b64 exec, %[m2]\n\tbuffer_load_dwordx4 %[a2], %[o], %[r], 0 offen offset:%c[s2] sc1\n\t"
      "s_mov_b64 exec, %[m3]\n\tbuffer_load_dwordx4 %[a3], %[o], %[r], 0 offen offset:%c[s3] sc1\n\t"
      "s_mov_b64 exec, -1\n\ts_waitcnt vmcnt(0)"
      : [a0] "+v"(a[0]), [a1] "+v"(a[1]), [a2] "+v"(a[2]), [a3] "+v"(a[3])
      : [m0] "s"(m[0]), [m1] "s"(m[1]), [m2] "s"(m[2]), [m3] "s"(m[3]), [o] "v"(off), [r] "s"(r), [s1] "i"(STEP), [s2] "i"(2 * STEP), [s3] "i"(3 * STEP)
      : "memory");
}
__device__ __forceinline__ bool missing4(const u32x4 (&a)[4], u64 (&m)[4], unsigned tag) {
#pragma unroll
  for (int i = 0; i < 4; ++i) m[i] &= __builtin_amdgcn_ballot_w64(a[i][1] != tag) | __builtin_amdgcn_ballot_w64(a[i][3] != tag);
  return (m[0] | m[1] | m[2] | m[3]) != 0ull;
}
struct Sel { unsigned lo[4], hi[4]; };
__device__ __forceinline__ Sel make_sel(int lane) {
  Sel s;
  const int j = lane & 7;
#pragma unroll
  for (int t = 0; t < 4; ++t) {
    const bool on = (j >> 1) == t;
    s.lo[t] = on ? ((j & 1) ? 0x01000c0cu : 0x0c0c0100u) : 0x0c0c0c0cu;
    s.hi[t] = on ? ((j & 1) ? 0x03020c0cu : 0x0c0c0302u) : 0x0c0c0c0cu;
  }
  return s;
}
__device__ __forceinline__ u32x4 place(unsigned src, const unsigned (&sel)[4]) {
  u32x4 r;
#pragma unroll
  for (int t = 0; t < 4; ++t) r[t] = __builtin_amdgcn_perm(src, src, sel[t]);
  return r;
}

#define STAMP(i) do { SCHED_FENCE(); st[i] = __builtin_amdgcn_s_memrealtime(); SCHED_FENCE(); } while (0)

#define KSTAMP_OUT() do { STAMP(7); if (wave == 1 && bid == 8) { const unsigned long long d = 3000 + (st[0] - st[0]) * 8; \
    while (__builtin_amdgcn_s_memrealtime() - st[0] < d) __builtin_amdgcn_s_sleep(1); } } while (0)
#define KA0 "s36"
#define KA1 "s37"
#define KA6 "s42"
#define KA7 "s43"
#define KB23 "s[54:55]"
#define RT "s[56:59]"
#define RT01 "s[56:57]"
#define RT0 "s56"
#define RT1 "s57"
#define RT2 "s58"
#define RT3 "s59"
__global__ void __launch_bounds__(1024) fused_kernel(const void*, const void*, const void*, const void*, const void*, const void*, const void*,
                                                     const void*, void*, void*) {
  __shared__ __attribute__((aligned(16))) unsigned char smem[L_END];
  LAS unsigned char* L = (LAS unsigned char*)smem;
  volatile LAS unsigned* flag = (volatile LAS unsigned*)(L + L_FLAG);
  unsigned long long st[8] = {0, 0, 0, 0, 0, 0, 0, 0};
  STAMP(0);
  const int bid = blockIdx.x, tid = threadIdx.x, lane = tid & 63;
  u32x16 ka;
  u32x4 kb, rt;
  unsigned epochv = 0, wave, t0, t1, t2, v0, v1;
  asm volatile(
      "8:\n\ts_load_dwordx16 %[ka], %[kp], 0x0\n\ts_load_dwordx4 %[kb], %[kp], 0x40\n\t"
      "v_readfirstlane_b32 %[w], %[tid]\n\ts_lshr_b32 %[w], %[w], 6\n\t"
      "s_cmp_lt_u32 %[w], 8\n\ts_cbranch_scc1 1f\n\t"
      "s_add_u32 %[t0], %[bid], 1\n\ts_and_b32 %[t0], %[t0], 3\n\t"
      "s_lshl_b32 %[t2], %[w], 11\n\ts_add_u32 m0, %[t2], %[lx]\n\t"
      "s_cmp_eq_u32 %[w], 8\n\ts_cbranch_scc1 5f\n\t"
      "v_lshrrev_b32 %[v0], 3, %[lane]\n\tv_and_b32 %[v1], 7, %[lane]\n\tv_lshlrev_b32 %[v0], 13, %[v0]\n\tv_lshl_or_b32 %[v0], %[v1], 4, %[v0]\n\t"
      "s_mul_i32 %[t0], %[t0], 0xe0000\n\ts_lshl_b32 %[t1], %[w], 17\n\ts_add_u32 %[t0], %[t0], %[t1]\n\t"
      "s_lshl_b32 %[t1], %[bid], 4\n\ts_and_b32 %[t1], %[t1], 0xf80\n\ts_add_u32 %[t0], %[t0], %[t1]\n\t"
      "s_lshl_b32 %[t1], %[bid], 10\n\ts_and_b32 %[t1], %[t1], 0x1000\n\ts_add_u32 %[t0], %[t0], %[t1]\n\t"
      "s_sub_u32 %[t0], %[t0], 0x120000\n\ts_add_u32 %[t1], %[t0], 0x10000\n\t"
      "s_mov_b32 " RT2 ", 0x380000\n\ts_mov_b32 " RT3 ", 0x20000\n\t"
      "s_waitcnt lgkmcnt(0)\n\ts_mov_b32 " RT0 ", " KA6 "\n\ts_and_b32 " RT1 ", " KA7 ", 0xffff\n\t"
      "s_branch 6f\n"
      "5:\n\t"
      "v_lshrrev_b32 %[v0], 4, %[lane]\n\tv_and_b32 %[v1], 15, %[lane]\n\tv_min_u32 %[v1], 13, %[v1]\n\tv_mul_u32_u24 %[v0], 0x380, %[v0]\n\tv_lshl_add_u32 %[v0], %[v1], 4, %[v0]\n\t"
      "s_mul_i32 %[t0], %[t0], 0xe0\n\tv_add_u32 %[v0], %[t0], %[v0]\n\ts_mov_b32 %[t0], 0\n\ts_movk_i32 %[t1], 0xe00\n\t"
      "s_movk_i32 " RT2 ", 0x1c00\n\ts_mov_b32 " RT3 ", 0x20000\n\t"
      "s_waitcnt lgkmcnt(0)\n\ts_mov_b32 " RT0 ", " KA0 "\n\ts_and_b32 " RT1 ", " KA1 ", 0xffff\n"
      "6:\n\t"
      "buffer_load_dwordx4 %[v0], " RT ", %[t0] offen nt lds\n\t"
      "s_add_u32 m0, m0, 0x400\n\ts_nop 0\n\tbuffer_load_dwordx4 %[v0], " RT ", %[t1] offen nt lds\n\t"
      "s_barrier\n\ts_branch 4f\n"
      "1:\n\ts_cmp_eq_u32 %[w], 0\n\ts_cbranch_scc1 2f\n\t"
      "s_cmp_lt_u32 %[w], 5\n\ts_cbranch_scc1 4f\n\t"
      "s_cmp_eq_u32 %[w], 7\n\ts_cbranch_scc1 3f\n\t"
      "s_cmp_lt_u32 %[bid], 192\n\ts_cbranch_scc1 4f\n\t"
      "s_waitcnt lgkmcnt(0)\n\ts_barrier\n\ts_branch 4f\n"
      "3:\n\t"
      "v_lshlrev_b32 %[v0], 2, %[lane]\n\tds_write_b32 %[v0], %[e]\n\t"
      "s_getpc_b64 " RT01 "\n7:\n\ts_sub_u32 " RT0 ", " RT0 ", 7b-8b\n\ts_subb_u32 " RT1 ", " RT1 ", 0\n\ts_and_b32 " RT0 ", " RT0 ", 0xffffff00\n\ts_and_b32 " RT1 ", " RT1 ", 0xffff\n\t"
      "s_mov_b32 " RT2 ", %[cl]\n\ts_mov_b32 " RT3 ", 0x20000\n\ts_mov_b32 m0, %[lc]\n\t"
      "s_lshr_b32 %[t0], %[bid], 3\n\ts_lshl_b32 %[t0], %[t0], 10\n\tv_lshlrev_b32 %[v0], 4, %[lane]\n\tv_add_u32 %[v0], %[t0], %[v0]\n\t"
      "buffer_load_dwordx4 %[v0], " RT ", 0 offen lds\n\t"
      "s_waitcnt lgkmcnt(0)\n\ts_barrier\n\ts_branch 4f\n"
      "2:\n\ts_waitcnt lgkmcnt(0)\n\tglobal_load_dword %[e], %[e], " KB23 " sc1\n\ts_barrier\n\ts_waitcnt vmcnt(0)\n"
      "4:"
      : [ka] "={s[36:51]}"(ka), [kb] "={s[52:55]}"(kb), [rt] "={s[56:59]}"(rt), [w] "=&s"(wave), [t0] "=&s"(t0), [t1] "=&s"(t1), [t2] "=&s"(t2),
        [v0] "=&v"(v0), [v1] "=&v"(v1), [e] "+v"(epochv)
      : [kp] "{s[0:1]}"(__builtin_amdgcn_kernarg_segment_ptr()), [bid] "{s2}"(bid), [tid] "v"(tid), [lane] "v"(lane), [lx] "i"(L_X - 8 * 2048),
        [lc] "i"(L_WO), [cl] "i"(CODE_BYTES)
      : "memory", "scc");
#define WINDOW(p) (((p) + 0x3fffffull) & ~0x3fffffull)
#define ARG(v, i) ((u64)v[2 * (i)] | ((u64)v[2 * (i) + 1] << 32))
  const int xc = bid & 7, jj = bid >> 3, kvh = jj & 7, s = jj >> 3, b = (xc - 2 * s) & 7;
  const int nkw = s == 3 ? 4 : 6;
  unsigned tag = 0;

  if (__builtin_expect(wave - 1u < (unsigned)nkw, 1)) {
    const bool four = s == 3 && wave >= 3;
    const int urel = four && wave == 4 ? 16 : (wave - 1) * 6;
    const unsigned hsh = (((unsigned)bid * 8u + wave) * 2654435761u) >> 24;
    unsigned ord = (unsigned)((four ? 0x3210321032ull : 0x543210543210ull) >> (((hsh * (four ? 4u : 6u)) >> 8) * 4u));
    int klo = (lane & 7) * 512 + ((lane >> 3) & 1) * 64 + (lane >> 4) * 16 + (s * 36 + urel) * 4096;
    unsigned kvoff = (unsigned)(((b * HKV + kvh) * SK + (SK - WIN)) * DH * 2);
    unsigned brot = (wave + jj + xc) * 128u;
    u32x16 kak = ka;
    u32x4 kbk = kb;
    asm volatile("s_waitcnt lgkmcnt(0)\n\ts_barrier" : "+{s[36:51]}"(kak), "+{s[52:55]}"(kbk), "+v"(klo), "+s"(kvoff), "+s"(ord), "+s"(brot)::"memory");
    const auto rk = RSRC(ARG(kak, 1) + kvoff, WIN * DH * 2);
    u32x4 r[24];
    STAMP(4);
#pragma unroll
    for (int i = 0; i < 24; ++i) {
      if (i >= WK0) vmwait<WK - 1>();
      if (i == WK0) STAMP(5);
      r[i] = __builtin_amdgcn_raw_buffer_load_b128(rk, i < 16 || !four ? klo : OOB, ((ord >> (i & ~3)) & 15u) * 4096 + ((brot + i * 128) & 0x180), KAUX);
      SCHED_FENCE();
      if (i == KTRIG) lds_inc(L, F_KISS, lane);
    }
    STAMP(1);
    const u64 ws = WINDOW(ARG(kbk, 1));
    const auto rws = RSRC(ws, WS_END);
    const int kw = wave - 1, nu = four ? 4 : 6, ulo = s * 36, uhi = s == 3 ? 128 : ulo + 36, u0 = ulo + urel;
    __builtin_amdgcn_s_setprio(2);
    lds_wait_ge(flag + F_QRDY, 1u);
    tag = __builtin_amdgcn_readfirstlane(flag[F_TAG]);
    STAMP(2);
    u32x4 qa[4];
#pragma unroll
    for (int t = 0; t < 4; ++t) qa[t] = *(const LAS u32x4*)(L + L_Q + (lane & 1) * 512 + ((brot + t * 128) & 0x180) + ((lane >> 1) & 1) * 64 + (lane >> 4) * 16);
    float s0 = 0.f, s1 = 0.f;
#pragma unroll
    for (int u = 0; u < 6; ++u) {
      f32x4 c = {0, 0, 0, 0};
#pragma unroll
      for (int t = 0; t < 4; ++t) c = mfma(qa[t], r[4 * u + t], c);
      float c0 = (lane & 8) ? c[2] : c[0], c1 = (lane & 8) ? c[3] : c[1];
      c0 += dpp<ROR8>(c0); c1 += dpp<ROR8>(c1);
      if ((unsigned)(lane >> 3) == ((ord >> (4 * u)) & 15u) && (u < 4 || !four)) { s0 = c0; s1 = c1; }
    }
    const unsigned pk = pk_bf16(s0, s1);
    const unsigned pa = bperm(pk, (2 * lane) & 63), pb = bperm(pk, (2 * lane + 1) & 63);
    if (lane < 4 * nu) {
      ST128((u32x4{pa, tag, pb, tag}), rws, WS_S + (b * 8 + kvh) * 8192 + u0 * 64 + lane * 16, STAUX);
      *(LAS u32x4*)(L + L_SM + urel * 64 + lane * 16) = u32x4{pa, tag, pb, tag};
    }
    MEM_FENCE();
    lds_inc(L, F_KCNT, lane);
    STAMP(3);
    if (wave <= 4) {
      u32x4 sg[2];
      int of[2];
      bool own[2];
#pragma unroll
      for (int i = 0; i < 2; ++i) {
        const int myu = (kw + 4 * i) * 16 + (lane >> 2);
        own[i] = myu >= ulo && myu < uhi;
        of[i] = own[i] ? OOB : WS_S + (b * 8 + kvh) * 8192 + (kw + 4 * i) * 1024 + lane * 16;
      }
      __builtin_amdgcn_s_sleep(SSLEEP);
      gatherm<2>(sg, of, tag, rws);
      lds_wait_ge(flag + F_KCNT, (unsigned)nkw);
#pragma unroll
      for (int i = 0; i < 2; ++i) {
        const int c = kw + 4 * i;
        const u32x4 mv = *(const LAS u32x4*)(L + L_SM + (own[i] ? (c * 16 - ulo) * 64 + lane * 16 : 0));
        const unsigned sa = own[i] ? mv[0] : sg[i][0], sb = own[i] ? mv[2] : sg[i][2];
        const float m0 = wave_max(fmaxf(bf_lo(sa), bf_lo(sb))), m1 = wave_max(fmaxf(bf_hi(sa), bf_hi(sb)));
        const float n0 = m0 == -INFINITY ? 0.f : m0, n1 = m1 == -INFINITY ? 0.f : m1;
        const float ea = __builtin_amdgcn_exp2f((bf_lo(sa) - n0) * C2), eb = __builtin_amdgcn_exp2f((bf_lo(sb) - n0) * C2);
        const float ec = __builtin_amdgcn_exp2f((bf_hi(sa) - n1) * C2), ed = __builtin_amdgcn_exp2f((bf_hi(sb) - n1) * C2);
        *(LAS unsigned*)(L + L_P + c * 512 + lane * 4) = pk_bf16(ea, eb);
        *(LAS unsigned*)(L + L_P + c * 512 + 256 + lane * 4) = pk_bf16(ec, ed);
        const float l0 = wave_sum(ea + eb), l1 = wave_sum(ec + ed);
        if (lane == 0) *(LAS f32x4*)(L + L_ST + c * 16) = f32x4{m0, l0, m1, l1};
      }
      MEM_FENCE();
      lds_inc(L, F_PR + kw, lane);
      lds_inc(L, F_SCNT, lane);
      STAMP(6);
    }
    KSTAMP_OUT();
    return;
  }
  const GAS u16* x = (const GAS u16*)ARG(ka, 0);
  const u64 vc = ARG(ka, 2), wo = ARG(ka, 4), w1 = ARG(ka, 5), w2 = ARG(ka, 6), ws = WINDOW(ARG(kb, 1));
  const GAS u16* gw = (const GAS u16*)ARG(ka, 7);
  GAS u16* out = (GAS u16*)ARG(kb, 0);
  const auto rws = RSRC(ws, WS_END);
  const int cb = jj + 32 * (xc >> 2), rb = (xc + 1) & 3;
  const int grp = kvh * 4 + s;
  if (wave >= 8) {
    const unsigned kvoff = (unsigned)(((b * HKV + kvh) * SK + (SK - WIN)) * DH * 2);
    const int w = wave - 8;
    vmwait<0>();
    lds_inc(L, F_WQC, lane);
    tag = get_tag(flag);
    if (w >= 4) {
      __builtin_amdgcn_s_setprio(3);
      const int ct = w - 4;
      const int xrow = ((lane & 7) >> 1) + 4 * (lane & 1);
      LAS unsigned char* xp = L + L_X + xrow * 256 + (lane >> 4) * 16;
      LAS unsigned char* xp2 = L + L_X + xrow * 256 + 192 + (lane >> 4) * 8;
      LAS unsigned char* tb = L + L_WQ + ((lane >> 4) * 8 + ((lane >> 2) & 3)) * 128 + (lane & 3) * 8 + ct * 32;
      LAS unsigned char* tb2 = L + L_WQ + (96 + (lane >> 4) * 4 + ((lane >> 2) & 3)) * 128 + (lane & 3) * 8 + ct * 32;
      const int dest = ((lane >> 4) & 1) * 2 + (lane >> 5);
      const int qo = WS_QP + (cb * 4 + dest) * QPSTR + rb * 1024 + ct * 256 + (lane & 15) * 16;
      lds_wait_ge(flag + F_WQC, 8u);
      STAMP(1);
      const u32x2 pt = trd(tb2), xb = *(const LAS u32x2*)xp2;
      f32x4 acc = mfma(u32x4{xb[0], xb[1], 0u, 0u}, u32x4{pt[0], pt[1], 0u, 0u}, f32x4{0, 0, 0, 0});
#pragma unroll
      for (int ks = 0; ks < 3; ++ks) {
        const u32x2 p0 = trd(tb + ks * 4096), p1 = trd(tb + ks * 4096 + 512);
        acc = mfma(*(const LAS u32x4*)(xp + ks * 64), u32x4{p0[0], p0[1], p1[0], p1[1]}, acc);
      }
      const float q0 = lane >= 32 ? acc[2] : acc[0], q1 = lane >= 32 ? acc[3] : acc[1];
      if (dest != rb) ST128((u32x4{__float_as_uint(q0), tag, __float_as_uint(q1), tag}), rws, qo, STAUX);
      else *(LAS f32x2*)(L + L_QO + (ct * 16 + (lane & 15)) * 8) = f32x2{q0, q1};
      LGKM0();
      lds_inc(L, F_QPC, lane);
      __builtin_amdgcn_s_setprio(0);
    }
    const auto rv = RSRC(vc + kvoff, WIN * DH * 2);
    u32x4 r[16];
    const int trot = w + jj + xc;
    const int voff = (w * 128 + (lane >> 4) * 8 + ((lane >> 3) & 1)) * 512 + s * 128 + (lane & 7) * 16;
    const Sel sel = make_sel(lane);
    lds_wait_ge(flag + F_KISS, (unsigned)nkw);
    STAMP(2);
#define V_ISSUE(i) do { if ((i) > VSPLIT) vmwait<WVE - 1>(); else if ((i) >= WV) { if (S3W && ((S3W == 2) || ((i) & 1)) && s == 3) vmwait<WV>(); else vmwait<WV - 1>(); } \
      r[i] = __builtin_amdgcn_raw_buffer_load_b128(rv, voff + ((i) & 3) * 1024, ((((i) >> 2) + trot) & 3) * 16384, VAUX); SCHED_FENCE(); \
      if ((i) == VTRIG) lds_inc(L, F_VISS, lane); } while (0)
#define V_USE(i) do { a0 = mfma(place(pp[(i) >> 2][(i) & 3], sel.lo), r[i], a0); a1 = mfma(place(pp[(i) >> 2][(i) & 3], sel.hi), r[i], a1); } while (0)
#pragma unroll
    for (int i = 0; i <= VSPLIT; ++i) V_ISSUE(i);
    STAMP(3);
    u32x4 pp[4];
    f32x4 a0 = {0, 0, 0, 0}, a1 = {0, 0, 0, 0};
#define P_READ() _Pragma("unroll") for (int t = 0; t < 4; ++t) \
      pp[t] = *(const LAS u32x4*)(L + L_P + w * 512 + ((lane >> 3) & 1) * 256 + (((t + trot) & 3) * 32 + (lane >> 4) * 8) * 2)
    float f;
    unsigned wad = (unsigned)(uintptr_t)(L + L_OP) + w * 512 + ((lane >> 5) * 64 + (lane & 7) * 8 + ((lane >> 4) & 1) * 4) * 4;
    unsigned rad = (unsigned)(uintptr_t)(L + L_OP) + (lane >> 5) * 2048 + (lane & 31) * 16;
    int oad = WS_OB + inbox(grp) + b * 512 + lane * 16;
#define F_GET() do { lds_wait_ge(flag + F_FRDY, 1u); f = *(const LAS float*)(L + L_FW + w * 8 + (lane >> 5) * 4); \
      asm volatile("" : "+v"(f), "+v"(wad), "+v"(rad), "+v"(oad)); } while (0)
    if (__builtin_expect(__builtin_amdgcn_readfirstlane(flag[F_PR + (w & 3)]) != 0u, 1)) {
      MEM_FENCE();
      STAMP(4);
      P_READ();
      constexpr int PER = VPER;
#pragma unroll
      for (int i = VSPLIT + 1; i < 16; ++i) {
        V_ISSUE(i);
#pragma unroll
        for (int k = 0; k < 16; ++k)
          if (k >= (i - VSPLIT - 1) * PER && k < (i - VSPLIT) * PER && k <= i - WVE) V_USE(k);
        SCHED_FENCE();
      }
#pragma unroll
      for (int k = 0; k < 16 - WVE; ++k) {
        bool done = false;
#pragma unroll
        for (int i = VSPLIT + 1; i < 16; ++i) done = done || (k >= (i - VSPLIT - 1) * PER && k < (i - VSPLIT) * PER && k <= i - WVE);
        if (!done) { V_USE(k); if (k & 1) SCHED_FENCE(); }
      }
      SCHED_FENCE();
      F_GET();
      u32x4 la[WVE], lb[WVE];
#pragma unroll
      for (int k = 0; k < WVE; ++k) {
        la[k] = place(pp[(16 - WVE + k) >> 2][(16 - WVE + k) & 3], sel.lo);
        lb[k] = place(pp[(16 - WVE + k) >> 2][(16 - WVE + k) & 3], sel.hi);
        asm volatile("" : "+v"(la[k]), "+v"(lb[k]));
      }
      SCHED_FENCE();
#pragma unroll
      for (int k = 0; k < WVE; ++k) { a0 = mfma(la[k], r[16 - WVE + k], a0); a1 = mfma(lb[k], r[16 - WVE + k], a1); }
    } else {
#pragma unroll
      for (int i = VSPLIT + 1; i < 16; ++i) V_ISSUE(i);
      STAMP(6);
      lds_wait_ge(flag + F_PR + (w & 3), 1u);
      P_READ();
#pragma unroll
      for (int k = 0; k < 16; ++k) { V_USE(k); if (k & 1) SCHED_FENCE(); }
      F_GET();
    }
    f32x4 t = (lane & 8) ? a1 : a0;
#pragma unroll
    for (int k = 0; k < 4; ++k) t[k] = (t[k] + dpp<ROR8>(t[k])) * f;
    STAMP(5);
    unsigned old;
    f32x4 o0v, o1v, o2v, o3v;
    asm volatile("ds_write_b128 %[wa], %[t]\n\tds_add_rtn_u32 %[old], %[ca], %[one]\n\t"
                 "ds_read_b128 %[p0], %[ra]\n\tds_read_b128 %[p1], %[ra] offset:512\n\tds_read_b128 %[p2], %[ra] offset:1024\n\tds_read_b128 %[p3], %[ra] offset:1536\n\t"
                 "s_waitcnt lgkmcnt(0)"
                 : [old] "=&v"(old), [p0] "=&v"(o0v), [p1] "=&v"(o1v), [p2] "=&v"(o2v), [p3] "=&v"(o3v)
                 : [wa] "v"(wad), [t] "v"(t), [ca] "v"((unsigned)(uintptr_t)(L + (lane == 0 ? F_OCNT * 4 : 128 + (lane & 31) * 4))), [one] "v"(1u), [ra] "v"(rad)
                 : "memory");
    if (__builtin_amdgcn_readfirstlane(old) == 7u) {
      f32x4 ov = (o0v + o1v) + (o2v + o3v);
#pragma unroll
      for (int k = 0; k < 4; ++k) { float hi, lo = x32_a(ov[k], hi); ov[k] = lo + hi; }
      if (lane < 32) {
        const u32x4 ogr = {pk_bf16(ov[0], ov[1]), tag, pk_bf16(ov[2], ov[3]), tag};
        ST128(ogr, rws, oad, STAUX);
        ST128(ogr, rws, oad + 8192, STAUX);
        *(LAS u32x4*)(L + L_OM + lane * 16) = ogr;
      }
      MEM_FENCE();
      lds_inc(L, F_OMR, lane);
    }
  } else if (wave == 0) {
    __builtin_amdgcn_s_setprio(3);
    tag = __builtin_amdgcn_readfirstlane(epochv) * 1664525u + 1013904223u;
    if (lane == 0) *(volatile LAS u32x2*)flag = u32x2{1u, tag};
    lds_wait_ge(flag + F_QPC, 4u);
    STAMP(2);
    __builtin_amdgcn_s_sleep(QSLEEP1);
    {
      const int po = WS_QP + (cb * 4 + rb) * QPSTR + lane * 16;
      u32x4 pg[3];
      int of[3] = {po + ((rb + 1) & 3) * 1024, po + ((rb + 2) & 3) * 1024, po + ((rb + 3) & 3) * 1024};
      gatherm<3>(pg, of, tag, rws);
      const f32x2 own = *(const LAS f32x2*)(L + L_QO + lane * 8);
      const float a0 = (own[0] + __uint_as_float(pg[0][0])) + (__uint_as_float(pg[1][0]) + __uint_as_float(pg[2][0]));
      const float a1 = (own[1] + __uint_as_float(pg[0][2])) + (__uint_as_float(pg[1][2]) + __uint_as_float(pg[2][2]));
      const float n0 = dpp<XOR1>(a0), n1 = dpp<XOR1>(a1);
      const unsigned pk = (lane & 1) ? pk_bf16(n1, a1) : pk_bf16(a0, n0);
      const int src = (lane & 15) * 4 + ((lane >> 4) & 1);
      const unsigned pa = bperm(pk, src), pb = bperm(pk, src + 2);
      if (lane < 32) ST128((u32x4{pa, tag, pb, tag}), rws, WS_Q + ((cb >> 3) * 8 + rb + 4 * (lane >> 4)) * QSTR + (cb & 7) * 256 + (lane & 15) * 16, STAUX);
    }
    STAMP(6);
    __builtin_amdgcn_s_sleep(QSLEEP2);
    {
      const int qoff = WS_Q + (kvh * 8 + b) * QSTR + lane * 16;
      u32x4 qq[2];
      int of[2] = {qoff, qoff + 1024};
      gatherm<2>(qq, of, tag, rws);
      *(LAS u32x2*)(L + L_Q + lane * 8) = u32x2{qq[0][0], qq[0][2]};
      *(LAS u32x2*)(L + L_Q + 512 + lane * 8) = u32x2{qq[1][0], qq[1][2]};
      LGKM0();
      if (lane == 0) flag[F_QRDY] = 1u;
    }
    STAMP(3);
    u32x4 xg = {0, 0, 0, 0}, gg = {0, 0, 0, 0};
    if (lane < 56) {
      xg = *(const GAS u32x4*)(x + b * DM + lane * 8);
      gg = *(const GAS u32x4*)(gw + lane * 8);
    }
    float z = 0.f;
#pragma unroll
    for (int t = 0; t < 4; ++t) z += rbf(bf_lo(xg[t]) * bf_lo(gg[t])) + rbf(bf_hi(xg[t]) * bf_hi(gg[t]));
    z = rbf(wave_sum(z));
    const float g = rbf(1.f / (1.f + __expf(-z)));
    const unsigned g1u = __float_as_uint(rbf(1.f - g));
    if (lane == 0) *(volatile LAS u32x2*)(flag + F_G1V) = u32x2{1u, g1u};
    lds_wait_ge(flag + F_SCNT, 4u);
    {
      const f32x2 ml = *(const LAS f32x2*)(L + L_ST + (lane & 15) * 8);
      float M = fmaxf(ml[0], dpp<XOR2>(ml[0]));
      M = fmaxf(M, dpp<0x124>(M)); M = fmaxf(M, dpp<ROR8>(M));
      const float al = __builtin_amdgcn_exp2f((ml[0] - M) * C2);
      float Ls = al * ml[1];
      Ls += dpp<XOR2>(Ls); Ls += dpp<0x124>(Ls); Ls += dpp<ROR8>(Ls);
      if (lane < 16) *(LAS float*)(L + L_FW + lane * 4) = al * (g / Ls);
      LGKM0();
      if (lane == 0) flag[F_FRDY] = 1u;
    }
    STAMP(4);
  } else if (wave != 7 && xc < 7) {
    const int j = wave - 5, t = xc * 8 + kvh;
    const unsigned lb = (unsigned)(uintptr_t)(L + L_T) + j * 14336;
    const auto r1 = RSRC(w1, DM * FF * 2);
    const int o = ((j * 224 + (lane >> 2)) * FF + t * 32 + (lane & 3) * 8) * 2;
#pragma unroll
    for (int i = 0; i < 14; ++i) {
      if (i >= WT) vmwait<WT - 1>();
      if (W1P) dma16p(r1, o + i * 16 * FF * 2, lb + i * 1024); else dma16(r1, o + i * 16 * FF * 2, lb + i * 1024);
    }
    {
      const auto rx = RSRC((u64)x, NB * DM * 2);
#pragma unroll
      for (int i = 0; i < 4; ++i) {
        vmwait<WT - 1>();
        dma16p(rx, (j * 4 + i) * 1024 + lane * 16, (unsigned)(uintptr_t)(L + L_XA) + (j * 4 + i) * 1024);
      }
    }
    tag = get_tag(flag);
    vmwait<0>();
    lds_inc(L, F_TCNT, lane);
    lds_wait_ge(flag + F_TCNT, 2u);
    STAMP(6);
    LAS unsigned char* tb = L + L_T + ((lane >> 4) * 8 + ((lane >> 2) & 3)) * 64 + j * 32 + (lane & 3) * 8;
    f32x4 acc0 = {0, 0, 0, 0}, acc1 = {0, 0, 0, 0};
#pragma unroll
    for (int ks = 0; ks < 14; ++ks) {
      const u32x2 p0 = trd(tb + ks * 2048), p1 = trd(tb + ks * 2048 + 256);
      const u32x4 xa = *(const LAS u32x4*)(L + L_XA + (lane & 7) * (DM * 2) + ks * 64 + (lane >> 4) * 16);
      if (ks & 1) acc1 = mfma(u32x4{p0[0], p0[1], p1[0], p1[1]}, xa, acc1);
      else acc0 = mfma(u32x4{p0[0], p0[1], p1[0], p1[1]}, xa, acc0);
    }
    acc0 += acc1;
    float hv[4];
#pragma unroll
    for (int k = 0; k < 4; ++k) {
      const float v = rbf(acc0[k]);
      hv[k] = v / (1.f + __expf(-v));
    }
    if (!(lane & 8))
      ST128((u32x4{pk_bf16(hv[0], hv[1]), tag, pk_bf16(hv[2], hv[3]), tag}), rws, WS_HB + t * 1024 + j * 512 + (lane >> 4) * 128 + (lane & 7) * 16, STAUX);
  }
  const int cpair = (xc ^ 1) * 32 + jj;
  if (wave == 7) {
    const int fgrp = b == 7 ? -1 : s < 3 ? kvh * 3 + s : (kvh < 4 ? 24 + kvh : -1);
    tag = get_tag(flag);
    const bool hi8 = lane & 8;
    const unsigned upper = (__builtin_amdgcn_s_getreg(20 | (3 << 11)) >> 2) & 1u;
    if (s == 0) {
      const u64 pa = ws + WS_PR + kvh * 16384 + b * 1024;
      int vote = 0;
#pragma unroll
      for (int i = 0; i < NPAIR; ++i) {
        u64 t0, t1, t2;
        unsigned d;
        asm volatile("s_memtime %0\n\ts_waitcnt lgkmcnt(0)\n\ts_load_dword %3, %4, %5\n\ts_waitcnt lgkmcnt(0)\n\ts_memtime %1\n\ts_waitcnt lgkmcnt(0)\n\t"
                     "s_load_dword %3, %4, %6\n\ts_waitcnt lgkmcnt(0)\n\ts_memtime %2\n\ts_waitcnt lgkmcnt(0)"
                     : "=&s"(t0), "=&s"(t1), "=&s"(t2), "=&s"(d) : "s"(pa), "i"(i * 128), "i"(i * 128 + 8192) : "memory");
        vote += (unsigned)(t1 - t0) > (unsigned)(t2 - t1) ? 1 : -1;
      }
      lds_wait_ge(flag + F_G1V, 1u);
      if (lane < 8) ST128((u32x4{flag[F_G1], tag, (unsigned)(upper ? -vote : vote), tag}), rws, WS_G + (kvh * 8 + b) * 128 + lane * 16, STAUX);
      STAMP(1);
    }
    u32x4 hb[3];
    int nearc;
    {
      const unsigned lb = (unsigned)(uintptr_t)(L + L_WO);
      const auto r2 = RSRC(w2, FF * DM * 2);
      const auto ro = RSRC(wo, NQ * DM * 2);
      const int off2 = (fgrp * 64 + (lane >> 3)) * 896 + b * 128 + (lane & 7) * 16;
      const int offo = (kvh * 512 + s * 64 + (lane >> 3)) * 896 + b * 128 + (lane & 7) * 16;
      lds_wait_ge(flag + HFLAG, HTRIG);
      {
        int of[3] = {fgrp >= 0 ? WS_HB + fgrp * 2048 + lane * 16 : OOB, fgrp >= 0 ? WS_HB + fgrp * 2048 + 1024 + lane * 16 : OOB, WS_G + (kvh * 8 + (lane & 7)) * 128};
        gatherm<3>(hb, of, tag, rws);
        float vs = (float)(int)hb[2][2];
        vs += dpp<XOR1>(vs); vs += dpp<XOR2>(vs); vs += dpp<HMIRROR>(vs);
        nearc = (((__builtin_amdgcn_readfirstlane(__float_as_uint(vs)) >> 31) ^ 1u ^ upper ^ FLIP) & 1u) * 8192;
        if (lane == 0) *(volatile LAS u32x2*)(flag + F_NRV) = u32x2{1u, (unsigned)nearc};
      }
      if (b == 7) { KSTAMP_OUT(); return; }
      lds_wait_ge(flag + F_VISS, 8u);
      STAMP(2);
      if (fgrp >= 0) {
#pragma unroll
        for (int i = 0; i < 8; ++i) dma16(r2, off2 + i * 8 * 896, lb + 16384 + i * 1024);
      }
#pragma unroll
      for (int i = 0; i < 16; ++i) dma16(ro, offo + ((i >> 3) * 256 + (i & 7) * 8) * 896, lb + i * 1024);
      STAMP(3);
    }
    __builtin_amdgcn_s_setprio(3);
    const bool ownb = (lane & 7) == b;
    const int ooff = WS_OB + nearc + inbox(grp) + (lane & 7) * 512 + (lane >> 3) * 16;
    u32x4 ob[4];
#pragma unroll
    for (int i = 0; i < 4; ++i) ob[i] = LD128(rws, ownb ? OOB : ooff + i * 128, LDAUX);
    SCHED_FENCE();
    LAS unsigned char* tb = L + L_WO + ((lane >> 4) * 8 + ((lane >> 2) & 3)) * 128 + (lane & 3) * 8;
    f32x4 acc[4] = {{0, 0, 0, 0}, {0, 0, 0, 0}, {0, 0, 0, 0}, {0, 0, 0, 0}};
    if (fgrp >= 0) {
      vmwait<20>();
      const u32x4 hv = {hb[0][0], hb[0][2], hb[1][0], hb[1][2]};
      const float g1 = __uint_as_float(hb[2][0]);
#pragma unroll
      for (int ks = 0; ks < 2; ++ks) {
        const u32x4 bf = {hi8 ? 0u : hv[2 * ks], hi8 ? 0u : hv[2 * ks + 1], hi8 ? hv[2 * ks] : 0u, hi8 ? hv[2 * ks + 1] : 0u};
#pragma unroll
        for (int ct = 0; ct < 4; ++ct) {
          const u32x2 p0 = trd(tb + 16384 + ks * 4096 + ct * 32), p1 = trd(tb + 16384 + ks * 4096 + 512 + ct * 32);
          acc[ct] = mfma(u32x4{p0[0], p0[1], p1[0], p1[1]}, bf, acc[ct]);
        }
      }
#pragma unroll
      for (int ct = 0; ct < 4; ++ct) acc[ct] *= g1;
    }
    u32x4 wf[4][4];
#pragma unroll
    for (int ks = 0; ks < 4; ++ks) {
      if (ks == 0) vmwait<16>(); else if (ks == 1) vmwait<12>(); else if (ks == 2) vmwait<8>(); else vmwait<4>();
#pragma unroll
      for (int ct = 0; ct < 4; ++ct) {
        const u32x2 p0 = trd(tb + ks * 4096 + ct * 32), p1 = trd(tb + ks * 4096 + 512 + ct * 32);
        wf[ks][ct] = u32x4{p0[0], p0[1], p1[0], p1[1]};
      }
    }
    int po = WS_PO + (nearc ^ (((xc ^ b) >> 2) & 1) * 8192) + inbox(b * 32 + (lane >> 3)) + grp * 128 + (lane & 7) * 16;
    asm volatile("" : "+v"(po));
    STAMP(4);
    lds_wait_ge(flag + F_OMR, 1u);
#pragma unroll
    for (int i = 0; i < 4; ++i) {
      const u32x4 v = *(const LAS u32x4*)(L + L_OM + i * 128 + (lane >> 3) * 16);
      ob[i] = ownb ? v : ob[i];
    }
    u64 ms[4] = {~0ull, ~0ull, ~0ull, ~0ull};
    if (missing4(ob, ms, tag)) {
#pragma nounroll
      for (unsigned n = 0; n < SPIN_CAP; ++n) {
        pass4<128>(ob, ms, ooff, rws);
        if (!missing4(ob, ms, tag)) break;
      }
    }
    STAMP(6);
    u32x4 bf[4];
#pragma unroll
    for (int ks = 0; ks < 4; ++ks) bf[ks] = u32x4{hi8 ? 0u : ob[ks][0], hi8 ? 0u : ob[ks][2], hi8 ? ob[ks][0] : 0u, hi8 ? ob[ks][2] : 0u};
#pragma unroll
    for (int ct = 0; ct < 4; ++ct)
#pragma unroll
      for (int ks = 0; ks < 4; ++ks) acc[ct] = mfma(wf[ks][ct], bf[ks], acc[ct]);
    u32x4 pv[4];
#pragma unroll
    for (int ct = 0; ct < 4; ++ct) {
      const float k0 = hi8 ? acc[ct][2] : acc[ct][0], k1 = hi8 ? acc[ct][3] : acc[ct][1];
      const float s0 = hi8 ? acc[ct][0] : acc[ct][2], s1 = hi8 ? acc[ct][1] : acc[ct][3];
      pv[ct] = u32x4{__float_as_uint(k0 + dpp<ROR8>(s0)), tag, __float_as_uint(k1 + dpp<ROR8>(s1)), tag};
      __builtin_amdgcn_raw_buffer_store_b128(pv[ct], rws, po, ct * 4 * 16384, STAUX);
    }
    STAMP(5);
    po ^= 8192;
#pragma unroll
    for (int ct = 0; ct < 4; ++ct) __builtin_amdgcn_raw_buffer_store_b128(pv[ct], rws, po, ct * 4 * 16384, STAUX);
  } else if (wave == 0 && cpair < 224) {
    __builtin_amdgcn_s_setprio(3);
    lds_wait_ge(flag + F_OCNT, 8u);
    STAMP(5);
    __builtin_amdgcn_s_sleep(FSLEEP);
    lds_wait_ge(flag + F_NRV, 1u);
    const int poff = WS_PO + __builtin_amdgcn_readfirstlane(flag[F_NR]) + inbox(cpair) + lane * 16;
    u32x4 pg[4] = {{0, 0, 0, 0}, {0, 0, 0, 0}, {0, 0, 0, 0}, {0, 0, 0, 0}};
    u64 ms[4] = {~0ull, ~0ull, ~0ull, ~0ull};
    GAS unsigned* op = (GAS unsigned*)(out + (lane & 7) * DM + cpair * 2);
    asm volatile("" : "+v"(op));
#pragma nounroll
    for (unsigned n = 0; n < SPIN_CAP; ++n) {
      pass4<1024>(pg, ms, poff, rws);
      if (!missing4(pg, ms, tag)) {
        float a0 = (__uint_as_float(pg[0][0]) + __uint_as_float(pg[1][0])) + (__uint_as_float(pg[2][0]) + __uint_as_float(pg[3][0]));
        float a1 = (__uint_as_float(pg[0][2]) + __uint_as_float(pg[1][2])) + (__uint_as_float(pg[2][2]) + __uint_as_float(pg[3][2]));
        a0 += dpp<ROR8>(a0); a1 += dpp<ROR8>(a1);
        float o, t;
        t = x16_a(a0, o); a0 = t + o; t = x16_a(a1, o); a1 = t + o;
        t = x32_a(a0, o); a0 = t + o; t = x32_a(a1, o); a1 = t + o;
        if (lane < 8) *op = pk_bf16(a0, a1);
        if (bid == 0 && lane == 0) __hip_atomic_store((GAS unsigned*)ARG(kb, 1), tag, __ATOMIC_RELAXED, __HIP_MEMORY_SCOPE_AGENT);
        STAMP(1);
        break;
      }
    }
  }
  KSTAMP_OUT();
}

extern "C" void kernel_launch(void* const* d_in, const int* in_sizes, int n_in, void* d_out, int out_size, void* d_ws, size_t ws_size,
                              hipStream_t stream) {
  fused_kernel<<<256, 1024, 0, stream>>>(d_in[0], d_in[1], d_in[2], d_in[3], d_in[4], d_in[6], d_in[7], d_in[8], d_out, d_ws);
}
```

```cpp
#include <hip/hip_runtime.h>
#include <hip/hip_bf16.h>
#include <stdint.h>

#define STAMPS 0
#define CODEPF 1
#define CODE_BYTES 19200
#define VAUX 2
#define KAUX 2
#define KLATE 0
#define W1P 0
#define U1 58
#define WNT " nt"
#define WK2 WK
#define WV2 WV
#define WVE WV
#define QSLEEP1 10
#define QSLEEP2 10
#define FSLEEP 10
#define WK 4
#define WV 3
#define WT 3
#define WK0 5
#define HFLAG F_SCNT
#define HTRIG 2u
#define NPAIR 6
#define FLIP 1
#define S3W 0
#define VPER 3
#define SSLEEP 24
#define VSPLIT 10
#define VTRIG 15
#define KTRIG 22

typedef unsigned u32x4 __attribute__((ext_vector_type(4)));
typedef unsigned u32x2 __attribute__((ext_vector_type(2)));
typedef float f32x4 __attribute__((ext_vector_type(4)));
typedef float f32x2 __attribute__((ext_vector_type(2)));
typedef short s16x4 __attribute__((ext_vector_type(4)));
typedef short s16x8 __attribute__((ext_vector_type(8)));
typedef __bf16 bf16x2 __attribute__((ext_vector_type(2)));
typedef __bf16 bf16x8 __attribute__((ext_vector_type(8)));
typedef unsigned short u16;
typedef unsigned u32x16 __attribute__((ext_vector_type(16)));
typedef unsigned long long u64;
#define LAS __attribute__((address_space(3)))
#define GAS __attribute__((address_space(1)))

constexpr int NB = 8, DM = 448, HKV = 8, SK = 4096, DH = 256, WIN = 1024, FF = 1792, NQ = 4096;
constexpr int PSTR = 4096 + 128;
constexpr int QPSTR = 4096 + 256, QSTR = 2048 + 128, HSTR = 256;
constexpr int WS_G = 4096;
constexpr int WS_QP = 16384;
constexpr int WS_Q = WS_QP + 256 * QPSTR;
constexpr int WS_HB = WS_Q + 64 * QSTR;
constexpr int WS_S = WS_HB + 65536;
constexpr int WS_PR = (WS_S + 524288 + 16383) & ~16383;
constexpr int WS_OB = 2 << 20;
constexpr int WS_PO = WS_OB + 16 * 16384;
constexpr int WS_END = WS_PO + 112 * 16384;
static_assert(WS_PR + 8 * 16384 <= WS_OB && WS_END == 4 << 20, "two 2 MB pages");
__device__ __forceinline__ int inbox(int k) { return (k >> 1) * 16384 + (k & 1) * 4096; }
constexpr int WS_STAMP = 8 << 20;
constexpr int L_FLAG = 0, L_Q = 256, L_QO = L_Q + 1024, L_ST = L_QO + 512, L_FW = L_ST + 128, L_OM = L_FW + 64, L_SM = L_OM + 512, L_P = L_SM + 2304, L_OP = L_P + 4096, L_WO = L_OP + 4096, L_W2 = L_WO + 16384,
              L_X = L_W2 + 8192, L_WQ = L_X + 2048, L_T = L_WQ + 14336, L_XA = L_T + 28672, L_END = L_XA + 8192;
enum { F_TAGV = 0, F_TAG = 1, F_QRDY = 2, F_WQC = 3, F_QPC = 4, F_KISS = 5, F_OCNT = 6, F_TCNT = 7, F_VISS = 8, F_SCNT = 9, F_FRDY = 10, F_KCNT = 11, F_OMR = 12, F_G1V = 14, F_G1 = 15, F_PR = 16, F_NRV = 20, F_NR = 21 };
#define LDAUX 16
#define STAUX 16
#define WS_AT_END 0
constexpr int AUX_NT = 2, AUX_SC1 = 16;
constexpr float C2 = 0.0625f * 1.4426950408889634f;
constexpr int OOB = 0x7f000000;
constexpr unsigned SPIN_CAP = 1u << 18;

#define RSRC(p, n) __builtin_amdgcn_make_buffer_rsrc((void*)(p), 0, (n), 0x00020000)
#define LD128(r, off, aux) __builtin_amdgcn_raw_buffer_load_b128((r), (off), 0, (aux))
#define ST128(v, r, off, aux) __builtin_amdgcn_raw_buffer_store_b128((v), (r), (off), 0, (aux))
#define SCHED_FENCE() __builtin_amdgcn_sched_barrier(0)
#define MEM_FENCE() asm volatile("" ::: "memory")
template <int N> __device__ __forceinline__ void vmwait() { asm volatile("s_waitcnt vmcnt(%0)" ::"n"(N) : "memory"); }
#define LGKM0() asm volatile("s_waitcnt lgkmcnt(0)" ::: "memory")

__device__ __forceinline__ f32x4 mfma(u32x4 a, u32x4 b, f32x4 c) {
  return __builtin_amdgcn_mfma_f32_16x16x32_bf16(__builtin_bit_cast(bf16x8, a), __builtin_bit_cast(bf16x8, b), c, 0, 0, 0);
}
__device__ __forceinline__ u32x2 trd(LAS unsigned char* p) {
  return __builtin_bit_cast(u32x2, __builtin_amdgcn_ds_read_tr16_b64_v4i16((LAS s16x4*)p));
}
__device__ __forceinline__ unsigned pk_bf16(float lo, float hi) {
  f32x2 v = {lo, hi};
  return __builtin_bit_cast(unsigned, __builtin_convertvector(v, bf16x2));
}
__device__ __forceinline__ float bf_lo(unsigned u) { return __uint_as_float(u << 16); }
__device__ __forceinline__ float bf_hi(unsigned u) { return __uint_as_float(u & 0xffff0000u); }
__device__ __forceinline__ float rbf(float f) { return bf_lo(pk_bf16(f, f)); }
template <int CTRL> __device__ __forceinline__ float dpp(float x) {
  return __builtin_bit_cast(float, __builtin_amdgcn_mov_dpp(__builtin_bit_cast(int, x), CTRL, 0xf, 0xf, true));
}
constexpr int XOR1 = 0xB1, XOR2 = 0x4E, HMIRROR = 0x141, ROR8 = 0x128;
__device__ __forceinline__ float x16_a(float x, float& o) {
  auto s = __builtin_amdgcn_permlane16_swap(__float_as_uint(x), __float_as_uint(x), false, false);
  o = __uint_as_float(s[1]);
  return __uint_as_float(s[0]);
}
__device__ __forceinline__ float x32_a(float x, float& o) {
  auto s = __builtin_amdgcn_permlane32_swap(__float_as_uint(x), __float_as_uint(x), false, false);
  o = __uint_as_float(s[1]);
  return __uint_as_float(s[0]);
}
__device__ __forceinline__ float wave_max(float x) {
  x = fmaxf(x, dpp<XOR1>(x)); x = fmaxf(x, dpp<XOR2>(x)); x = fmaxf(x, dpp<HMIRROR>(x)); x = fmaxf(x, dpp<ROR8>(x));
  float o, a = x16_a(x, o); x = fmaxf(a, o);
  a = x32_a(x, o); return fmaxf(a, o);
}
__device__ __forceinline__ float wave_sum(float x) {
  x += dpp<XOR1>(x); x += dpp<XOR2>(x); x += dpp<HMIRROR>(x); x += dpp<ROR8>(x);
  float o, a = x16_a(x, o); x = a + o;
  a = x32_a(x, o); return a + o;
}
__device__ __forceinline__ void lds_wait_ge(volatile LAS unsigned* f, unsigned want) {
#pragma nounroll
  for (unsigned n = 0; n < (1u << 22); ++n) {
    if (__builtin_amdgcn_readfirstlane(*f) >= want) break;
    __builtin_amdgcn_s_sleep(1);
  }
  MEM_FENCE();
}
__device__ __forceinline__ unsigned bperm(unsigned v, int srclane) { return __builtin_amdgcn_ds_bpermute(srclane << 2, v); }
__device__ __forceinline__ void lds_inc(LAS unsigned char* L, int f, int lane) {
  if (lane == 0) asm volatile("ds_add_u32 %0, %1" ::"v"((unsigned)(uintptr_t)(L + f * 4)), "v"(1u) : "memory");
}
__device__ __forceinline__ unsigned lds_inc_ret(LAS unsigned char* L, int f, int lane) {
  unsigned old = 0;
  if (lane == 0) asm volatile("ds_add_rtn_u32 %0, %1, %2\n\ts_waitcnt lgkmcnt(0)" : "=&v"(old) : "v"((unsigned)(uintptr_t)(L + f * 4)), "v"(1u) : "memory");
  return __builtin_amdgcn_readfirstlane(old);
}
__device__ __forceinline__ unsigned get_tag(volatile LAS unsigned* flag) {
  lds_wait_ge(flag + 0, 1u);
  return __builtin_amdgcn_readfirstlane(flag[1]);
}
__device__ __forceinline__ void dma16(__amdgpu_buffer_rsrc_t r, int voff, unsigned ldsdst) {
  unsigned keep;
  asm volatile("s_mov_b32 %0, m0\n\ts_mov_b32 m0, %3\n\ts_nop 0\n\tbuffer_load_dwordx4 %1, %2, 0 offen" WNT " lds\n\ts_mov_b32 m0, %0"
               : "=&s"(keep) : "v"(voff), "s"(r), "s"(ldsdst) : "memory");
}
__device__ __forceinline__ void dma16p(__amdgpu_buffer_rsrc_t r, int voff, unsigned ldsdst) {
  unsigned keep;
  asm volatile("s_mov_b32 %0, m0\n\ts_mov_b32 m0, %3\n\ts_nop 0\n\tbuffer_load_dwordx4 %1, %2, 0 offen lds\n\ts_mov_b32 m0, %0"
               : "=&s"(keep) : "v"(voff), "s"(r), "s"(ldsdst) : "memory");
}
template <int N>
__device__ __forceinline__ void gatherm(u32x4 (&a)[N], int (&off)[N], unsigned tag, __amdgpu_buffer_rsrc_t r) {
#pragma nounroll
  for (unsigned n = 0; n < SPIN_CAP; ++n) {
    MEM_FENCE();
    u32x4 v[N];
#pragma unroll
    for (int i = 0; i < N; ++i) v[i] = LD128(r, off[i], LDAUX);
    bool all = true;
#pragma unroll
    for (int i = 0; i < N; ++i) {
      const bool mine = off[i] != OOB, ok = v[i][1] == tag && v[i][3] == tag;
      a[i][0] = mine ? v[i][0] : a[i][0];
      a[i][2] = mine ? v[i][2] : a[i][2];
      off[i] = ok ? OOB : off[i];
      all = all && (ok || !mine);
    }
    if (__all(all)) break;
  }
}
template <int STEP>
__device__ __forceinline__ void pass4(u32x4 (&a)[4], const u64 (&m)[4], int off, __amdgpu_buffer_rsrc_t r) {
  asm volatile(
      "s_mov_b64 exec, %[m0]\n\tbuffer_load_dwordx4 %[a0], %[o], %[r], 0 offen sc1\n\t"
      "s_mov_b64 exec, %[m1]\n\tbuffer_load_dwordx4 %[a1], %[o], %[r], 0 offen offset:%c[s1] sc1\n\t"
      "s_mov_b64 exec, %[m2]\n\tbuffer_load_dwordx4 %[a2], %[o], %[r], 0 offen offset:%c[s2] sc1\n\t"
      "s_mov_b64 exec, %[m3]\n\tbuffer_load_dwordx4 %[a3], %[o], %[r], 0 offen offset:%c[s3] sc1\n\t"
      "s_mov_b64 exec, -1\n\ts_waitcnt vmcnt(0)"
      : [a0] "+v"(a[0]), [a1] "+v"(a[1]), [a2] "+v"(a[2]), [a3] "+v"(a[3])
      : [m0] "s"(m[0]), [m1] "s"(m[1]), [m2] "s"(m[2]), [m3] "s"(m[3]), [o] "v"(off), [r] "s"(r), [s1] "i"(STEP), [s2] "i"(2 * STEP), [s3] "i"(3 * STEP)
      : "memory");
}
__device__ __forceinline__ bool missing4(const u32x4 (&a)[4], u64 (&m)[4], unsigned tag) {
#pragma unroll
  for (int i = 0; i < 4; ++i) m[i] &= __builtin_amdgcn_ballot_w64(a[i][1] != tag) | __builtin_amdgcn_ballot_w64(a[i][3] != tag);
  return (m[0] | m[1] | m[2] | m[3]) != 0ull;
}
struct Sel { unsigned lo[4], hi[4]; };
__device__ __forceinline__ Sel make_sel(int lane) {
  Sel s;
  const int j = lane & 7;
#pragma unroll
  for (int t = 0; t < 4; ++t) {
    const bool on = (j >> 1) == t;
    s.lo[t] = on ? ((j & 1) ? 0x01000c0cu : 0x0c0c0100u) : 0x0c0c0c0cu;
    s.hi[t] = on ? ((j & 1) ? 0x03020c0cu : 0x0c0c0302u) : 0x0c0c0c0cu;
  }
  return s;
}
__device__ __forceinline__ u32x4 place(unsigned src, const unsigned (&sel)[4]) {
  u32x4 r;
#pragma unroll
  for (int t = 0; t < 4; ++t) r[t] = __builtin_amdgcn_perm(src, src, sel[t]);
  return r;
}

#define STAMP(i) do { SCHED_FENCE(); st[i] = __builtin_amdgcn_s_memrealtime(); SCHED_FENCE(); } while (0)

#define KSTAMP_OUT() do { if (wave == 7 && bid == 8) { const unsigned long long d = 3000 + (tbar - st[0]) * 8; \
    while (__builtin_amdgcn_s_memrealtime() - st[0] < d) __builtin_amdgcn_s_sleep(1); } } while (0)
#define KA0 "s36"
#define KA1 "s37"
#define KA6 "s42"
#define KA7 "s43"
#define KB23 "s[54:55]"
#define RT "s[56:59]"
#define RT01 "s[56:57]"
#define RT0 "s56"
#define RT1 "s57"
#define RT2 "s58"
#define RT3 "s59"
__global__ void __launch_bounds__(1024) fused_kernel(const void*, const void*, const void*, const void*, const void*, const void*, const void*,
                                                     const void*, void*, void*) {
  __shared__ __attribute__((aligned(16))) unsigned char smem[L_END];
  LAS unsigned char* L = (LAS unsigned char*)smem;
  volatile LAS unsigned* flag = (volatile LAS unsigned*)(L + L_FLAG);
  unsigned long long st[8] = {0, 0, 0, 0, 0, 0, 0, 0};
  STAMP(0);
  const int bid = blockIdx.x, tid = threadIdx.x, lane = tid & 63;
  u32x16 ka;
  u32x4 kb, rt;
  u64 tbar = 0;
  unsigned epochv = 0, wave, t0, t1, t2, v0, v1;
  asm volatile(
      "8:\n\ts_load_dwordx16 %[ka], %[kp], 0x0\n\ts_load_dwordx4 %[kb], %[kp], 0x40\n\t"
      "v_readfirstlane_b32 %[w], %[tid]\n\ts_lshr_b32 %[w], %[w], 6\n\t"
      "s_cmp_lt_u32 %[w], 8\n\ts_cbranch_scc1 1f\n\t"
      "s_add_u32 %[t0], %[bid], 1\n\ts_and_b32 %[t0], %[t0], 3\n\t"
      "s_lshl_b32 %[t2], %[w], 11\n\ts_add_u32 m0, %[t2], %[lx]\n\t"
      "s_cmp_eq_u32 %[w], 8\n\ts_cbranch_scc1 5f\n\t"
      "v_lshrrev_b32 %[v0], 3, %[lane]\n\tv_and_b32 %[v1], 7, %[lane]\n\tv_lshlrev_b32 %[v0], 13, %[v0]\n\tv_lshl_or_b32 %[v0], %[v1], 4, %[v0]\n\t"
      "s_mul_i32 %[t0], %[t0], 0xe0000\n\ts_lshl_b32 %[t1], %[w], 17\n\ts_add_u32 %[t0], %[t0], %[t1]\n\t"
      "s_lshl_b32 %[t1], %[bid], 4\n\ts_and_b32 %[t1], %[t1], 0xf80\n\ts_add_u32 %[t0], %[t0], %[t1]\n\t"
      "s_lshl_b32 %[t1], %[bid], 10\n\ts_and_b32 %[t1], %[t1], 0x1000\n\ts_add_u32 %[t0], %[t0], %[t1]\n\t"
      "s_sub_u32 %[t0], %[t0], 0x120000\n\ts_add_u32 %[t1], %[t0], 0x10000\n\t"
      "s_mov_b32 " RT2 ", 0x380000\n\ts_mov_b32 " RT3 ", 0x20000\n\t"
      "s_waitcnt lgkmcnt(0)\n\ts_mov_b32 " RT0 ", " KA6 "\n\ts_and_b32 " RT1 ", " KA7 ", 0xffff\n\t"
      "s_branch 6f\n"
      "5:\n\t"
      "v_lshrrev_b32 %[v0], 4, %[lane]\n\tv_and_b32 %[v1], 15, %[lane]\n\tv_min_u32 %[v1], 13, %[v1]\n\tv_mul_u32_u24 %[v0], 0x380, %[v0]\n\tv_lshl_add_u32 %[v0], %[v1], 4, %[v0]\n\t"
      "s_mul_i32 %[t0], %[t0], 0xe0\n\tv_add_u32 %[v0], %[t0], %[v0]\n\ts_mov_b32 %[t0], 0\n\ts_movk_i32 %[t1], 0xe00\n\t"
      "s_movk_i32 " RT2 ", 0x1c00\n\ts_mov_b32 " RT3 ", 0x20000\n\t"
      "s_waitcnt lgkmcnt(0)\n\ts_mov_b32 " RT0 ", " KA0 "\n\ts_and_b32 " RT1 ", " KA1 ", 0xffff\n"
      "6:\n\t"
      "buffer_load_dwordx4 %[v0], " RT ", %[t0] offen nt lds\n\t"
      "s_add_u32 m0, m0, 0x400\n\ts_nop 0\n\tbuffer_load_dwordx4 %[v0], " RT ", %[t1] offen nt lds\n\t"
      "s_memrealtime %[tb]\n\ts_waitcnt lgkmcnt(0)\n\ts_barrier\n\ts_branch 4f\n"
      "1:\n\ts_cmp_eq_u32 %[w], 0\n\ts_cbranch_scc1 2f\n\t"
      "s_cmp_lt_u32 %[w], 5\n\ts_cbranch_scc1 4f\n\t"
      "s_cmp_eq_u32 %[w], 7\n\ts_cbranch_scc1 3f\n\t"
      "s_cmp_lt_u32 %[bid], 192\n\ts_cbranch_scc1 4f\n\t"
      "s_waitcnt lgkmcnt(0)\n\ts_memrealtime %[tb]\n\ts_waitcnt lgkmcnt(0)\n\ts_barrier\n\ts_branch 4f\n"
      "3:\n\t"
      "v_lshlrev_b32 %[v0], 2, %[lane]\n\tds_write_b32 %[v0], %[e]\n\t"
      "s_getpc_b64 " RT01 "\n7:\n\ts_sub_u32 " RT0 ", " RT0 ", 7b-8b\n\ts_subb_u32 " RT1 ", " RT1 ", 0\n\ts_and_b32 " RT0 ", " RT0 ", 0xffffff00\n\ts_and_b32 " RT1 ", " RT1 ", 0xffff\n\t"
      "s_mov_b32 " RT2 ", %[cl]\n\ts_mov_b32 " RT3 ", 0x20000\n\ts_mov_b32 m0, %[lc]\n\t"
      "s_lshr_b32 %[t0], %[bid], 3\n\ts_lshl_b32 %[t0], %[t0], 10\n\tv_lshlrev_b32 %[v0], 4, %[lane]\n\tv_add_u32 %[v0], %[t0], %[v0]\n\t"
      "buffer_load_dwordx4 %[v0], " RT ", 0 offen lds\n\t"
      "s_waitcnt lgkmcnt(0)\n\ts_memrealtime %[tb]\n\ts_waitcnt lgkmcnt(0)\n\ts_barrier\n\ts_branch 4f\n"
      "2:\n\ts_waitcnt lgkmcnt(0)\n\tglobal_load_dword %[e], %[e], " KB23 " sc1\n\ts_memrealtime %[tb]\n\ts_waitcnt lgkmcnt(0)\n\ts_barrier\n\ts_waitcnt vmcnt(0)\n"
      "4:"
      : [ka] "={s[36:51]}"(ka), [kb] "={s[52:55]}"(kb), [rt] "={s[56:59]}"(rt), [w] "=&s"(wave), [t0] "=&s"(t0), [t1] "=&s"(t1), [t2] "=&s"(t2),
        [v0] "=&v"(v0), [v1] "=&v"(v1), [e] "+v"(epochv), [tb] "+s"(tbar)
      : [kp] "{s[0:1]}"(__builtin_amdgcn_kernarg_segment_ptr()), [bid] "{s2}"(bid), [tid] "v"(tid), [lane] "v"(lane), [lx] "i"(L_X - 8 * 2048),
        [lc] "i"(L_WO), [cl] "i"(CODE_BYTES)
      : "memory", "scc");
#define WINDOW(p) (((p) + 0x3fffffull) & ~0x3fffffull)
#define ARG(v, i) ((u64)v[2 * (i)] | ((u64)v[2 * (i) + 1] << 32))
  const int xc = bid & 7, jj = bid >> 3, kvh = jj & 7, s = jj >> 3, b = (xc - 2 * s) & 7;
  const int nkw = s == 3 ? 4 : 6;
  unsigned tag = 0;

  if (__builtin_expect(wave - 1u < (unsigned)nkw, 1)) {
    const bool four = s == 3 && wave >= 3;
    const int urel = four && wave == 4 ? 16 : (wave - 1) * 6;
    const unsigned hsh = (((unsigned)bid * 8u + wave) * 2654435761u) >> 24;
    unsigned ord = (unsigned)((four ? 0x3210321032ull : 0x543210543210ull) >> (((hsh * (four ? 4u : 6u)) >> 8) * 4u));
    int klo = (lane & 7) * 512 + ((lane >> 3) & 1) * 64 + (lane >> 4) * 16 + (s * 36 + urel) * 4096;
    unsigned kvoff = (unsigned)(((b * HKV + kvh) * SK + (SK - WIN)) * DH * 2);
    unsigned brot = (wave + jj + xc) * 128u;
    u32x16 kak = ka;
    u32x4 kbk = kb;
    asm volatile("s_waitcnt lgkmcnt(0)\n\ts_barrier" : "+{s[36:51]}"(kak), "+{s[52:55]}"(kbk), "+v"(klo), "+s"(kvoff), "+s"(ord), "+s"(brot)::"memory");
    const auto rk = RSRC(ARG(kak, 1) + kvoff, WIN * DH * 2);
    u32x4 r[24];
    STAMP(4);
#pragma unroll
    for (int i = 0; i < 24; ++i) {
      if (i >= WK0) vmwait<WK - 1>();
      if (i == WK0) STAMP(5);
      r[i] = __builtin_amdgcn_raw_buffer_load_b128(rk, i < 16 || !four ? klo : OOB, ((ord >> (i & ~3)) & 15u) * 4096 + ((brot + i * 128) & 0x180), KAUX);
      SCHED_FENCE();
      if (i == KTRIG) lds_inc(L, F_KISS, lane);
    }
    STAMP(1);
    const u64 ws = WINDOW(ARG(kbk, 1));
    const auto rws = RSRC(ws, WS_END);
    const int kw = wave - 1, nu = four ? 4 : 6, ulo = s * 36, uhi = s == 3 ? 128 : ulo + 36, u0 = ulo + urel;
    __builtin_amdgcn_s_setprio(2);
    lds_wait_ge(flag + F_QRDY, 1u);
    tag = __builtin_amdgcn_readfirstlane(flag[F_TAG]);
    STAMP(2);
    u32x4 qa[4];
#pragma unroll
    for (int t = 0; t < 4; ++t) qa[t] = *(const LAS u32x4*)(L + L_Q + (lane & 1) * 512 + ((brot + t * 128) & 0x180) + ((lane >> 1) & 1) * 64 + (lane >> 4) * 16);
    float s0 = 0.f, s1 = 0.f;
#pragma unroll
    for (int u = 0; u < 6; ++u) {
      f32x4 c = {0, 0, 0, 0};
#pragma unroll
      for (int t = 0; t < 4; ++t) c = mfma(qa[t], r[4 * u + t], c);
      float c0 = (lane & 8) ? c[2] : c[0], c1 = (lane & 8) ? c[3] : c[1];
      c0 += dpp<ROR8>(c0); c1 += dpp<ROR8>(c1);
      if ((unsigned)(lane >> 3) == ((ord >> (4 * u)) & 15u) && (u < 4 || !four)) { s0 = c0; s1 = c1; }
    }
    const unsigned pk = pk_bf16(s0, s1);
    const unsigned pa = bperm(pk, (2 * lane) & 63), pb = bperm(pk, (2 * lane + 1) & 63);
    if (lane < 4 * nu) {
      ST128((u32x4{pa, tag, pb, tag}), rws, WS_S + (b * 8 + kvh) * 8192 + u0 * 64 + lane * 16, STAUX);
      *(LAS u32x4*)(L + L_SM + urel * 64 + lane * 16) = u32x4{pa, tag, pb, tag};
    }
    MEM_FENCE();
    lds_inc(L, F_KCNT, lane);
    STAMP(3);
    if (wave <= 4) {
      u32x4 sg[2];
      int of[2];
      bool own[2];
#pragma unroll
      for (int i = 0; i < 2; ++i) {
        const int myu = (kw + 4 * i) * 16 + (lane >> 2);
        own[i] = myu >= ulo && myu < uhi;
        of[i] = own[i] ? OOB : WS_S + (b * 8 + kvh) * 8192 + (kw + 4 * i) * 1024 + lane * 16;
      }
      __builtin_amdgcn_s_sleep(SSLEEP);
      gatherm<2>(sg, of, tag, rws);
      lds_wait_ge(flag + F_KCNT, (unsigned)nkw);
#pragma unroll
      for (int i = 0; i < 2; ++i) {
        const int c = kw + 4 * i;
        const u32x4 mv = *(const LAS u32x4*)(L + L_SM + (own[i] ? (c * 16 - ulo) * 64 + lane * 16 : 0));
        const unsigned sa = own[i] ? mv[0] : sg[i][0], sb = own[i] ? mv[2] : sg[i][2];
        const float m0 = wave_max(fmaxf(bf_lo(sa), bf_lo(sb))), m1 = wave_max(fmaxf(bf_hi(sa), bf_hi(sb)));
        const float n0 = m0 == -INFINITY ? 0.f : m0, n1 = m1 == -INFINITY ? 0.f : m1;
        const float ea = __builtin_amdgcn_exp2f((bf_lo(sa) - n0) * C2), eb = __builtin_amdgcn_exp2f((bf_lo(sb) - n0) * C2);
        const float ec = __builtin_amdgcn_exp2f((bf_hi(sa) - n1) * C2), ed = __builtin_amdgcn_exp2f((bf_hi(sb) - n1) * C2);
        *(LAS unsigned*)(L + L_P + c * 512 + lane * 4) = pk_bf16(ea, eb);
        *(LAS unsigned*)(L + L_P + c * 512 + 256 + lane * 4) = pk_bf16(ec, ed);
        const float l0 = wave_sum(ea + eb), l1 = wave_sum(ec + ed);
        if (lane == 0) *(LAS f32x4*)(L + L_ST + c * 16) = f32x4{m0, l0, m1, l1};
      }
      MEM_FENCE();
      lds_inc(L, F_PR + kw, lane);
      lds_inc(L, F_SCNT, lane);
      STAMP(6);
    }
    return;
  }
  const u64 tafter = __builtin_amdgcn_s_memrealtime();
  const GAS u16* x = (const GAS u16*)ARG(ka, 0);
  const u64 vc = ARG(ka, 2), wo = ARG(ka, 4), w1 = ARG(ka, 5), w2 = ARG(ka, 6), ws = WINDOW(ARG(kb, 1));
  const GAS u16* gw = (const GAS u16*)ARG(ka, 7);
  GAS u16* out = (GAS u16*)ARG(kb, 0);
  const auto rws = RSRC(ws, WS_END);
  const int cb = jj + 32 * (xc >> 2), rb = (xc + 1) & 3;
  const int grp = kvh * 4 + s;
  if (wave >= 8) {
    const unsigned kvoff = (unsigned)(((b * HKV + kvh) * SK + (SK - WIN)) * DH * 2);
    const int w = wave - 8;
    vmwait<0>();
    lds_inc(L, F_WQC, lane);
    tag = get_tag(flag);
    if (w >= 4) {
      __builtin_amdgcn_s_setprio(3);
      const int ct = w - 4;
      const int xrow = ((lane & 7) >> 1) + 4 * (lane & 1);
      LAS unsigned char* xp = L + L_X + xrow * 256 + (lane >> 4) * 16;
      LAS unsigned char* xp2 = L + L_X + xrow * 256 + 192 + (lane >> 4) * 8;
      LAS unsigned char* tb = L + L_WQ + ((lane >> 4) * 8 + ((lane >> 2) & 3)) * 128 + (lane & 3) * 8 + ct * 32;
      LAS unsigned char* tb2 = L + L_WQ + (96 + (lane >> 4) * 4 + ((lane >> 2) & 3)) * 128 + (lane & 3) * 8 + ct * 32;
      const int dest = ((lane >> 4) & 1) * 2 + (lane >> 5);
      const int qo = WS_QP + (cb * 4 + dest) * QPSTR + rb * 1024 + ct * 256 + (lane & 15) * 16;
      lds_wait_ge(flag + F_WQC, 8u);
      STAMP(1);
      const u32x2 pt = trd(tb2), xb = *(const LAS u32x2*)xp2;
      f32x4 acc = mfma(u32x4{xb[0], xb[1], 0u, 0u}, u32x4{pt[0], pt[1], 0u, 0u}, f32x4{0, 0, 0, 0});
#pragma unroll
      for (int ks = 0; ks < 3; ++ks) {
        const u32x2 p0 = trd(tb + ks * 4096), p1 = trd(tb + ks * 4096 + 512);
        acc = mfma(*(const LAS u32x4*)(xp + ks * 64), u32x4{p0[0], p0[1], p1[0], p1[1]}, acc);
      }
      const float q0 = lane >= 32 ? acc[2] : acc[0], q1 = lane >= 32 ? acc[3] : acc[1];
      if (dest != rb) ST128((u32x4{__float_as_uint(q0), tag, __float_as_uint(q1), tag}), rws, qo, STAUX);
      else *(LAS f32x2*)(L + L_QO + (ct * 16 + (lane & 15)) * 8) = f32x2{q0, q1};
      LGKM0();
      lds_inc(L, F_QPC, lane);
      __builtin_amdgcn_s_setprio(0);
    }
    const auto rv = RSRC(vc + kvoff, WIN * DH * 2);
    u32x4 r[16];
    const int trot = w + jj + xc;
    const int voff = (w * 128 + (lane >> 4) * 8 + ((lane >> 3) & 1)) * 512 + s * 128 + (lane & 7) * 16;
    const Sel sel = make_sel(lane);
    lds_wait_ge(flag + F_KISS, (unsigned)nkw);
    STAMP(2);
#define V_ISSUE(i) do { if ((i) > VSPLIT) vmwait<WVE - 1>(); else if ((i) >= WV) { if (S3W && ((S3W == 2) || ((i) & 1)) && s == 3) vmwait<WV>(); else vmwait<WV - 1>(); } \
      r[i] = __builtin_amdgcn_raw_buffer_load_b128(rv, voff + ((i) & 3) * 1024, ((((i) >> 2) + trot) & 3) * 16384, VAUX); SCHED_FENCE(); \
      if ((i) == VTRIG) lds_inc(L, F_VISS, lane); } while (0)
#define V_USE(i) do { a0 = mfma(place(pp[(i) >> 2][(i) & 3], sel.lo), r[i], a0); a1 = mfma(place(pp[(i) >> 2][(i) & 3], sel.hi), r[i], a1); } while (0)
#pragma unroll
    for (int i = 0; i <= VSPLIT; ++i) V_ISSUE(i);
    STAMP(3);
    u32x4 pp[4];
    f32x4 a0 = {0, 0, 0, 0}, a1 = {0, 0, 0, 0};
#define P_READ() _Pragma("unroll") for (int t = 0; t < 4; ++t) \
      pp[t] = *(const LAS u32x4*)(L + L_P + w * 512 + ((lane >> 3) & 1) * 256 + (((t + trot) & 3) * 32 + (lane >> 4) * 8) * 2)
    float f;
    unsigned wad = (unsigned)(uintptr_t)(L + L_OP) + w * 512 + ((lane >> 5) * 64 + (lane & 7) * 8 + ((lane >> 4) & 1) * 4) * 4;
    unsigned rad = (unsigned)(uintptr_t)(L + L_OP) + (lane >> 5) * 2048 + (lane & 31) * 16;
    int oad = WS_OB + inbox(grp) + b * 512 + lane * 16;
#define F_GET() do { lds_wait_ge(flag + F_FRDY, 1u); f = *(const LAS float*)(L + L_FW + w * 8 + (lane >> 5) * 4); \
      asm volatile("" : "+v"(f), "+v"(wad), "+v"(rad), "+v"(oad)); } while (0)
    if (__builtin_expect(__builtin_amdgcn_readfirstlane(flag[F_PR + (w & 3)]) != 0u, 1)) {
      MEM_FENCE();
      STAMP(4);
      P_READ();
      constexpr int PER = VPER;
#pragma unroll
      for (int i = VSPLIT + 1; i < 16; ++i) {
        V_ISSUE(i);
#pragma unroll
        for (int k = 0; k < 16; ++k)
          if (k >= (i - VSPLIT - 1) * PER && k < (i - VSPLIT) * PER && k <= i - WVE) V_USE(k);
        SCHED_FENCE();
      }
#pragma unroll
      for (int k = 0; k < 16 - WVE; ++k) {
        bool done = false;
#pragma unroll
        for (int i = VSPLIT + 1; i < 16; ++i) done = done || (k >= (i - VSPLIT - 1) * PER && k < (i - VSPLIT) * PER && k <= i - WVE);
        if (!done) { V_USE(k); if (k & 1) SCHED_FENCE(); }
      }
      SCHED_FENCE();
      F_GET();
      u32x4 la[WVE], lb[WVE];
#pragma unroll
      for (int k = 0; k < WVE; ++k) {
        la[k] = place(pp[(16 - WVE + k) >> 2][(16 - WVE + k) & 3], sel.lo);
        lb[k] = place(pp[(16 - WVE + k) >> 2][(16 - WVE + k) & 3], sel.hi);
        asm volatile("" : "+v"(la[k]), "+v"(lb[k]));
      }
      SCHED_FENCE();
#pragma unroll
      for (int k = 0; k < WVE; ++k) { a0 = mfma(la[k], r[16 - WVE + k], a0); a1 = mfma(lb[k], r[16 - WVE + k], a1); }
    } else {
#pragma unroll
      for (int i = VSPLIT + 1; i < 16; ++i) V_ISSUE(i);
      STAMP(6);
      lds_wait_ge(flag + F_PR + (w & 3), 1u);
      P_READ();
#pragma unroll
      for (int k = 0; k < 16; ++k) { V_USE(k); if (k & 1) SCHED_FENCE(); }
      F_GET();
    }
    f32x4 t = (lane & 8) ? a1 : a0;
#pragma unroll
    for (int k = 0; k < 4; ++k) t[k] = (t[k] + dpp<ROR8>(t[k])) * f;
    STAMP(5);
    unsigned old;
    f32x4 o0v, o1v, o2v, o3v;
    asm volatile("ds_write_b128 %[wa], %[t]\n\tds_add_rtn_u32 %[old], %[ca], %[one]\n\t"
                 "ds_read_b128 %[p0], %[ra]\n\tds_read_b128 %[p1], %[ra] offset:512\n\tds_read_b128 %[p2], %[ra] offset:1024\n\tds_read_b128 %[p3], %[ra] offset:1536\n\t"
                 "s_waitcnt lgkmcnt(0)"
                 : [old] "=&v"(old), [p0] "=&v"(o0v), [p1] "=&v"(o1v), [p2] "=&v"(o2v), [p3] "=&v"(o3v)
                 : [wa] "v"(wad), [t] "v"(t), [ca] "v"((unsigned)(uintptr_t)(L + (lane == 0 ? F_OCNT * 4 : 128 + (lane & 31) * 4))), [one] "v"(1u), [ra] "v"(rad)
                 : "memory");
    if (__builtin_amdgcn_readfirstlane(old) == 7u) {
      f32x4 ov = (o0v + o1v) + (o2v + o3v);
#pragma unroll
      for (int k = 0; k < 4; ++k) { float hi, lo = x32_a(ov[k], hi); ov[k] = lo + hi; }
      if (lane < 32) {
        const u32x4 ogr = {pk_bf16(ov[0], ov[1]), tag, pk_bf16(ov[2], ov[3]), tag};
        ST128(ogr, rws, oad, STAUX);
        ST128(ogr, rws, oad + 8192, STAUX);
        *(LAS u32x4*)(L + L_OM + lane * 16) = ogr;
      }
      MEM_FENCE();
      lds_inc(L, F_OMR, lane);
    }
  } else if (wave == 0) {
    __builtin_amdgcn_s_setprio(3);
    tag = __builtin_amdgcn_readfirstlane(epochv) * 1664525u + 1013904223u;
    if (lane == 0) *(volatile LAS u32x2*)flag = u32x2{1u, tag};
    lds_wait_ge(flag + F_QPC, 4u);
    STAMP(2);
    __builtin_amdgcn_s_sleep(QSLEEP1);
    {
      const int po = WS_QP + (cb * 4 + rb) * QPSTR + lane * 16;
      u32x4 pg[3];
      int of[3] = {po + ((rb + 1) & 3) * 1024, po + ((rb + 2) & 3) * 1024, po + ((rb + 3) & 3) * 1024};
      gatherm<3>(pg, of, tag, rws);
      const f32x2 own = *(const LAS f32x2*)(L + L_QO + lane * 8);
      const float a0 = (own[0] + __uint_as_float(pg[0][0])) + (__uint_as_float(pg[1][0]) + __uint_as_float(pg[2][0]));
      const float a1 = (own[1] + __uint_as_float(pg[0][2])) + (__uint_as_float(pg[1][2]) + __uint_as_float(pg[2][2]));
      const float n0 = dpp<XOR1>(a0), n1 = dpp<XOR1>(a1);
      const unsigned pk = (lane & 1) ? pk_bf16(n1, a1) : pk_bf16(a0, n0);
      const int src = (lane & 15) * 4 + ((lane >> 4) & 1);
      const unsigned pa = bperm(pk, src), pb = bperm(pk, src + 2);
      if (lane < 32) ST128((u32x4{pa, tag, pb, tag}), rws, WS_Q + ((cb >> 3) * 8 + rb + 4 * (lane >> 4)) * QSTR + (cb & 7) * 256 + (lane & 15) * 16, STAUX);
    }
    STAMP(6);
    __builtin_amdgcn_s_sleep(QSLEEP2);
    {
      const int qoff = WS_Q + (kvh * 8 + b) * QSTR + lane * 16;
      u32x4 qq[2];
      int of[2] = {qoff, qoff + 1024};
      gatherm<2>(qq, of, tag, rws);
      *(LAS u32x2*)(L + L_Q + lane * 8) = u32x2{qq[0][0], qq[0][2]};
      *(LAS u32x2*)(L + L_Q + 512 + lane * 8) = u32x2{qq[1][0], qq[1][2]};
      LGKM0();
      if (lane == 0) flag[F_QRDY] = 1u;
    }
    STAMP(3);
    u32x4 xg = {0, 0, 0, 0}, gg = {0, 0, 0, 0};
    if (lane < 56) {
      xg = *(const GAS u32x4*)(x + b * DM + lane * 8);
      gg = *(const GAS u32x4*)(gw + lane * 8);
    }
    float z = 0.f;
#pragma unroll
    for (int t = 0; t < 4; ++t) z += rbf(bf_lo(xg[t]) * bf_lo(gg[t])) + rbf(bf_hi(xg[t]) * bf_hi(gg[t]));
    z = rbf(wave_sum(z));
    const float g = rbf(1.f / (1.f + __expf(-z)));
    const unsigned g1u = __float_as_uint(rbf(1.f - g));
    if (lane == 0) *(volatile LAS u32x2*)(flag + F_G1V) = u32x2{1u, g1u};
    lds_wait_ge(flag + F_SCNT, 4u);
    {
      const f32x2 ml = *(const LAS f32x2*)(L + L_ST + (lane & 15) * 8);
      float M = fmaxf(ml[0], dpp<XOR2>(ml[0]));
      M = fmaxf(M, dpp<0x124>(M)); M = fmaxf(M, dpp<ROR8>(M));
      const float al = __builtin_amdgcn_exp2f((ml[0] - M) * C2);
      float Ls = al * ml[1];
      Ls += dpp<XOR2>(Ls); Ls += dpp<0x124>(Ls); Ls += dpp<ROR8>(Ls);
      if (lane < 16) *(LAS float*)(L + L_FW + lane * 4) = al * (g / Ls);
      LGKM0();
      if (lane == 0) flag[F_FRDY] = 1u;
    }
    STAMP(4);
  } else if (wave != 7 && xc < 7) {
    const int j = wave - 5, t = xc * 8 + kvh;
    const unsigned lb = (unsigned)(uintptr_t)(L + L_T) + j * 14336;
    const auto r1 = RSRC(w1, DM * FF * 2);
    const int o = ((j * 224 + (lane >> 2)) * FF + t * 32 + (lane & 3) * 8) * 2;
#pragma unroll
    for (int i = 0; i < 14; ++i) {
      if (i >= WT) vmwait<WT - 1>();
      if (W1P) dma16p(r1, o + i * 16 * FF * 2, lb + i * 1024); else dma16(r1, o + i * 16 * FF * 2, lb + i * 1024);
    }
    {
      const auto rx = RSRC((u64)x, NB * DM * 2);
#pragma unroll
      for (int i = 0; i < 4; ++i) {
        vmwait<WT - 1>();
        dma16p(rx, (j * 4 + i) * 1024 + lane * 16, (unsigned)(uintptr_t)(L + L_XA) + (j * 4 + i) * 1024);
      }
    }
    tag = get_tag(flag);
    vmwait<0>();
    lds_inc(L, F_TCNT, lane);
    lds_wait_ge(flag + F_TCNT, 2u);
    STAMP(6);
    LAS unsigned char* tb = L + L_T + ((lane >> 4) * 8 + ((lane >> 2) & 3)) * 64 + j * 32 + (lane & 3) * 8;
    f32x4 acc0 = {0, 0, 0, 0}, acc1 = {0, 0, 0, 0};
#pragma unroll
    for (int ks = 0; ks < 14; ++ks) {
      const u32x2 p0 = trd(tb + ks * 2048), p1 = trd(tb + ks * 2048 + 256);
      const u32x4 xa = *(const LAS u32x4*)(L + L_XA + (lane & 7) * (DM * 2) + ks * 64 + (lane >> 4) * 16);
      if (ks & 1) acc1 = mfma(u32x4{p0[0], p0[1], p1[0], p1[1]}, xa, acc1);
      else acc0 = mfma(u32x4{p0[0], p0[1], p1[0], p1[1]}, xa, acc0);
    }
    acc0 += acc1;
    float hv[4];
#pragma unroll
    for (int k = 0; k < 4; ++k) {
      const float v = rbf(acc0[k]);
      hv[k] = v / (1.f + __expf(-v));
    }
    if (!(lane & 8))
      ST128((u32x4{pk_bf16(hv[0], hv[1]), tag, pk_bf16(hv[2], hv[3]), tag}), rws, WS_HB + t * 1024 + j * 512 + (lane >> 4) * 128 + (lane & 7) * 16, STAUX);
  }
  const int cpair = (xc ^ 1) * 32 + jj;
  if (wave == 7) {
    const int fgrp = b == 7 ? -1 : s < 3 ? kvh * 3 + s : (kvh < 4 ? 24 + kvh : -1);
    tag = get_tag(flag);
    const bool hi8 = lane & 8;
    const unsigned upper = (__builtin_amdgcn_s_getreg(20 | (3 << 11)) >> 2) & 1u;
    if (s == 0) {
      const u64 pa = ws + WS_PR + kvh * 16384 + b * 1024;
      int vote = 0;
#pragma unroll
      for (int i = 0; i < NPAIR; ++i) {
        u64 t0, t1, t2;
        unsigned d;
        asm volatile("s_memtime %0\n\ts_waitcnt lgkmcnt(0)\n\ts_load_dword %3, %4, %5\n\ts_waitcnt lgkmcnt(0)\n\ts_memtime %1\n\ts_waitcnt lgkmcnt(0)\n\t"
                     "s_load_dword %3, %4, %6\n\ts_waitcnt lgkmcnt(0)\n\ts_memtime %2\n\ts_waitcnt lgkmcnt(0)"
                     : "=&s"(t0), "=&s"(t1), "=&s"(t2), "=&s"(d) : "s"(pa), "i"(i * 128), "i"(i * 128 + 8192) : "memory");
        vote += (unsigned)(t1 - t0) > (unsigned)(t2 - t1) ? 1 : -1;
      }
      lds_wait_ge(flag + F_G1V, 1u);
      if (lane < 8) ST128((u32x4{flag[F_G1], tag, (unsigned)(upper ? -vote : vote), tag}), rws, WS_G + (kvh * 8 + b) * 128 + lane * 16, STAUX);
      STAMP(1);
    }
    u32x4 hb[3];
    int nearc;
    {
      const unsigned lb = (unsigned)(uintptr_t)(L + L_WO);
      const auto r2 = RSRC(w2, FF * DM * 2);
      const auto ro = RSRC(wo, NQ * DM * 2);
      const int off2 = (fgrp * 64 + (lane >> 3)) * 896 + b * 128 + (lane & 7) * 16;
      const int offo = (kvh * 512 + s * 64 + (lane >> 3)) * 896 + b * 128 + (lane & 7) * 16;
      lds_wait_ge(flag + HFLAG, HTRIG);
      {
        int of[3] = {fgrp >= 0 ? WS_HB + fgrp * 2048 + lane * 16 : OOB, fgrp >= 0 ? WS_HB + fgrp * 2048 + 1024 + lane * 16 : OOB, WS_G + (kvh * 8 + (lane & 7)) * 128};
        gatherm<3>(hb, of, tag, rws);
        float vs = (float)(int)hb[2][2];
        vs += dpp<XOR1>(vs); vs += dpp<XOR2>(vs); vs += dpp<HMIRROR>(vs);
        nearc = (((__builtin_amdgcn_readfirstlane(__float_as_uint(vs)) >> 31) ^ 1u ^ upper ^ FLIP) & 1u) * 8192;
        if (lane == 0) *(volatile LAS u32x2*)(flag + F_NRV) = u32x2{1u, (unsigned)nearc};
      }
      if (b == 7) { KSTAMP_OUT(); return; }
      lds_wait_ge(flag + F_VISS, 8u);
      STAMP(2);
      if (fgrp >= 0) {
#pragma unroll
        for (int i = 0; i < 8; ++i) dma16(r2, off2 + i * 8 * 896, lb + 16384 + i * 1024);
      }
#pragma unroll
      for (int i = 0; i < 16; ++i) dma16(ro, offo + ((i >> 3) * 256 + (i & 7) * 8) * 896, lb + i * 1024);
      STAMP(3);
    }
    __builtin_amdgcn_s_setprio(3);
    const bool ownb = (lane & 7) == b;
    const int ooff = WS_OB + nearc + inbox(grp) + (lane & 7) * 512 + (lane >> 3) * 16;
    u32x4 ob[4];
#pragma unroll
    for (int i = 0; i < 4; ++i) ob[i] = LD128(rws, ownb ? OOB : ooff + i * 128, LDAUX);
    SCHED_FENCE();
    LAS unsigned char* tb = L + L_WO + ((lane >> 4) * 8 + ((lane >> 2) & 3)) * 128 + (lane & 3) * 8;
    f32x4 acc[4] = {{0, 0, 0, 0}, {0, 0, 0, 0}, {0, 0, 0, 0}, {0, 0, 0, 0}};
    if (fgrp >= 0) {
      vmwait<20>();
      const u32x4 hv = {hb[0][0], hb[0][2], hb[1][0], hb[1][2]};
      const float g1 = __uint_as_float(hb[2][0]);
#pragma unroll
      for (int ks = 0; ks < 2; ++ks) {
        const u32x4 bf = {hi8 ? 0u : hv[2 * ks], hi8 ? 0u : hv[2 * ks + 1], hi8 ? hv[2 * ks] : 0u, hi8 ? hv[2 * ks + 1] : 0u};
#pragma unroll
        for (int ct = 0; ct < 4; ++ct) {
          const u32x2 p0 = trd(tb + 16384 + ks * 4096 + ct * 32), p1 = trd(tb + 16384 + ks * 4096 + 512 + ct * 32);
          acc[ct] = mfma(u32x4{p0[0], p0[1], p1[0], p1[1]}, bf, acc[ct]);
        }
      }
#pragma unroll
      for (int ct = 0; ct < 4; ++ct) acc[ct] *= g1;
    }
    u32x4 wf[4][4];
#pragma unroll
    for (int ks = 0; ks < 4; ++ks) {
      if (ks == 0) vmwait<16>(); else if (ks == 1) vmwait<12>(); else if (ks == 2) vmwait<8>(); else vmwait<4>();
#pragma unroll
      for (int ct = 0; ct < 4; ++ct) {
        const u32x2 p0 = trd(tb + ks * 4096 + ct * 32), p1 = trd(tb + ks * 4096 + 512 + ct * 32);
        wf[ks][ct] = u32x4{p0[0], p0[1], p1[0], p1[1]};
      }
    }
    int po = WS_PO + (nearc ^ (((xc ^ b) >> 2) & 1) * 8192) + inbox(b * 32 + (lane >> 3)) + grp * 128 + (lane & 7) * 16;
    asm volatile("" : "+v"(po));
    STAMP(4);
    lds_wait_ge(flag + F_OMR, 1u);
#pragma unroll
    for (int i = 0; i < 4; ++i) {
      const u32x4 v = *(const LAS u32x4*)(L + L_OM + i * 128 + (lane >> 3) * 16);
      ob[i] = ownb ? v : ob[i];
    }
    u64 ms[4] = {~0ull, ~0ull, ~0ull, ~0ull};
    if (missing4(ob, ms, tag)) {
#pragma nounroll
      for (unsigned n = 0; n < SPIN_CAP; ++n) {
        pass4<128>(ob, ms, ooff, rws);
        if (!missing4(ob, ms, tag)) break;
      }
    }
    STAMP(6);
    u32x4 bf[4];
#pragma unroll
    for (int ks = 0; ks < 4; ++ks) bf[ks] = u32x4{hi8 ? 0u : ob[ks][0], hi8 ? 0u : ob[ks][2], hi8 ? ob[ks][0] : 0u, hi8 ? ob[ks][2] : 0u};
#pragma unroll
    for (int ct = 0; ct < 4; ++ct)
#pragma unroll
      for (int ks = 0; ks < 4; ++ks) acc[ct] = mfma(wf[ks][ct], bf[ks], acc[ct]);
    u32x4 pv[4];
#pragma unroll
    for (int ct = 0; ct < 4; ++ct) {
      const float k0 = hi8 ? acc[ct][2] : acc[ct][0], k1 = hi8 ? acc[ct][3] : acc[ct][1];
      const float s0 = hi8 ? acc[ct][0] : acc[ct][2], s1 = hi8 ? acc[ct][1] : acc[ct][3];
      pv[ct] = u32x4{__float_as_uint(k0 + dpp<ROR8>(s0)), tag, __float_as_uint(k1 + dpp<ROR8>(s1)), tag};
      __builtin_amdgcn_raw_buffer_store_b128(pv[ct], rws, po, ct * 4 * 16384, STAUX);
    }
    STAMP(5);
    po ^= 8192;
#pragma unroll
    for (int ct = 0; ct < 4; ++ct) __builtin_amdgcn_raw_buffer_store_b128(pv[ct], rws, po, ct * 4 * 16384, STAUX);
  } else if (wave == 0 && cpair < 224) {
    __builtin_amdgcn_s_setprio(3);
    lds_wait_ge(flag + F_OCNT, 8u);
    STAMP(5);
    __builtin_amdgcn_s_sleep(FSLEEP);
    lds_wait_ge(flag + F_NRV, 1u);
    const int poff = WS_PO + __builtin_amdgcn_readfirstlane(flag[F_NR]) + inbox(cpair) + lane * 16;
    u32x4 pg[4] = {{0, 0, 0, 0}, {0, 0, 0, 0}, {0, 0, 0, 0}, {0, 0, 0, 0}};
    u64 ms[4] = {~0ull, ~0ull, ~0ull, ~0ull};
    GAS unsigned* op = (GAS unsigned*)(out + (lane & 7) * DM + cpair * 2);
    asm volatile("" : "+v"(op));
#pragma nounroll
    for (unsigned n = 0; n < SPIN_CAP; ++n) {
      pass4<1024>(pg, ms, poff, rws);
      if (!missing4(pg, ms, tag)) {
        float a0 = (__uint_as_float(pg[0][0]) + __uint_as_float(pg[1][0])) + (__uint_as_float(pg[2][0]) + __uint_as_float(pg[3][0]));
        float a1 = (__uint_as_float(pg[0][2]) + __uint_as_float(pg[1][2])) + (__uint_as_float(pg[2][2]) + __uint_as_float(pg[3][2]));
        a0 += dpp<ROR8>(a0); a1 += dpp<ROR8>(a1);
        float o, t;
        t = x16_a(a0, o); a0 = t + o; t = x16_a(a1, o); a1 = t + o;
        t = x32_a(a0, o); a0 = t + o; t = x32_a(a1, o); a1 = t + o;
        if (lane < 8) *op = pk_bf16(a0, a1);
        if (bid == 0 && lane == 0) __hip_atomic_store((GAS unsigned*)ARG(kb, 1), tag, __ATOMIC_RELAXED, __HIP_MEMORY_SCOPE_AGENT);
        STAMP(1);
        break;
      }
    }
  }
  KSTAMP_OUT();
}

extern "C" void kernel_launch(void* const* d_in, const int* in_sizes, int n_in, void* d_out, int out_size, void* d_ws, size_t ws_size,
                              hipStream_t stream) {
  fused_kernel<<<256, 1024, 0, stream>>>(d_in[0], d_in[1], d_in[2], d_in[3], d_in[4], d_in[6], d_in[7], d_in[8], d_out, d_ws);
}
```

```cpp
#include <hip/hip_runtime.h>
#include <hip/hip_bf16.h>
#include <stdint.h>

#define STAMPS 0
#define CODEPF 1
#define CODE_BYTES 19456
#define VAUX 2
#define KAUX 2
#define KLATE 0
#define W1P 0
#define U1 58
#define WNT " nt"
#define WK2 WK
#define WV2 WV
#define WVE WV
#define QSLEEP1 10
#define QSLEEP2 10
#define FSLEEP 10
#define WK 4
#define WV 3
#define WT 3
#define WK0 5
#define HFLAG F_SCNT
#define HTRIG 2u
#define NPAIR 6
#define FLIP 1
#define S3W 0
#define VPER 3
#define SSLEEP 24
#define VSPLIT 10
#define VTRIG 15
#define KTRIG 22

typedef unsigned u32x4 __attribute__((ext_vector_type(4)));
typedef unsigned u32x2 __attribute__((ext_vector_type(2)));
typedef float f32x4 __attribute__((ext_vector_type(4)));
typedef float f32x2 __attribute__((ext_vector_type(2)));
typedef short s16x4 __attribute__((ext_vector_type(4)));
typedef short s16x8 __attribute__((ext_vector_type(8)));
typedef __bf16 bf16x2 __attribute__((ext_vector_type(2)));
typedef __bf16 bf16x8 __attribute__((ext_vector_type(8)));
typedef unsigned short u16;
typedef unsigned u32x16 __attribute__((ext_vector_type(16)));
typedef unsigned long long u64;
#define LAS __attribute__((address_space(3)))
#define GAS __attribute__((address_space(1)))

constexpr int NB = 8, DM = 448, HKV = 8, SK = 4096, DH = 256, WIN = 1024, FF = 1792, NQ = 4096;
constexpr int PSTR = 4096 + 128;
constexpr int QPSTR = 4096 + 256, QSTR = 2048 + 128, HSTR = 256;
constexpr int WS_G = 4096;
constexpr int WS_QP = 16384;
constexpr int WS_Q = WS_QP + 256 * QPSTR;
constexpr int WS_HB = WS_Q + 64 * QSTR;
constexpr int WS_S = WS_HB + 65536;
constexpr int WS_PR = (WS_S + 524288 + 16383) & ~16383;
constexpr int WS_OB = 2 << 20;
constexpr int WS_PO = WS_OB + 16 * 16384;
constexpr int WS_END = WS_PO + 112 * 16384;
static_assert(WS_PR + 8 * 16384 <= WS_OB && WS_END == 4 << 20, "two 2 MB pages");
__device__ __forceinline__ int inbox(int k) { return (k >> 1) * 16384 + (k & 1) * 4096; }
constexpr int WS_STAMP = 8 << 20;
constexpr int L_FLAG = 0, L_Q = 256, L_QO = L_Q + 1024, L_ST = L_QO + 512, L_FW = L_ST + 128, L_OM = L_FW + 64, L_SM = L_OM + 512, L_P = L_SM + 2304, L_OP = L_P + 4096, L_WO = L_OP + 4096, L_W2 = L_WO + 16384,
              L_X = L_W2 + 8192, L_WQ = L_X + 2048, L_T = L_WQ + 14336, L_XA = L_T + 28672, L_END = L_XA + 8192;
enum { F_TAGV = 0, F_TAG = 1, F_QRDY = 2, F_WQC = 3, F_QPC = 4, F_KISS = 5, F_OCNT = 6, F_TCNT = 7, F_VISS = 8, F_SCNT = 9, F_FRDY = 10, F_KCNT = 11, F_OMR = 12, F_G1V = 14, F_G1 = 15, F_PR = 16, F_NRV = 20, F_NR = 21 };
#define LDAUX 16
#define STAUX 16
#define WS_AT_END 0
constexpr int AUX_NT = 2, AUX_SC1 = 16;
constexpr float C2 = 0.0625f * 1.4426950408889634f;
constexpr int OOB = 0x7f000000;
constexpr unsigned SPIN_CAP = 1u << 18;

#define RSRC(p, n) __builtin_amdgcn_make_buffer_rsrc((void*)(p), 0, (n), 0x00020000)
#define LD128(r, off, aux) __builtin_amdgcn_raw_buffer_load_b128((r), (off), 0, (aux))
#define ST128(v, r, off, aux) __builtin_amdgcn_raw_buffer_store_b128((v), (r), (off), 0, (aux))
#define SCHED_FENCE() __builtin_amdgcn_sched_barrier(0)
#define MEM_FENCE() asm volatile("" ::: "memory")
template <int N> __device__ __forceinline__ void vmwait() { asm volatile("s_waitcnt vmcnt(%0)" ::"n"(N) : "memory"); }
#define LGKM0() asm volatile("s_waitcnt lgkmcnt(0)" ::: "memory")

__device__ __forceinline__ f32x4 mfma(u32x4 a, u32x4 b, f32x4 c) {
  return __builtin_amdgcn_mfma_f32_16x16x32_bf16(__builtin_bit_cast(bf16x8, a), __builtin_bit_cast(bf16x8, b), c, 0, 0, 0);
}
__device__ __forceinline__ u32x2 trd(LAS unsigned char* p) {
  return __builtin_bit_cast(u32x2, __builtin_amdgcn_ds_read_tr16_b64_v4i16((LAS s16x4*)p));
}
__device__ __forceinline__ unsigned pk_bf16(float lo, float hi) {
  f32x2 v = {lo, hi};
  return __builtin_bit_cast(unsigned, __builtin_convertvector(v, bf16x2));
}
__device__ __forceinline__ float bf_lo(unsigned u) { return __uint_as_float(u << 16); }
__device__ __forceinline__ float bf_hi(unsigned u) { return __uint_as_float(u & 0xffff0000u); }
__device__ __forceinline__ float rbf(float f) { return bf_lo(pk_bf16(f, f)); }
template <int CTRL> __device__ __forceinline__ float dpp(float x) {
  return __builtin_bit_cast(float, __builtin_amdgcn_mov_dpp(__builtin_bit_cast(int, x), CTRL, 0xf, 0xf, true));
}
constexpr int XOR1 = 0xB1, XOR2 = 0x4E, HMIRROR = 0x141, ROR8 = 0x128;
__device__ __forceinline__ float x16_a(float x, float& o) {
  auto s = __builtin_amdgcn_permlane16_swap(__float_as_uint(x), __float_as_uint(x), false, false);
  o = __uint_as_float(s[1]);
  return __uint_as_float(s[0]);
}
__device__ __forceinline__ float x32_a(float x, float& o) {
  auto s = __builtin_amdgcn_permlane32_swap(__float_as_uint(x), __float_as_uint(x), false, false);
  o = __uint_as_float(s[1]);
  return __uint_as_float(s[0]);
}
__device__ __forceinline__ float wave_max(float x) {
  x = fmaxf(x, dpp<XOR1>(x)); x = fmaxf(x, dpp<XOR2>(x)); x = fmaxf(x, dpp<HMIRROR>(x)); x = fmaxf(x, dpp<ROR8>(x));
  float o, a = x16_a(x, o); x = fmaxf(a, o);
  a = x32_a(x, o); return fmaxf(a, o);
}
__device__ __forceinline__ float wave_sum(float x) {
  x += dpp<XOR1>(x); x += dpp<XOR2>(x); x += dpp<HMIRROR>(x); x += dpp<ROR8>(x);
  float o, a = x16_a(x, o); x = a + o;
  a = x32_a(x, o); return a + o;
}
__device__ __forceinline__ void lds_wait_ge(volatile LAS unsigned* f, unsigned want) {
#pragma nounroll
  for (unsigned n = 0; n < (1u << 22); ++n) {
    if (__builtin_amdgcn_readfirstlane(*f) >= want) break;
    __builtin_amdgcn_s_sleep(1);
  }
  MEM_FENCE();
}
__device__ __forceinline__ unsigned bperm(unsigned v, int srclane) { return __builtin_amdgcn_ds_bpermute(srclane << 2, v); }
__device__ __forceinline__ void lds_inc(LAS unsigned char* L, int f, int lane) {
  if (lane == 0) asm volatile("ds_add_u32 %0, %1" ::"v"((unsigned)(uintptr_t)(L + f * 4)), "v"(1u) : "memory");
}
__device__ __forceinline__ unsigned lds_inc_ret(LAS unsigned char* L, int f, int lane) {
  unsigned old = 0;
  if (lane == 0) asm volatile("ds_add_rtn_u32 %0, %1, %2\n\ts_waitcnt lgkmcnt(0)" : "=&v"(old) : "v"((unsigned)(uintptr_t)(L + f * 4)), "v"(1u) : "memory");
  return __builtin_amdgcn_readfirstlane(old);
}
__device__ __forceinline__ unsigned get_tag(volatile LAS unsigned* flag) {
  lds_wait_ge(flag + 0, 1u);
  return __builtin_amdgcn_readfirstlane(flag[1]);
}
__device__ __forceinline__ void dma16(__amdgpu_buffer_rsrc_t r, int voff, unsigned ldsdst) {
  unsigned keep;
  asm volatile("s_mov_b32 %0, m0\n\ts_mov_b32 m0, %3\n\ts_nop 0\n\tbuffer_load_dwordx4 %1, %2, 0 offen" WNT " lds\n\ts_mov_b32 m0, %0"
               : "=&s"(keep) : "v"(voff), "s"(r), "s"(ldsdst) : "memory");
}
__device__ __forceinline__ void dma16p(__amdgpu_buffer_rsrc_t r, int voff, unsigned ldsdst) {
  unsigned keep;
  asm volatile("s_mov_b32 %0, m0\n\ts_mov_b32 m0, %3\n\ts_nop 0\n\tbuffer_load_dwordx4 %1, %2, 0 offen lds\n\ts_mov_b32 m0, %0"
               : "=&s"(keep) : "v"(voff), "s"(r), "s"(ldsdst) : "memory");
}
template <int N>
__device__ __forceinline__ void gatherm(u32x4 (&a)[N], int (&off)[N], unsigned tag, __amdgpu_buffer_rsrc_t r) {
#pragma nounroll
  for (unsigned n = 0; n < SPIN_CAP; ++n) {
    MEM_FENCE();
    u32x4 v[N];
#pragma unroll
    for (int i = 0; i < N; ++i) v[i] = LD128(r, off[i], LDAUX);
    bool all = true;
#pragma unroll
    for (int i = 0; i < N; ++i) {
      const bool mine = off[i] != OOB, ok = v[i][1] == tag && v[i][3] == tag;
      a[i][0] = mine ? v[i][0] : a[i][0];
      a[i][2] = mine ? v[i][2] : a[i][2];
      off[i] = ok ? OOB : off[i];
      all = all && (ok || !mine);
    }
    if (__all(all)) break;
  }
}
template <int STEP>
__device__ __forceinline__ void pass4(u32x4 (&a)[4], const u64 (&m)[4], int off, __amdgpu_buffer_rsrc_t r) {
  asm volatile(
      "s_mov_b64 exec, %[m0]\n\tbuffer_load_dwordx4 %[a0], %[o], %[r], 0 offen sc1\n\t"
      "s_mov_b64 exec, %[m1]\n\tbuffer_load_dwordx4 %[a1], %[o], %[r], 0 offen offset:%c[s1] sc1\n\t"
      "s_mov_b64 exec, %[m2]\n\tbuffer_load_dwordx4 %[a2], %[o], %[r], 0 offen offset:%c[s2] sc1\n\t"
      "s_mov_b64 exec, %[m3]\n\tbuffer_load_dwordx4 %[a3], %[o], %[r], 0 offen offset:%c[s3] sc1\n\t"
      "s_mov_b64 exec, -1\n\ts_waitcnt vmcnt(0)"
      : [a0] "+v"(a[0]), [a1] "+v"(a[1]), [a2] "+v"(a[2]), [a3] "+v"(a[3])
      : [m0] "s"(m[0]), [m1] "s"(m[1]), [m2] "s"(m[2]), [m3] "s"(m[3]), [o] "v"(off), [r] "s"(r), [s1] "i"(STEP), [s2] "i"(2 * STEP), [s3] "i"(3 * STEP)
      : "memory");
}
__device__ __forceinline__ bool missing4(const u32x4 (&a)[4], u64 (&m)[4], unsigned tag) {
#pragma unroll
  for (int i = 0; i < 4; ++i) m[i] &= __builtin_amdgcn_ballot_w64(a[i][1] != tag) | __builtin_amdgcn_ballot_w64(a[i][3] != tag);
  return (m[0] | m[1] | m[2] | m[3]) != 0ull;
}
struct Sel { unsigned lo[4], hi[4]; };
__device__ __forceinline__ Sel make_sel(int lane) {
  Sel s;
  const int j = lane & 7;
#pragma unroll
  for (int t = 0; t < 4; ++t) {
    const bool on = (j >> 1) == t;
    s.lo[t] = on ? ((j & 1) ? 0x01000c0cu : 0x0c0c0100u) : 0x0c0c0c0cu;
    s.hi[t] = on ? ((j & 1) ? 0x03020c0cu : 0x0c0c0302u) : 0x0c0c0c0cu;
  }
  return s;
}
__device__ __forceinline__ u32x4 place(unsigned src, const unsigned (&sel)[4]) {
  u32x4 r;
#pragma unroll
  for (int t = 0; t < 4; ++t) r[t] = __builtin_amdgcn_perm(src, src, sel[t]);
  return r;
}

#define STAMP(i) do {} while (0)

#define KSTAMP_OUT() do {} while (0)
#define KA0 "s36"
#define KA1 "s37"
#define KA6 "s42"
#define KA7 "s43"
#define KB23 "s[54:55]"
#define RT "s[56:59]"
#define RT01 "s[56:57]"
#define RT0 "s56"
#define RT1 "s57"
#define RT2 "s58"
#define RT3 "s59"
__global__ void __launch_bounds__(1024) fused_kernel(const void*, const void*, const void*, const void*, const void*, const void*, const void*,
                                                     const void*, void*, void*) {
  __shared__ __attribute__((aligned(16))) unsigned char smem[L_END];
  LAS unsigned char* L = (LAS unsigned char*)smem;
  volatile LAS unsigned* flag = (volatile LAS unsigned*)(L + L_FLAG);
  STAMP(0);
  const int bid = blockIdx.x, tid = threadIdx.x, lane = tid & 63;
  u32x16 ka;
  u32x4 kb, rt;
  unsigned epochv = 0, wave, t0, t1, t2, v0, v1;
  asm volatile(
      "8:\n\ts_load_dwordx16 %[ka], %[kp], 0x0\n\ts_load_dwordx4 %[kb], %[kp], 0x40\n\t"
      "v_readfirstlane_b32 %[w], %[tid]\n\ts_lshr_b32 %[w], %[w], 6\n\t"
      "s_cmp_lt_u32 %[w], 8\n\ts_cbranch_scc1 1f\n\t"
      "s_add_u32 %[t0], %[bid], 1\n\ts_and_b32 %[t0], %[t0], 3\n\t"
      "s_lshl_b32 %[t2], %[w], 11\n\ts_add_u32 m0, %[t2], %[lx]\n\t"
      "s_cmp_eq_u32 %[w], 8\n\ts_cbranch_scc1 5f\n\t"
      "v_lshrrev_b32 %[v0], 3, %[lane]\n\tv_and_b32 %[v1], 7, %[lane]\n\tv_lshlrev_b32 %[v0], 13, %[v0]\n\tv_lshl_or_b32 %[v0], %[v1], 4, %[v0]\n\t"
      "s_mul_i32 %[t0], %[t0], 0xe0000\n\ts_lshl_b32 %[t1], %[w], 17\n\ts_add_u32 %[t0], %[t0], %[t1]\n\t"
      "s_lshl_b32 %[t1], %[bid], 4\n\ts_and_b32 %[t1], %[t1], 0xf80\n\ts_add_u32 %[t0], %[t0], %[t1]\n\t"
      "s_lshl_b32 %[t1], %[bid], 10\n\ts_and_b32 %[t1], %[t1], 0x1000\n\ts_add_u32 %[t0], %[t0], %[t1]\n\t"
      "s_sub_u32 %[t0], %[t0], 0x120000\n\ts_add_u32 %[t1], %[t0], 0x10000\n\t"
      "s_mov_b32 " RT2 ", 0x380000\n\ts_mov_b32 " RT3 ", 0x20000\n\t"
      "s_waitcnt lgkmcnt(0)\n\ts_mov_b32 " RT0 ", " KA6 "\n\ts_and_b32 " RT1 ", " KA7 ", 0xffff\n\t"
      "s_branch 6f\n"
      "5:\n\t"
      "v_lshrrev_b32 %[v0], 4, %[lane]\n\tv_and_b32 %[v1], 15, %[lane]\n\tv_min_u32 %[v1], 13, %[v1]\n\tv_mul_u32_u24 %[v0], 0x380, %[v0]\n\tv_lshl_add_u32 %[v0], %[v1], 4, %[v0]\n\t"
      "s_mul_i32 %[t0], %[t0], 0xe0\n\tv_add_u32 %[v0], %[t0], %[v0]\n\ts_mov_b32 %[t0], 0\n\ts_movk_i32 %[t1], 0xe00\n\t"
      "s_movk_i32 " RT2 ", 0x1c00\n\ts_mov_b32 " RT3 ", 0x20000\n\t"
      "s_waitcnt lgkmcnt(0)\n\ts_mov_b32 " RT0 ", " KA0 "\n\ts_and_b32 " RT1 ", " KA1 ", 0xffff\n"
      "6:\n\t"
      "buffer_load_dwordx4 %[v0], " RT ", %[t0] offen nt lds\n\t"
      "s_add_u32 m0, m0, 0x400\n\ts_nop 0\n\tbuffer_load_dwordx4 %[v0], " RT ", %[t1] offen nt lds\n\t"
      "s_barrier\n\ts_branch 4f\n"
      "1:\n\ts_cmp_eq_u32 %[w], 0\n\ts_cbranch_scc1 2f\n\t"
      "s_cmp_lt_u32 %[w], 5\n\ts_cbranch_scc1 4f\n\t"
      "s_cmp_eq_u32 %[w], 7\n\ts_cbranch_scc1 3f\n\t"
      "s_cmp_lt_u32 %[bid], 192\n\ts_cbranch_scc1 4f\n\t"
      "s_waitcnt lgkmcnt(0)\n\ts_barrier\n\ts_branch 4f\n"
      "3:\n\t"
      "v_lshlrev_b32 %[v0], 2, %[lane]\n\tds_write_b32 %[v0], %[e]\n\t"
      "s_getpc_b64 " RT01 "\n7:\n\ts_sub_u32 " RT0 ", " RT0 ", 7b-8b\n\ts_subb_u32 " RT1 ", " RT1 ", 0\n\ts_and_b32 " RT0 ", " RT0 ", 0xffffff00\n\ts_and_b32 " RT1 ", " RT1 ", 0xffff\n\t"
      "s_mov_b32 " RT2 ", %[cl]\n\ts_mov_b32 " RT3 ", 0x20000\n\ts_mov_b32 m0, %[lc]\n\t"
      "s_lshr_b32 %[t0], %[bid], 3\n\ts_lshl_b32 %[t0], %[t0], 10\n\tv_lshlrev_b32 %[v0], 4, %[lane]\n\tv_add_u32 %[v0], %[t0], %[v0]\n\t"
      "buffer_load_dwordx4 %[v0], " RT ", 0 offen lds\n\t"
      "s_waitcnt lgkmcnt(0)\n\ts_barrier\n\ts_branch 4f\n"
      "2:\n\ts_waitcnt lgkmcnt(0)\n\tglobal_load_dword %[e], %[e], " KB23 " sc1\n\ts_barrier\n\ts_waitcnt vmcnt(0)\n"
      "4:"
      : [ka] "={s[36:51]}"(ka), [kb] "={s[52:55]}"(kb), [rt] "={s[56:59]}"(rt), [w] "=&s"(wave), [t0] "=&s"(t0), [t1] "=&s"(t1), [t2] "=&s"(t2),
        [v0] "=&v"(v0), [v1] "=&v"(v1), [e] "+v"(epochv)
      : [kp] "{s[0:1]}"(__builtin_amdgcn_kernarg_segment_ptr()), [bid] "{s2}"(bid), [tid] "v"(tid), [lane] "v"(lane), [lx] "i"(L_X - 8 * 2048),
        [lc] "i"(L_WO), [cl] "i"(CODE_BYTES)
      : "memory", "scc");
#define WINDOW(p) (((p) + 0x3fffffull) & ~0x3fffffull)
#define ARG(v, i) ((u64)v[2 * (i)] | ((u64)v[2 * (i) + 1] << 32))
  const int xc = bid & 7, jj = bid >> 3, kvh = jj & 7, s = jj >> 3, b = (xc - 2 * s) & 7;
  const int nkw = s == 3 ? 4 : 6;
  unsigned tag = 0;

  if (__builtin_expect(wave - 1u < (unsigned)nkw, 1)) {
    const bool four = s == 3 && wave >= 3;
    const int urel = four && wave == 4 ? 16 : (wave - 1) * 6;
    const unsigned hsh = (((unsigned)bid * 8u + wave) * 2654435761u) >> 24;
    unsigned ord = (unsigned)((four ? 0x3210321032ull : 0x543210543210ull) >> (((hsh * (four ? 4u : 6u)) >> 8) * 4u));
    int klo = (lane & 7) * 512 + ((lane >> 3) & 1) * 64 + (lane >> 4) * 16 + (s * 36 + urel) * 4096;
    unsigned kvoff = (unsigned)(((b * HKV + kvh) * SK + (SK - WIN)) * DH * 2);
    unsigned brot = (wave + jj + xc) * 128u;
    u32x16 kak = ka;
    u32x4 kbk = kb;
    unsigned d0, d1, wooff = (kvh * 512 + s * 64) * (DM * 2), w2off = (kvh * 4 + s) * 56 * (DM * 2);
    asm volatile("s_waitcnt lgkmcnt(0)\n\t"
                 "s_cmp_gt_u32 %[w], 2\n\ts_cbranch_scc1 1f\n\t"
                 "s_cmp_eq_u32 %[w], 2\n\ts_cbranch_scc1 2f\n\t"
                 "s_load_dword %[d0], s[38:39], %[kv]\n\ts_load_dword %[d1], s[40:41], %[kv]\n\ts_branch 4f\n"
                 "2:\n\ts_load_dword %[d0], s[44:45], %[wo]\n\ts_load_dword %[d1], s[48:49], %[w2]\n\ts_branch 4f\n"
                 "1:\n\ts_cmp_gt_u32 %[w], 4\n\ts_cbranch_scc1 4f\n\t"
                 "s_cmp_eq_u32 %[w], 4\n\ts_cbranch_scc1 3f\n\t"
                 "s_add_u32 vcc_lo, s54, 0x3fffff\n\ts_addc_u32 vcc_hi, s55, 0\n\ts_and_b32 vcc_lo, vcc_lo, 0xffc00000\n\t"
                 "s_load_dword %[d0], vcc, 0x800\n\ts_orn2_b32 vcc_lo, vcc_lo, 0xffdfffff\n\ts_branch 4f\n"
                 "3:\n\ts_load_dword %[d0], s[50:51], 0x0\n\ts_load_dword %[d1], s[46:47], 0x0\n"
                 "4:\n\ts_barrier"
                 : "+{s[36:51]}"(kak), "+{s[52:55]}"(kbk), "+v"(klo), [kv] "+s"(kvoff), "+s"(ord), "+s"(brot), [d0] "=&s"(d0), [d1] "=&s"(d1)
                 : [w] "s"(wave), [wo] "s"(wooff), [w2] "s"(w2off)
                 : "memory", "scc", "vcc");
    const auto rk = RSRC(ARG(kak, 1) + kvoff, WIN * DH * 2);
    u32x4 r[24];
    STAMP(4);
#pragma unroll
    for (int i = 0; i < 24; ++i) {
      if (i >= WK0) vmwait<WK - 1>();
      if (i == WK0) STAMP(5);
      r[i] = __builtin_amdgcn_raw_buffer_load_b128(rk, i < 16 || !four ? klo : OOB, ((ord >> (i & ~3)) & 15u) * 4096 + ((brot + i * 128) & 0x180), KAUX);
      SCHED_FENCE();
      if (i == KTRIG) lds_inc(L, F_KISS, lane);
    }
    STAMP(1);
    asm volatile("s_waitcnt lgkmcnt(0)" : "+s"(d0), "+s"(d1)::"memory");
    const u64 ws = WINDOW(ARG(kbk, 1));
    const auto rws = RSRC(ws, WS_END);
    const int kw = wave - 1, nu = four ? 4 : 6, ulo = s * 36, uhi = s == 3 ? 128 : ulo + 36, u0 = ulo + urel;
    __builtin_amdgcn_s_setprio(2);
    lds_wait_ge(flag + F_QRDY, 1u);
    tag = __builtin_amdgcn_readfirstlane(flag[F_TAG]);
    STAMP(2);
    u32x4 qa[4];
#pragma unroll
    for (int t = 0; t < 4; ++t) qa[t] = *(const LAS u32x4*)(L + L_Q + (lane & 1) * 512 + ((brot + t * 128) & 0x180) + ((lane >> 1) & 1) * 64 + (lane >> 4) * 16);
    float s0 = 0.f, s1 = 0.f;
#pragma unroll
    for (int u = 0; u < 6; ++u) {
      f32x4 c = {0, 0, 0, 0};
#pragma unroll
      for (int t = 0; t < 4; ++t) c = mfma(qa[t], r[4 * u + t], c);
      float c0 = (lane & 8) ? c[2] : c[0], c1 = (lane & 8) ? c[3] : c[1];
      c0 += dpp<ROR8>(c0); c1 += dpp<ROR8>(c1);
      if ((unsigned)(lane >> 3) == ((ord >> (4 * u)) & 15u) && (u < 4 || !four)) { s0 = c0; s1 = c1; }
    }
    const unsigned pk = pk_bf16(s0, s1);
    const unsigned pa = bperm(pk, (2 * lane) & 63), pb = bperm(pk, (2 * lane + 1) & 63);
    if (lane < 4 * nu) {
      ST128((u32x4{pa, tag, pb, tag}), rws, WS_S + (b * 8 + kvh) * 8192 + u0 * 64 + lane * 16, STAUX);
      *(LAS u32x4*)(L + L_SM + urel * 64 + lane * 16) = u32x4{pa, tag, pb, tag};
    }
    MEM_FENCE();
    lds_inc(L, F_KCNT, lane);
    STAMP(3);
    if (wave <= 4) {
      u32x4 sg[2];
      int of[2];
      bool own[2];
#pragma unroll
      for (int i = 0; i < 2; ++i) {
        const int myu = (kw + 4 * i) * 16 + (lane >> 2);
        own[i] = myu >= ulo && myu < uhi;
        of[i] = own[i] ? OOB : WS_S + (b * 8 + kvh) * 8192 + (kw + 4 * i) * 1024 + lane * 16;
      }
      __builtin_amdgcn_s_sleep(SSLEEP);
      gatherm<2>(sg, of, tag, rws);
      lds_wait_ge(flag + F_KCNT, (unsigned)nkw);
#pragma unroll
      for (int i = 0; i < 2; ++i) {
        const int c = kw + 4 * i;
        const u32x4 mv = *(const LAS u32x4*)(L + L_SM + (own[i] ? (c * 16 - ulo) * 64 + lane * 16 : 0));
        const unsigned sa = own[i] ? mv[0] : sg[i][0], sb = own[i] ? mv[2] : sg[i][2];
        const float m0 = wave_max(fmaxf(bf_lo(sa), bf_lo(sb))), m1 = wave_max(fmaxf(bf_hi(sa), bf_hi(sb)));
        const float n0 = m0 == -INFINITY ? 0.f : m0, n1 = m1 == -INFINITY ? 0.f : m1;
        const float ea = __builtin_amdgcn_exp2f((bf_lo(sa) - n0) * C2), eb = __builtin_amdgcn_exp2f((bf_lo(sb) - n0) * C2);
        const float ec = __builtin_amdgcn_exp2f((bf_hi(sa) - n1) * C2), ed = __builtin_amdgcn_exp2f((bf_hi(sb) - n1) * C2);
        *(LAS unsigned*)(L + L_P + c * 512 + lane * 4) = pk_bf16(ea, eb);
        *(LAS unsigned*)(L + L_P + c * 512 + 256 + lane * 4) = pk_bf16(ec, ed);
        const float l0 = wave_sum(ea + eb), l1 = wave_sum(ec + ed);
        if (lane == 0) *(LAS f32x4*)(L + L_ST + c * 16) = f32x4{m0, l0, m1, l1};
      }
      MEM_FENCE();
      lds_inc(L, F_PR + kw, lane);
      lds_inc(L, F_SCNT, lane);
      STAMP(6);
    }
    KSTAMP_OUT();
    return;
  }
  const GAS u16* x = (const GAS u16*)ARG(ka, 0);
  const u64 vc = ARG(ka, 2), wo = ARG(ka, 4), w1 = ARG(ka, 5), w2 = ARG(ka, 6), ws = WINDOW(ARG(kb, 1));
  const GAS u16* gw = (const GAS u16*)ARG(ka, 7);
  GAS u16* out = (GAS u16*)ARG(kb, 0);
  const auto rws = RSRC(ws, WS_END);
  const int cb = jj + 32 * (xc >> 2), rb = (xc + 1) & 3;
  const int grp = kvh * 4 + s;
  if (wave >= 8) {
    const unsigned kvoff = (unsigned)(((b * HKV + kvh) * SK + (SK - WIN)) * DH * 2);
    const int w = wave - 8;
    vmwait<0>();
    lds_inc(L, F_WQC, lane);
    tag = get_tag(flag);
    if (w >= 4) {
      __builtin_amdgcn_s_setprio(3);
      const int ct = w - 4;
      const int xrow = ((lane & 7) >> 1) + 4 * (lane & 1);
      LAS unsigned char* xp = L + L_X + xrow * 256 + (lane >> 4) * 16;
      LAS unsigned char* xp2 = L + L_X + xrow * 256 + 192 + (lane >> 4) * 8;
      LAS unsigned char* tb = L + L_WQ + ((lane >> 4) * 8 + ((lane >> 2) & 3)) * 128 + (lane & 3) * 8 + ct * 32;
      LAS unsigned char* tb2 = L + L_WQ + (96 + (lane >> 4) * 4 + ((lane >> 2) & 3)) * 128 + (lane & 3) * 8 + ct * 32;
      const int dest = ((lane >> 4) & 1) * 2 + (lane >> 5);
      const int qo = WS_QP + (cb * 4 + dest) * QPSTR + rb * 1024 + ct * 256 + (lane & 15) * 16;
      lds_wait_ge(flag + F_WQC, 8u);
      STAMP(1);
      const u32x2 pt = trd(tb2), xb = *(const LAS u32x2*)xp2;
      f32x4 acc = mfma(u32x4{xb[0], xb[1], 0u, 0u}, u32x4{pt[0], pt[1], 0u, 0u}, f32x4{0, 0, 0, 0});
#pragma unroll
      for (int ks = 0; ks < 3; ++ks) {
        const u32x2 p0 = trd(tb + ks * 4096), p1 = trd(tb + ks * 4096 + 512);
        acc = mfma(*(const LAS u32x4*)(xp + ks * 64), u32x4{p0[0], p0[1], p1[0], p1[1]}, acc);
      }
      const float q0 = lane >= 32 ? acc[2] : acc[0], q1 = lane >= 32 ? acc[3] : acc[1];
      if (dest != rb) ST128((u32x4{__float_as_uint(q0), tag, __float_as_uint(q1), tag}), rws, qo, STAUX);
      else *(LAS f32x2*)(L + L_QO + (ct * 16 + (lane & 15)) * 8) = f32x2{q0, q1};
      LGKM0();
      lds_inc(L, F_QPC, lane);
      __builtin_amdgcn_s_setprio(0);
    }
    const auto rv = RSRC(vc + kvoff, WIN * DH * 2);
    u32x4 r[16];
    const int trot = w + jj + xc;
    const int voff = (w * 128 + (lane >> 4) * 8 + ((lane >> 3) & 1)) * 512 + s * 128 + (lane & 7) * 16;
    const Sel sel = make_sel(lane);
    lds_wait_ge(flag + F_KISS, (unsigned)nkw);
    STAMP(2);
#define V_ISSUE(i) do { if ((i) > VSPLIT) vmwait<WVE - 1>(); else if ((i) >= WV) { if (S3W && ((S3W == 2) || ((i) & 1)) && s == 3) vmwait<WV>(); else vmwait<WV - 1>(); } \
      r[i] = __builtin_amdgcn_raw_buffer_load_b128(rv, voff + ((i) & 3) * 1024, ((((i) >> 2) + trot) & 3) * 16384, VAUX); SCHED_FENCE(); \
      if ((i) == VTRIG) lds_inc(L, F_VISS, lane); } while (0)
#define V_USE(i) do { a0 = mfma(place(pp[(i) >> 2][(i) & 3], sel.lo), r[i], a0); a1 = mfma(place(pp[(i) >> 2][(i) & 3], sel.hi), r[i], a1); } while (0)
#pragma unroll
    for (int i = 0; i <= VSPLIT; ++i) V_ISSUE(i);
    STAMP(3);
    u32x4 pp[4];
    f32x4 a0 = {0, 0, 0, 0}, a1 = {0, 0, 0, 0};
#define P_READ() _Pragma("unroll") for (int t = 0; t < 4; ++t) \
      pp[t] = *(const LAS u32x4*)(L + L_P + w * 512 + ((lane >> 3) & 1) * 256 + (((t + trot) & 3) * 32 + (lane >> 4) * 8) * 2)
    float f;
    unsigned wad = (unsigned)(uintptr_t)(L + L_OP) + w * 512 + ((lane >> 5) * 64 + (lane & 7) * 8 + ((lane >> 4) & 1) * 4) * 4;
    unsigned rad = (unsigned)(uintptr_t)(L + L_OP) + (lane >> 5) * 2048 + (lane & 31) * 16;
    int oad = WS_OB + inbox(grp) + b * 512 + lane * 16;
#define F_GET() do { lds_wait_ge(flag + F_FRDY, 1u); f = *(const LAS float*)(L + L_FW + w * 8 + (lane >> 5) * 4); \
      asm volatile("" : "+v"(f), "+v"(wad), "+v"(rad), "+v"(oad)); } while (0)
    if (__builtin_expect(__builtin_amdgcn_readfirstlane(flag[F_PR + (w & 3)]) != 0u, 1)) {
      MEM_FENCE();
      STAMP(4);
      P_READ();
      constexpr int PER = VPER;
#pragma unroll
      for (int i = VSPLIT + 1; i < 16; ++i) {
        V_ISSUE(i);
#pragma unroll
        for (int k = 0; k < 16; ++k)
          if (k >= (i - VSPLIT - 1) * PER && k < (i - VSPLIT) * PER && k <= i - WVE) V_USE(k);
        SCHED_FENCE();
      }
#pragma unroll
      for (int k = 0; k < 16 - WVE; ++k) {
        bool done = false;
#pragma unroll
        for (int i = VSPLIT + 1; i < 16; ++i) done = done || (k >= (i - VSPLIT - 1) * PER && k < (i - VSPLIT) * PER && k <= i - WVE);
        if (!done) { V_USE(k); if (k & 1) SCHED_FENCE(); }
      }
      SCHED_FENCE();
      F_GET();
      u32x4 la[WVE], lb[WVE];
#pragma unroll
      for (int k = 0; k < WVE; ++k) {
        la[k] = place(pp[(16 - WVE + k) >> 2][(16 - WVE + k) & 3], sel.lo);
        lb[k] = place(pp[(16 - WVE + k) >> 2][(16 - WVE + k) & 3], sel.hi);
        asm volatile("" : "+v"(la[k]), "+v"(lb[k]));
      }
      SCHED_FENCE();
#pragma unroll
      for (int k = 0; k < WVE; ++k) { a0 = mfma(la[k], r[16 - WVE + k], a0); a1 = mfma(lb[k], r[16 - WVE + k], a1); }
    } else {
#pragma unroll
      for (int i = VSPLIT + 1; i < 16; ++i) V_ISSUE(i);
      STAMP(6);
      lds_wait_ge(flag + F_PR + (w & 3), 1u);
      P_READ();
#pragma unroll
      for (int k = 0; k < 16; ++k) { V_USE(k); if (k & 1) SCHED_FENCE(); }
      F_GET();
    }
    f32x4 t = (lane & 8) ? a1 : a0;
#pragma unroll
    for (int k = 0; k < 4; ++k) t[k] = (t[k] + dpp<ROR8>(t[k])) * f;
    STAMP(5);
    unsigned old;
    f32x4 o0v, o1v, o2v, o3v;
    asm volatile("ds_write_b128 %[wa], %[t]\n\tds_add_rtn_u32 %[old], %[ca], %[one]\n\t"
                 "ds_read_b128 %[p0], %[ra]\n\tds_read_b128 %[p1], %[ra] offset:512\n\tds_read_b128 %[p2], %[ra] offset:1024\n\tds_read_b128 %[p3], %[ra] offset:1536\n\t"
                 "s_waitcnt lgkmcnt(0)"
                 : [old] "=&v"(old), [p0] "=&v"(o0v), [p1] "=&v"(o1v), [p2] "=&v"(o2v), [p3] "=&v"(o3v)
                 : [wa] "v"(wad), [t] "v"(t), [ca] "v"((unsigned)(uintptr_t)(L + (lane == 0 ? F_OCNT * 4 : 128 + (lane & 31) * 4))), [one] "v"(1u), [ra] "v"(rad)
                 : "memory");
    if (__builtin_amdgcn_readfirstlane(old) == 7u) {
      f32x4 ov = (o0v + o1v) + (o2v + o3v);
#pragma unroll
      for (int k = 0; k < 4; ++k) { float hi, lo = x32_a(ov[k], hi); ov[k] = lo + hi; }
      if (lane < 32) {
        const u32x4 ogr = {pk_bf16(ov[0], ov[1]), tag, pk_bf16(ov[2], ov[3]), tag};
        ST128(ogr, rws, oad, STAUX);
        ST128(ogr, rws, oad + 8192, STAUX);
        *(LAS u32x4*)(L + L_OM + lane * 16) = ogr;
      }
      MEM_FENCE();
      lds_inc(L, F_OMR, lane);
    }
  } else if (wave == 0) {
    __builtin_amdgcn_s_setprio(3);
    tag = __builtin_amdgcn_readfirstlane(epochv) * 1664525u + 1013904223u;
    if (lane == 0) *(volatile LAS u32x2*)flag = u32x2{1u, tag};
    lds_wait_ge(flag + F_QPC, 4u);
    STAMP(2);
    __builtin_amdgcn_s_sleep(QSLEEP1);
    {
      const int po = WS_QP + (cb * 4 + rb) * QPSTR + lane * 16;
      u32x4 pg[3];
      int of[3] = {po + ((rb + 1) & 3) * 1024, po + ((rb + 2) & 3) * 1024, po + ((rb + 3) & 3) * 1024};
      gatherm<3>(pg, of, tag, rws);
      const f32x2 own = *(const LAS f32x2*)(L + L_QO + lane * 8);
      const float a0 = (own[0] + __uint_as_float(pg[0][0])) + (__uint_as_float(pg[1][0]) + __uint_as_float(pg[2][0]));
      const float a1 = (own[1] + __uint_as_float(pg[0][2])) + (__uint_as_float(pg[1][2]) + __uint_as_float(pg[2][2]));
      const float n0 = dpp<XOR1>(a0), n1 = dpp<XOR1>(a1);
      const unsigned pk = (lane & 1) ? pk_bf16(n1, a1) : pk_bf16(a0, n0);
      const int src = (lane & 15) * 4 + ((lane >> 4) & 1);
      const unsigned pa = bperm(pk, src), pb = bperm(pk, src + 2);
      if (lane < 32) ST128((u32x4{pa, tag, pb, tag}), rws, WS_Q + ((cb >> 3) * 8 + rb + 4 * (lane >> 4)) * QSTR + (cb & 7) * 256 + (lane & 15) * 16, STAUX);
    }
    STAMP(6);
    __builtin_amdgcn_s_sleep(QSLEEP2);
    {
      const int qoff = WS_Q + (kvh * 8 + b) * QSTR + lane * 16;
      u32x4 qq[2];
      int of[2] = {qoff, qoff + 1024};
      gatherm<2>(qq, of, tag, rws);
      *(LAS u32x2*)(L + L_Q + lane * 8) = u32x2{qq[0][0], qq[0][2]};
      *(LAS u32x2*)(L + L_Q + 512 + lane * 8) = u32x2{qq[1][0], qq[1][2]};
      LGKM0();
      if (lane == 0) flag[F_QRDY] = 1u;
    }
    STAMP(3);
    u32x4 xg = {0, 0, 0, 0}, gg = {0, 0, 0, 0};
    if (lane < 56) {
      xg = *(const GAS u32x4*)(x + b * DM + lane * 8);
      gg = *(const GAS u32x4*)(gw + lane * 8);
    }
    float z = 0.f;
#pragma unroll
    for (int t = 0; t < 4; ++t) z += rbf(bf_lo(xg[t]) * bf_lo(gg[t])) + rbf(bf_hi(xg[t]) * bf_hi(gg[t]));
    z = rbf(wave_sum(z));
    const float g = rbf(1.f / (1.f + __expf(-z)));
    const unsigned g1u = __float_as_uint(rbf(1.f - g));
    if (lane == 0) *(volatile LAS u32x2*)(flag + F_G1V) = u32x2{1u, g1u};
    lds_wait_ge(flag + F_SCNT, 4u);
    {
      const f32x2 ml = *(const LAS f32x2*)(L + L_ST + (lane & 15) * 8);
      float M = fmaxf(ml[0], dpp<XOR2>(ml[0]));
      M = fmaxf(M, dpp<0x124>(M)); M = fmaxf(M, dpp<ROR8>(M));
      const float al = __builtin_amdgcn_exp2f((ml[0] - M) * C2);
      float Ls = al * ml[1];
      Ls += dpp<XOR2>(Ls); Ls += dpp<0x124>(Ls); Ls += dpp<ROR8>(Ls);
      if (lane < 16) *(LAS float*)(L + L_FW + lane * 4) = al * (g / Ls);
      LGKM0();
      if (lane == 0) flag[F_FRDY] = 1u;
    }
    STAMP(4);
  } else if (wave != 7 && xc < 7) {
    const int j = wave - 5, t = xc * 8 + kvh;
    const unsigned lb = (unsigned)(uintptr_t)(L + L_T) + j * 14336;
    const auto r1 = RSRC(w1, DM * FF * 2);
    const int o = ((j * 224 + (lane >> 2)) * FF + t * 32 + (lane & 3) * 8) * 2;
#pragma unroll
    for (int i = 0; i < 14; ++i) {
      if (i >= WT) vmwait<WT - 1>();
      if (W1P) dma16p(r1, o + i * 16 * FF * 2, lb + i * 1024); else dma16(r1, o + i * 16 * FF * 2, lb + i * 1024);
    }
    {
      const auto rx = RSRC((u64)x, NB * DM * 2);
#pragma unroll
      for (int i = 0; i < 4; ++i) {
        vmwait<WT - 1>();
        dma16p(rx, (j * 4 + i) * 1024 + lane * 16, (unsigned)(uintptr_t)(L + L_XA) + (j * 4 + i) * 1024);
      }
    }
    tag = get_tag(flag);
    vmwait<0>();
    lds_inc(L, F_TCNT, lane);
    lds_wait_ge(flag + F_TCNT, 2u);
    STAMP(6);
    LAS unsigned char* tb = L + L_T + ((lane >> 4) * 8 + ((lane >> 2) & 3)) * 64 + j * 32 + (lane & 3) * 8;
    f32x4 acc0 = {0, 0, 0, 0}, acc1 = {0, 0, 0, 0};
#pragma unroll
    for (int ks = 0; ks < 14; ++ks) {
      const u32x2 p0 = trd(tb + ks * 2048), p1 = trd(tb + ks * 2048 + 256);
      const u32x4 xa = *(const LAS u32x4*)(L + L_XA + (lane & 7) * (DM * 2) + ks * 64 + (lane >> 4) * 16);
      if (ks & 1) acc1 = mfma(u32x4{p0[0], p0[1], p1[0], p1[1]}, xa, acc1);
      else acc0 = mfma(u32x4{p0[0], p0[1], p1[0], p1[1]}, xa, acc0);
    }
    acc0 += acc1;
    float hv[4];
#pragma unroll
    for (int k = 0; k < 4; ++k) {
      const float v = rbf(acc0[k]);
      hv[k] = v / (1.f + __expf(-v));
    }
    if (!(lane & 8))
      ST128((u32x4{pk_bf16(hv[0], hv[1]), tag, pk_bf16(hv[2], hv[3]), tag}), rws, WS_HB + t * 1024 + j * 512 + (lane >> 4) * 128 + (lane & 7) * 16, STAUX);
  }
  const int cpair = (xc ^ 1) * 32 + jj;
  if (wave == 7) {
    const int fgrp = b == 7 ? -1 : s < 3 ? kvh * 3 + s : (kvh < 4 ? 24 + kvh : -1);
    tag = get_tag(flag);
    const bool hi8 = lane & 8;
    const unsigned upper = (__builtin_amdgcn_s_getreg(20 | (3 << 11)) >> 2) & 1u;
    if (s == 0) {
      const u64 pa = ws + WS_PR + kvh * 16384 + b * 1024;
      int vote = 0;
#pragma unroll
      for (int i = 0; i < NPAIR; ++i) {
        u64 t0, t1, t2;
        unsigned d;
        asm volatile("s_memtime %0\n\ts_waitcnt lgkmcnt(0)\n\ts_load_dword %3, %4, %5\n\ts_waitcnt lgkmcnt(0)\n\ts_memtime %1\n\ts_waitcnt lgkmcnt(0)\n\t"
                     "s_load_dword %3, %4, %6\n\ts_waitcnt lgkmcnt(0)\n\ts_memtime %2\n\ts_waitcnt lgkmcnt(0)"
                     : "=&s"(t0), "=&s"(t1), "=&s"(t2), "=&s"(d) : "s"(pa), "i"(i * 128), "i"(i * 128 + 8192) : "memory");
        vote += (unsigned)(t1 - t0) > (unsigned)(t2 - t1) ? 1 : -1;
      }
      lds_wait_ge(flag + F_G1V, 1u);
      if (lane < 8) ST128((u32x4{flag[F_G1], tag, (unsigned)(upper ? -vote : vote), tag}), rws, WS_G + (kvh * 8 + b) * 128 + lane * 16, STAUX);
      STAMP(1);
    }
    u32x4 hb[3];
    int nearc;
    {
      const unsigned lb = (unsigned)(uintptr_t)(L + L_WO);
      const auto r2 = RSRC(w2, FF * DM * 2);
      const auto ro = RSRC(wo, NQ * DM * 2);
      const int off2 = (fgrp * 64 + (lane >> 3)) * 896 + b * 128 + (lane & 7) * 16;
      const int offo = (kvh * 512 + s * 64 + (lane >> 3)) * 896 + b * 128 + (lane & 7) * 16;
      lds_wait_ge(flag + HFLAG, HTRIG);
      {
        int of[3] = {fgrp >= 0 ? WS_HB + fgrp * 2048 + lane * 16 : OOB, fgrp >= 0 ? WS_HB + fgrp * 2048 + 1024 + lane * 16 : OOB, WS_G + (kvh * 8 + (lane & 7)) * 128};
        gatherm<3>(hb, of, tag, rws);
        float vs = (float)(int)hb[2][2];
        vs += dpp<XOR1>(vs); vs += dpp<XOR2>(vs); vs += dpp<HMIRROR>(vs);
        nearc = (((__builtin_amdgcn_readfirstlane(__float_as_uint(vs)) >> 31) ^ 1u ^ upper ^ FLIP) & 1u) * 8192;
        if (lane == 0) *(volatile LAS u32x2*)(flag + F_NRV) = u32x2{1u, (unsigned)nearc};
      }
      if (b == 7) { KSTAMP_OUT(); return; }
      lds_wait_ge(flag + F_VISS, 8u);
      STAMP(2);
      if (fgrp >= 0) {
#pragma unroll
        for (int i = 0; i < 8; ++i) dma16(r2, off2 + i * 8 * 896, lb + 16384 + i * 1024);
      }
#pragma unroll
      for (int i = 0; i < 16; ++i) dma16(ro, offo + ((i >> 3) * 256 + (i & 7) * 8) * 896, lb + i * 1024);
      STAMP(3);
    }
    __builtin_amdgcn_s_setprio(3);
    const bool ownb = (lane & 7) == b;
    const int ooff = WS_OB + nearc + inbox(grp) + (lane & 7) * 512 + (lane >> 3) * 16;
    u32x4 ob[4];
#pragma unroll
    for (int i = 0; i < 4; ++i) ob[i] = LD128(rws, ownb ? OOB : ooff + i * 128, LDAUX);
    SCHED_FENCE();
    LAS unsigned char* tb = L + L_WO + ((lane >> 4) * 8 + ((lane >> 2) & 3)) * 128 + (lane & 3) * 8;
    f32x4 acc[4] = {{0, 0, 0, 0}, {0, 0, 0, 0}, {0, 0, 0, 0}, {0, 0, 0, 0}};
    if (fgrp >= 0) {
      vmwait<20>();
      const u32x4 hv = {hb[0][0], hb[0][2], hb[1][0], hb[1][2]};
      const float g1 = __uint_as_float(hb[2][0]);
#pragma unroll
      for (int ks = 0; ks < 2; ++ks) {
        const u32x4 bf = {hi8 ? 0u : hv[2 * ks], hi8 ? 0u : hv[2 * ks + 1], hi8 ? hv[2 * ks] : 0u, hi8 ? hv[2 * ks + 1] : 0u};
#pragma unroll
        for (int ct = 0; ct < 4; ++ct) {
          const u32x2 p0 = trd(tb + 16384 + ks * 4096 + ct * 32), p1 = trd(tb + 16384 + ks * 4096 + 512 + ct * 32);
          acc[ct] = mfma(u32x4{p0[0], p0[1], p1[0], p1[1]}, bf, acc[ct]);
        }
      }
#pragma unroll
      for (int ct = 0; ct < 4; ++ct) acc[ct] *= g1;
    }
    u32x4 wf[4][4];
#pragma unroll
    for (int ks = 0; ks < 4; ++ks) {
      if (ks == 0) vmwait<16>(); else if (ks == 1) vmwait<12>(); else if (ks == 2) vmwait<8>(); else vmwait<4>();
#pragma unroll
      for (int ct = 0; ct < 4; ++ct) {
        const u32x2 p0 = trd(tb + ks * 4096 + ct * 32), p1 = trd(tb + ks * 4096 + 512 + ct * 32);
        wf[ks][ct] = u32x4{p0[0], p0[1], p1[0], p1[1]};
      }
    }
    int po = WS_PO + (nearc ^ (((xc ^ b) >> 2) & 1) * 8192) + inbox(b * 32 + (lane >> 3)) + grp * 128 + (lane & 7) * 16;
    asm volatile("" : "+v"(po));
    STAMP(4);
    lds_wait_ge(flag + F_OMR, 1u);
#pragma unroll
    for (int i = 0; i < 4; ++i) {
      const u32x4 v = *(const LAS u32x4*)(L + L_OM + i * 128 + (lane >> 3) * 16);
      ob[i] = ownb ? v : ob[i];
    }
    u64 ms[4] = {~0ull, ~0ull, ~0ull, ~0ull};
    if (missing4(ob, ms, tag)) {
#pragma nounroll
      for (unsigned n = 0; n < SPIN_CAP; ++n) {
        pass4<128>(ob, ms, ooff, rws);
        if (!missing4(ob, ms, tag)) break;
      }
    }
    STAMP(6);
    u32x4 bf[4];
#pragma unroll
    for (int ks = 0; ks < 4; ++ks) bf[ks] = u32x4{hi8 ? 0u : ob[ks][0], hi8 ? 0u : ob[ks][2], hi8 ? ob[ks][0] : 0u, hi8 ? ob[ks][2] : 0u};
#pragma unroll
    for (int ct = 0; ct < 4; ++ct)
#pragma unroll
      for (int ks = 0; ks < 4; ++ks) acc[ct] = mfma(wf[ks][ct], bf[ks], acc[ct]);
    u32x4 pv[4];
#pragma unroll
    for (int ct = 0; ct < 4; ++ct) {
      const float k0 = hi8 ? acc[ct][2] : acc[ct][0], k1 = hi8 ? acc[ct][3] : acc[ct][1];
      const float s0 = hi8 ? acc[ct][0] : acc[ct][2], s1 = hi8 ? acc[ct][1] : acc[ct][3];
      pv[ct] = u32x4{__float_as_uint(k0 + dpp<ROR8>(s0)), tag, __float_as_uint(k1 + dpp<ROR8>(s1)), tag};
      __builtin_amdgcn_raw_buffer_store_b128(pv[ct], rws, po, ct * 4 * 16384, STAUX);
    }
    STAMP(5);
    po ^= 8192;
#pragma unroll
    for (int ct = 0; ct < 4; ++ct) __builtin_amdgcn_raw_buffer_store_b128(pv[ct], rws, po, ct * 4 * 16384, STAUX);
  } else if (wave == 0 && cpair < 224) {
    __builtin_amdgcn_s_setprio(3);
    lds_wait_ge(flag + F_OCNT, 8u);
    STAMP(5);
    __builtin_amdgcn_s_sleep(FSLEEP);
    lds_wait_ge(flag + F_NRV, 1u);
    const int poff = WS_PO + __builtin_amdgcn_readfirstlane(flag[F_NR]) + inbox(cpair) + lane * 16;
    u32x4 pg[4] = {{0, 0, 0, 0}, {0, 0, 0, 0}, {0, 0, 0, 0}, {0, 0, 0, 0}};
    u64 ms[4] = {~0ull, ~0ull, ~0ull, ~0ull};
    GAS unsigned* op = (GAS unsigned*)(out + (lane & 7) * DM + cpair * 2);
    asm volatile("" : "+v"(op));
#pragma nounroll
    for (unsigned n = 0; n < SPIN_CAP; ++n) {
      pass4<1024>(pg, ms, poff, rws);
      if (!missing4(pg, ms, tag)) {
        float a0 = (__uint_as_float(pg[0][0]) + __uint_as_float(pg[1][0])) + (__uint_as_float(pg[2][0]) + __uint_as_float(pg[3][0]));
        float a1 = (__uint_as_float(pg[0][2]) + __uint_as_float(pg[1][2])) + (__uint_as_float(pg[2][2]) + __uint_as_float(pg[3][2]));
        a0 += dpp<ROR8>(a0); a1 += dpp<ROR8>(a1);
        float o, t;
        t = x16_a(a0, o); a0 = t + o; t = x16_a(a1, o); a1 = t + o;
        t = x32_a(a0, o); a0 = t + o; t = x32_a(a1, o); a1 = t + o;
        if (lane < 8) *op = pk_bf16(a0, a1);
        if (bid == 0 && lane == 0) __hip_atomic_store((GAS unsigned*)ARG(kb, 1), tag, __ATOMIC_RELAXED, __HIP_MEMORY_SCOPE_AGENT);
        STAMP(1);
        break;
      }
    }
  }
  KSTAMP_OUT();
}

extern "C" void kernel_launch(void* const* d_in, const int* in_sizes, int n_in, void* d_out, int out_size, void* d_ws, size_t ws_size,
                              hipStream_t stream) {
  fused_kernel<<<256, 1024, 0, stream>>>(d_in[0], d_in[1], d_in[2], d_in[3], d_in[4], d_in[6], d_in[7], d_in[8], d_out, d_ws);
}
```

```cpp
#include <hip/hip_runtime.h>
#include <hip/hip_bf16.h>
#include <stdint.h>

constexpr int CODE_BYTES = 18944;
constexpr int WK0 = 5, WK = 4, WV = 3, WT = 3;
constexpr int VSPLIT = 12, VPER = 3;
#define QSLEEP 10
#define SSLEEP 24
#define FSLEEP 10
constexpr int NPAIR = 6;

typedef unsigned u32x4 __attribute__((ext_vector_type(4)));
typedef unsigned u32x2 __attribute__((ext_vector_type(2)));
typedef float f32x4 __attribute__((ext_vector_type(4)));
typedef float f32x2 __attribute__((ext_vector_type(2)));
typedef short s16x4 __attribute__((ext_vector_type(4)));
typedef __bf16 bf16x2 __attribute__((ext_vector_type(2)));
typedef __bf16 bf16x8 __attribute__((ext_vector_type(8)));
typedef unsigned short u16;
typedef unsigned u32x16 __attribute__((ext_vector_type(16)));
typedef unsigned long long u64;
#define LAS __attribute__((address_space(3)))
#define GAS __attribute__((address_space(1)))

constexpr int NB = 8, DM = 448, HKV = 8, SK = 4096, DH = 256, WIN = 1024, FF = 1792, NQ = 4096;
constexpr int QPSTR = 4096 + 256, QSTR = 2048 + 128;
constexpr int WS_G = 4096;
constexpr int WS_QP = 16384;
constexpr int WS_Q = WS_QP + 256 * QPSTR;
constexpr int WS_HB = WS_Q + 64 * QSTR;
constexpr int WS_S = WS_HB + 65536;
constexpr int WS_PR = (WS_S + 524288 + 16383) & ~16383;
constexpr int WS_OB = 2 << 20;
constexpr int WS_PO = WS_OB + 16 * 16384;
constexpr int WS_END = WS_PO + 112 * 16384;
static_assert(WS_PR + 8 * 16384 <= WS_OB && WS_END == 4 << 20, "two 2 MB pages");
__device__ __forceinline__ int inbox(int k) { return (k >> 1) * 16384 + (k & 1) * 4096; }
constexpr int L_FLAG = 0, L_Q = 256, L_QO = L_Q + 1024, L_ST = L_QO + 512, L_FW = L_ST + 128, L_OM = L_FW + 64, L_SM = L_OM + 512, L_P = L_SM + 2304, L_OP = L_P + 4096, L_WO = L_OP + 4096, L_W2 = L_WO + 16384,
              L_X = L_W2 + 8192, L_WQ = L_X + 2048, L_T = L_WQ + 14336, L_XA = L_T + 28672, L_END = L_XA + 8192;
enum { F_TAGV = 0, F_TAG = 1, F_QRDY = 2, F_WQC = 3, F_QPC = 4, F_KISS = 5, F_OCNT = 6, F_TCNT = 7, F_VISS = 8, F_SCNT = 9, F_FRDY = 10, F_KCNT = 11, F_OMR = 12, F_G1V = 14, F_G1 = 15, F_PR = 16, F_NRV = 20, F_NR = 21 };
constexpr int AUX_NT = 2, AUX_SC1 = 16;
constexpr float C2 = 0.0625f * 1.4426950408889634f;
constexpr int OOB = 0x7f000000;
constexpr unsigned SPIN_CAP = 1u << 18;

#define RSRC(p, n) __builtin_amdgcn_make_buffer_rsrc((void*)(p), 0, (n), 0x00020000)
#define LD128(r, off, aux) __builtin_amdgcn_raw_buffer_load_b128((r), (off), 0, (aux))
#define ST128(v, r, off, aux) __builtin_amdgcn_raw_buffer_store_b128((v), (r), (off), 0, (aux))
#define SCHED_FENCE() __builtin_amdgcn_sched_barrier(0)
#define MEM_FENCE() asm volatile("" ::: "memory")
template <int N> __device__ __forceinline__ void vmwait() { asm volatile("s_waitcnt vmcnt(%0)" ::"n"(N) : "memory"); }
#define LGKM0() asm volatile("s_waitcnt lgkmcnt(0)" ::: "memory")

__device__ __forceinline__ f32x4 mfma(u32x4 a, u32x4 b, f32x4 c) {
  return __builtin_amdgcn_mfma_f32_16x16x32_bf16(__builtin_bit_cast(bf16x8, a), __builtin_bit_cast(bf16x8, b), c, 0, 0, 0);
}
__device__ __forceinline__ u32x2 trd(LAS unsigned char* p) {
  return __builtin_bit_cast(u32x2, __builtin_amdgcn_ds_read_tr16_b64_v4i16((LAS s16x4*)p));
}
__device__ __forceinline__ unsigned pk_bf16(float lo, float hi) {
  f32x2 v = {lo, hi};
  return __builtin_bit_cast(unsigned, __builtin_convertvector(v, bf16x2));
}
__device__ __forceinline__ float bf_lo(unsigned u) { return __uint_as_float(u << 16); }
__device__ __forceinline__ float bf_hi(unsigned u) { return __uint_as_float(u & 0xffff0000u); }
__device__ __forceinline__ float rbf(float f) { return bf_lo(pk_bf16(f, f)); }
template <int CTRL> __device__ __forceinline__ float dpp(float x) {
  return __builtin_bit_cast(float, __builtin_amdgcn_mov_dpp(__builtin_bit_cast(int, x), CTRL, 0xf, 0xf, true));
}
constexpr int XOR1 = 0xB1, XOR2 = 0x4E, HMIRROR = 0x141, ROR8 = 0x128;
__device__ __forceinline__ float x16_a(float x, float& o) {
  auto s = __builtin_amdgcn_permlane16_swap(__float_as_uint(x), __float_as_uint(x), false, false);
  o = __uint_as_float(s[1]);
  return __uint_as_float(s[0]);
}
__device__ __forceinline__ float x32_a(float x, float& o) {
  auto s = __builtin_amdgcn_permlane32_swap(__float_as_uint(x), __float_as_uint(x), false, false);
  o = __uint_as_float(s[1]);
  return __uint_as_float(s[0]);
}
__device__ __forceinline__ float wave_max(float x) {
  x = fmaxf(x, dpp<XOR1>(x)); x = fmaxf(x, dpp<XOR2>(x)); x = fmaxf(x, dpp<HMIRROR>(x)); x = fmaxf(x, dpp<ROR8>(x));
  float o, a = x16_a(x, o); x = fmaxf(a, o);
  a = x32_a(x, o); return fmaxf(a, o);
}
__device__ __forceinline__ float wave_sum(float x) {
  x += dpp<XOR1>(x); x += dpp<XOR2>(x); x += dpp<HMIRROR>(x); x += dpp<ROR8>(x);
  float o, a = x16_a(x, o); x = a + o;
  a = x32_a(x, o); return a + o;
}
__device__ __forceinline__ void lds_wait_ge(volatile LAS unsigned* f, unsigned want) {
  unsigned n, t, v;
  asm volatile("s_mov_b32 %[n], 0x400000\n"
               "1:\n\tds_read_b32 %[v], %[a]\n\ts_waitcnt lgkmcnt(0)\n\tv_readfirstlane_b32 %[t], %[v]\n\ts_cmp_ge_u32 %[t], %[w]\n\ts_cbranch_scc1 2f\n\t"
               "s_sleep 1\n\ts_sub_u32 %[n], %[n], 1\n\ts_cmp_lg_u32 %[n], 0\n\ts_cbranch_scc1 1b\n"
               "2:"
               : [n] "=&s"(n), [t] "=&s"(t), [v] "=&v"(v) : [a] "v"((unsigned)(uintptr_t)f), [w] "s"(__builtin_amdgcn_readfirstlane(want)) : "memory", "scc");
}
__device__ __forceinline__ unsigned bperm(unsigned v, int srclane) { return __builtin_amdgcn_ds_bpermute(srclane << 2, v); }
__device__ __forceinline__ void lds_inc(LAS unsigned char* L, int f, int lane) {
  if (lane == 0) asm volatile("ds_add_u32 %0, %1" ::"v"((unsigned)(uintptr_t)(L + f * 4)), "v"(1u) : "memory");
}
__device__ __forceinline__ unsigned get_tag(volatile LAS unsigned* flag) {
  lds_wait_ge(flag + 0, 1u);
  return __builtin_amdgcn_readfirstlane(flag[1]);
}
__device__ __forceinline__ void dma16(__amdgpu_buffer_rsrc_t r, int voff, unsigned ldsdst) {
  unsigned keep;
  asm volatile("s_mov_b32 %0, m0\n\ts_mov_b32 m0, %3\n\ts_nop 0\n\tbuffer_load_dwordx4 %1, %2, 0 offen nt lds\n\ts_mov_b32 m0, %0"
               : "=&s"(keep) : "v"(voff), "s"(r), "s"(ldsdst) : "memory");
}
__device__ __forceinline__ void dma16p(__amdgpu_buffer_rsrc_t r, int voff, unsigned ldsdst) {
  unsigned keep;
  asm volatile("s_mov_b32 %0, m0\n\ts_mov_b32 m0, %3\n\ts_nop 0\n\tbuffer_load_dwordx4 %1, %2, 0 offen lds\n\ts_mov_b32 m0, %0"
               : "=&s"(keep) : "v"(voff), "s"(r), "s"(ldsdst) : "memory");
}
template <int N>
__device__ __forceinline__ void gatherm(u32x4 (&a)[N], int (&off)[N], unsigned tag, __amdgpu_buffer_rsrc_t r) {
#pragma nounroll
  for (unsigned n = 0; n < SPIN_CAP; ++n) {
    MEM_FENCE();
    u32x4 v[N];
#pragma unroll
    for (int i = 0; i < N; ++i) v[i] = LD128(r, off[i], AUX_SC1);
    bool all = true;
#pragma unroll
    for (int i = 0; i < N; ++i) {
      const bool mine = off[i] != OOB, ok = v[i][1] == tag && v[i][3] == tag;
      a[i][0] = mine ? v[i][0] : a[i][0];
      a[i][2] = mine ? v[i][2] : a[i][2];
      off[i] = ok ? OOB : off[i];
      all = all && (ok || !mine);
    }
    if (__all(all)) break;
  }
}
template <int STEP>
__device__ __forceinline__ void pass4(u32x4 (&a)[4], const u64 (&m)[4], int off, __amdgpu_buffer_rsrc_t r) {
  asm volatile(
      "s_mov_b64 exec, %[m0]\n\tbuffer_load_dwordx4 %[a0], %[o], %[r], 0 offen sc1\n\t"
      "s_mov_b64 exec, %[m1]\n\tbuffer_load_dwordx4 %[a1], %[o], %[r], 0 offen offset:%c[s1] sc1\n\t"
      "s_mov_b64 exec, %[m2]\n\tbuffer_load_dwordx4 %[a2], %[o], %[r], 0 offen offset:%c[s2] sc1\n\t"
      "s_mov_b64 exec, %[m3]\n\tbuffer_load_dwordx4 %[a3], %[o], %[r], 0 offen offset:%c[s3] sc1\n\t"
      "s_mov_b64 exec, -1\n\ts_waitcnt vmcnt(0)"
      : [a0] "+v"(a[0]), [a1] "+v"(a[1]), [a2] "+v"(a[2]), [a3] "+v"(a[3])
      : [m0] "s"(m[0]), [m1] "s"(m[1]), [m2] "s"(m[2]), [m3] "s"(m[3]), [o] "v"(off), [r] "s"(r), [s1] "i"(STEP), [s2] "i"(2 * STEP), [s3] "i"(3 * STEP)
      : "memory");
}
__device__ __forceinline__ bool missing4(const u32x4 (&a)[4], u64 (&m)[4], unsigned tag) {
#pragma unroll
  for (int i = 0; i < 4; ++i) m[i] &= __builtin_amdgcn_ballot_w64(a[i][1] != tag) | __builtin_amdgcn_ballot_w64(a[i][3] != tag);
  return (m[0] | m[1] | m[2] | m[3]) != 0ull;
}
struct Sel { unsigned lo[4], hi[4]; };
__device__ __forceinline__ Sel make_sel(int lane) {
  Sel s;
  const int j = lane & 7;
#pragma unroll
  for (int t = 0; t < 4; ++t) {
    const bool on = (j >> 1) == t;
    s.lo[t] = on ? ((j & 1) ? 0x01000c0cu : 0x0c0c0100u) : 0x0c0c0c0cu;
    s.hi[t] = on ? ((j & 1) ? 0x03020c0cu : 0x0c0c0302u) : 0x0c0c0c0cu;
  }
  return s;
}
__device__ __forceinline__ u32x4 place(unsigned src, const unsigned (&sel)[4]) {
  u32x4 r;
#pragma unroll
  for (int t = 0; t < 4; ++t) r[t] = __builtin_amdgcn_perm(src, src, sel[t]);
  return r;
}

#define KA0 "s36"
#define KA1 "s37"
#define KA6 "s42"
#define KA7 "s43"
#define KB23 "s[54:55]"
#define RT "s[56:59]"
#define RT01 "s[56:57]"
#define RT0 "s56"
#define RT1 "s57"
#define RT2 "s58"
#define RT3 "s59"
__global__ void __launch_bounds__(1024, 4) fused_kernel(const void*, const void*, const void*, const void*, const void*, const void*, const void*,
                                                     const void*, void*, void*) {
  __shared__ __attribute__((aligned(16))) unsigned char smem[L_END];
  LAS unsigned char* L = (LAS unsigned char*)smem;
  volatile LAS unsigned* flag = (volatile LAS unsigned*)(L + L_FLAG);
  const int bid = blockIdx.x, tid = threadIdx.x, lane = tid & 63;
  u32x16 ka;
  u32x4 kb, rt;
  unsigned epochv = 0, wave, t0, t1, t2, v0, v1;
  asm volatile(
      "8:\n\ts_load_dwordx16 %[ka], %[kp], 0x0\n\ts_load_dwordx4 %[kb], %[kp], 0x40\n\t"
      "v_readfirstlane_b32 %[w], %[tid]\n\ts_lshr_b32 %[w], %[w], 6\n\t"
      "s_cmp_lt_u32 %[w], 8\n\ts_cbranch_scc1 1f\n\t"
      "s_add_u32 %[t0], %[bid], 1\n\ts_and_b32 %[t0], %[t0], 3\n\t"
      "s_lshl_b32 %[t2], %[w], 11\n\ts_add_u32 m0, %[t2], %[lx]\n\t"
      "s_cmp_eq_u32 %[w], 8\n\ts_cbranch_scc1 5f\n\t"
      "v_lshrrev_b32 %[v0], 3, %[lane]\n\tv_and_b32 %[v1], 7, %[lane]\n\tv_lshlrev_b32 %[v0], 13, %[v0]\n\tv_lshl_or_b32 %[v0], %[v1], 4, %[v0]\n\t"
      "s_mul_i32 %[t0], %[t0], 0xe0000\n\ts_lshl_b32 %[t1], %[w], 17\n\ts_add_u32 %[t0], %[t0], %[t1]\n\t"
      "s_lshl_b32 %[t1], %[bid], 4\n\ts_and_b32 %[t1], %[t1], 0xf80\n\ts_add_u32 %[t0], %[t0], %[t1]\n\t"
      "s_lshl_b32 %[t1], %[bid], 10\n\ts_and_b32 %[t1], %[t1], 0x1000\n\ts_add_u32 %[t0], %[t0], %[t1]\n\t"
      "s_sub_u32 %[t0], %[t0], 0x120000\n\ts_add_u32 %[t1], %[t0], 0x10000\n\t"
      "s_mov_b32 " RT2 ", 0x380000\n\ts_mov_b32 " RT3 ", 0x20000\n\t"
      "s_waitcnt lgkmcnt(0)\n\ts_mov_b32 " RT0 ", " KA6 "\n\ts_and_b32 " RT1 ", " KA7 ", 0xffff\n\t"
      "s_branch 6f\n"
      "5:\n\t"
      "v_lshrrev_b32 %[v0], 4, %[lane]\n\tv_and_b32 %[v1], 15, %[lane]\n\tv_min_u32 %[v1], 13, %[v1]\n\tv_mul_u32_u24 %[v0], 0x380, %[v0]\n\tv_lshl_add_u32 %[v0], %[v1], 4, %[v0]\n\t"
      "s_mul_i32 %[t0], %[t0], 0xe0\n\tv_add_u32 %[v0], %[t0], %[v0]\n\ts_mov_b32 %[t0], 0\n\ts_movk_i32 %[t1], 0xe00\n\t"
      "s_movk_i32 " RT2 ", 0x1c00\n\ts_mov_b32 " RT3 ", 0x20000\n\t"
      "s_waitcnt lgkmcnt(0)\n\ts_mov_b32 " RT0 ", " KA0 "\n\ts_and_b32 " RT1 ", " KA1 ", 0xffff\n"
      "6:\n\t"
      "buffer_load_dwordx4 %[v0], " RT ", %[t0] offen nt lds\n\t"
      "s_add_u32 m0, m0, 0x400\n\ts_nop 0\n\tbuffer_load_dwordx4 %[v0], " RT ", %[t1] offen nt lds\n\t"
      "s_barrier\n\ts_branch 4f\n"
      "1:\n\ts_cmp_eq_u32 %[w], 0\n\ts_cbranch_scc1 2f\n\t"
      "s_cmp_lt_u32 %[w], 5\n\ts_cbranch_scc1 4f\n\t"
      "s_cmp_eq_u32 %[w], 7\n\ts_cbranch_scc1 3f\n\t"
      "s_cmp_lt_u32 %[bid], 192\n\ts_cbranch_scc1 4f\n\t"
      "s_waitcnt lgkmcnt(0)\n\ts_barrier\n\ts_branch 4f\n"
      "3:\n\t"
      "v_lshlrev_b32 %[v0], 2, %[lane]\n\tds_write_b32 %[v0], %[e]\n\t"
      "s_getpc_b64 " RT01 "\n7:\n\ts_sub_u32 " RT0 ", " RT0 ", 7b-8b\n\ts_subb_u32 " RT1 ", " RT1 ", 0\n\ts_and_b32 " RT0 ", " RT0 ", 0xffffff00\n\ts_and_b32 " RT1 ", " RT1 ", 0xffff\n\t"
      "s_mov_b32 " RT2 ", %[cl]\n\ts_mov_b32 " RT3 ", 0x20000\n\ts_mov_b32 m0, %[lc]\n\t"
      "s_lshr_b32 %[t0], %[bid], 3\n\ts_lshl_b32 %[t0], %[t0], 10\n\tv_lshlrev_b32 %[v0], 4, %[lane]\n\tv_add_u32 %[v0], %[t0], %[v0]\n\t"
      "buffer_load_dwordx4 %[v0], " RT ", 0 offen lds\n\t"
      "s_waitcnt lgkmcnt(0)\n\ts_barrier\n\ts_branch 4f\n"
      "2:\n\ts_waitcnt lgkmcnt(0)\n\tglobal_load_dword %[e], %[e], " KB23 " sc1\n\ts_barrier\n\ts_waitcnt vmcnt(0)\n"
      "4:"
      : [ka] "={s[36:51]}"(ka), [kb] "={s[52:55]}"(kb), [rt] "={s[56:59]}"(rt), [w] "=&s"(wave), [t0] "=&s"(t0), [t1] "=&s"(t1), [t2] "=&s"(t2),
        [v0] "=&v"(v0), [v1] "=&v"(v1), [e] "+v"(epochv)
      : [kp] "{s[0:1]}"(__builtin_amdgcn_kernarg_segment_ptr()), [bid] "{s2}"(bid), [tid] "v"(tid), [lane] "v"(lane), [lx] "i"(L_X - 8 * 2048),
        [lc] "i"(L_WO), [cl] "i"(CODE_BYTES)
      : "memory", "scc");
#define WINDOW(p) (((p) + 0x3fffffull) & ~0x3fffffull)
#define ARG(v, i) ((u64)v[2 * (i)] | ((u64)v[2 * (i) + 1] << 32))
  const int xc = bid & 7, jj = bid >> 3, kvh = jj & 7, s = jj >> 3, b = (xc - 2 * s) & 7;
  const int nkw = s == 3 ? 4 : 6;
  unsigned tag = 0;

  if (__builtin_expect(wave - 1u < (unsigned)nkw, 1)) {
    const bool four = s == 3 && wave >= 3;
    const int urel = four && wave == 4 ? 16 : (wave - 1) * 6;
    const unsigned hsh = (((unsigned)bid * 8u + wave) * 2654435761u) >> 24;
    unsigned ord = (unsigned)((four ? 0x3210321032ull : 0x543210543210ull) >> (((hsh * (four ? 4u : 6u)) >> 8) * 4u));
    int klo = (lane & 7) * 512 + ((lane >> 3) & 1) * 64 + (lane >> 4) * 16 + (s * 36 + urel) * 4096;
    unsigned kvoff = (unsigned)(((b * HKV + kvh) * SK + (SK - WIN)) * DH * 2);
    unsigned brot = (wave + jj + xc) * 128u;
    u32x16 kak = ka;
    u32x4 kbk = kb;
    asm volatile("s_waitcnt lgkmcnt(0)\n\ts_barrier" : "+{s[36:51]}"(kak), "+{s[52:55]}"(kbk), "+v"(klo), "+s"(kvoff), "+s"(ord), "+s"(brot)::"memory");
    const auto rk = RSRC(ARG(kak, 1) + kvoff, WIN * DH * 2);
    u32x4 r[24];
#pragma unroll
    for (int i = 0; i < 24; ++i) {
      if (i >= WK0) vmwait<WK - 1>();
      r[i] = __builtin_amdgcn_raw_buffer_load_b128(rk, i < 16 || !four ? klo : OOB, ((ord >> (i & ~3)) & 15u) * 4096 + ((brot + i * 128) & 0x180), AUX_NT);
      SCHED_FENCE();
    }
    lds_inc(L, F_KISS, lane);
    const u64 ws = WINDOW(ARG(kbk, 1));
    const auto rws = RSRC(ws, WS_END);
    const int kw = wave - 1, nu = four ? 4 : 6, ulo = s * 36, uhi = s == 3 ? 128 : ulo + 36, u0 = ulo + urel;
    __builtin_amdgcn_s_setprio(2);
    lds_wait_ge(flag + F_QRDY, 1u);
    tag = __builtin_amdgcn_readfirstlane(flag[F_TAG]);
    u32x4 qa[4];
#pragma unroll
    for (int t = 0; t < 4; ++t) qa[t] = *(const LAS u32x4*)(L + L_Q + (lane & 1) * 512 + ((brot + t * 128) & 0x180) + ((lane >> 1) & 1) * 64 + (lane >> 4) * 16);
    float s0 = 0.f, s1 = 0.f;
#pragma unroll
    for (int u = 0; u < 6; ++u) {
      f32x4 c = {0, 0, 0, 0};
#pragma unroll
      for (int t = 0; t < 4; ++t) c = mfma(qa[t], r[4 * u + t], c);
      float c0 = (lane & 8) ? c[2] : c[0], c1 = (lane & 8) ? c[3] : c[1];
      c0 += dpp<ROR8>(c0); c1 += dpp<ROR8>(c1);
      if ((unsigned)(lane >> 3) == ((ord >> (4 * u)) & 15u) && (u < 4 || !four)) { s0 = c0; s1 = c1; }
    }
    const unsigned pk = pk_bf16(s0, s1);
    const unsigned pa = bperm(pk, (2 * lane) & 63), pb = bperm(pk, (2 * lane + 1) & 63);
    if (lane < 4 * nu) {
      ST128((u32x4{pa, tag, pb, tag}), rws, WS_S + (b * 8 + kvh) * 8192 + u0 * 64 + lane * 16, AUX_SC1);
      *(LAS u32x4*)(L + L_SM + urel * 64 + lane * 16) = u32x4{pa, tag, pb, tag};
    }
    MEM_FENCE();
    lds_inc(L, F_KCNT, lane);
    if (wave <= 4) {
      u32x4 sg[2];
      int of[2];
      bool own[2];
#pragma unroll
      for (int i = 0; i < 2; ++i) {
        const int myu = (kw + 4 * i) * 16 + (lane >> 2);
        own[i] = myu >= ulo && myu < uhi;
        of[i] = own[i] ? OOB : WS_S + (b * 8 + kvh) * 8192 + (kw + 4 * i) * 1024 + lane * 16;
      }
      __builtin_amdgcn_s_sleep(SSLEEP);
      gatherm<2>(sg, of, tag, rws);
      lds_wait_ge(flag + F_KCNT, (unsigned)nkw);
#pragma unroll
      for (int i = 0; i < 2; ++i) {
        const int c = kw + 4 * i;
        const u32x4 mv = *(const LAS u32x4*)(L + L_SM + (own[i] ? (c * 16 - ulo) * 64 + lane * 16 : 0));
        const unsigned sa = own[i] ? mv[0] : sg[i][0], sb = own[i] ? mv[2] : sg[i][2];
        const float m0 = wave_max(fmaxf(bf_lo(sa), bf_lo(sb))), m1 = wave_max(fmaxf(bf_hi(sa), bf_hi(sb)));
        const float n0 = m0 == -INFINITY ? 0.f : m0, n1 = m1 == -INFINITY ? 0.f : m1;
        const float ea = __builtin_amdgcn_exp2f((bf_lo(sa) - n0) * C2), eb = __builtin_amdgcn_exp2f((bf_lo(sb) - n0) * C2);
        const float ec = __builtin_amdgcn_exp2f((bf_hi(sa) - n1) * C2), ed = __builtin_amdgcn_exp2f((bf_hi(sb) - n1) * C2);
        *(LAS unsigned*)(L + L_P + c * 512 + lane * 4) = pk_bf16(ea, eb);
        *(LAS unsigned*)(L + L_P + c * 512 + 256 + lane * 4) = pk_bf16(ec, ed);
        const float l0 = wave_sum(ea + eb), l1 = wave_sum(ec + ed);
        if (lane == 0) *(LAS f32x4*)(L + L_ST + c * 16) = f32x4{m0, l0, m1, l1};
      }
      MEM_FENCE();
      lds_inc(L, F_PR + kw, lane);
      lds_inc(L, F_SCNT, lane);
    }
    return;
  }
  const GAS u16* x = (const GAS u16*)ARG(ka, 0);
  const u64 vc = ARG(ka, 2), wo = ARG(ka, 4), w1 = ARG(ka, 5), w2 = ARG(ka, 6), ws = WINDOW(ARG(kb, 1));
  const GAS u16* gw = (const GAS u16*)ARG(ka, 7);
  GAS u16* out = (GAS u16*)ARG(kb, 0);
  const auto rws = RSRC(ws, WS_END);
  const int cb = jj + 32 * (xc >> 2), rb = (xc + 1) & 3;
  const int grp = kvh * 4 + s;
  if (wave >= 8) {
    const unsigned kvoff = (unsigned)(((b * HKV + kvh) * SK + (SK - WIN)) * DH * 2);
    const int w = wave - 8;
    vmwait<0>();
    lds_inc(L, F_WQC, lane);
    tag = get_tag(flag);
    if (w >= 4) {
      __builtin_amdgcn_s_setprio(3);
      const int ct = w - 4;
      const int xrow = ((lane & 7) >> 1) + 4 * (lane & 1);
      LAS unsigned char* xp = L + L_X + xrow * 256 + (lane >> 4) * 16;
      LAS unsigned char* xp2 = L + L_X + xrow * 256 + 192 + (lane >> 4) * 8;
      LAS unsigned char* tb = L + L_WQ + ((lane >> 4) * 8 + ((lane >> 2) & 3)) * 128 + (lane & 3) * 8 + ct * 32;
      LAS unsigned char* tb2 = L + L_WQ + (96 + (lane >> 4) * 4 + ((lane >> 2) & 3)) * 128 + (lane & 3) * 8 + ct * 32;
      const int dest = ((lane >> 4) & 1) * 2 + (lane >> 5);
      const int qo = WS_QP + (cb * 4 + dest) * QPSTR + rb * 1024 + ct * 256 + (lane & 15) * 16;
      lds_wait_ge(flag + F_WQC, 8u);
      const u32x2 pt = trd(tb2), xb = *(const LAS u32x2*)xp2;
      f32x4 acc = mfma(u32x4{xb[0], xb[1], 0u, 0u}, u32x4{pt[0], pt[1], 0u, 0u}, f32x4{0, 0, 0, 0});
#pragma unroll
      for (int ks = 0; ks < 3; ++ks) {
        const u32x2 p0 = trd(tb + ks * 4096), p1 = trd(tb + ks * 4096 + 512);
        acc = mfma(*(const LAS u32x4*)(xp + ks * 64), u32x4{p0[0], p0[1], p1[0], p1[1]}, acc);
      }
      const float q0 = lane >= 32 ? acc[2] : acc[0], q1 = lane >= 32 ? acc[3] : acc[1];
      if (dest != rb) ST128((u32x4{__float_as_uint(q0), tag, __float_as_uint(q1), tag}), rws, qo, AUX_SC1);
      else *(LAS f32x2*)(L + L_QO + (ct * 16 + (lane & 15)) * 8) = f32x2{q0, q1};
      LGKM0();
      lds_inc(L, F_QPC, lane);
      __builtin_amdgcn_s_setprio(0);
    }
    const auto rv = RSRC(vc + kvoff, WIN * DH * 2);
    u32x4 r[16];
    const int trot = w + jj + xc;
    const int voff = (w * 128 + (lane >> 4) * 8 + ((lane >> 3) & 1)) * 512 + s * 128 + (lane & 7) * 16;
    const Sel sel = make_sel(lane);
    lds_wait_ge(flag + F_KISS, (unsigned)nkw);
#define V_ISSUE(i) do { if ((i) >= WV) vmwait<WV - 1>(); \
      r[i] = __builtin_amdgcn_raw_buffer_load_b128(rv, voff + ((i) & 3) * 1024, ((((i) >> 2) + trot) & 3) * 16384, AUX_NT); SCHED_FENCE(); \
      if ((i) == 15) lds_inc(L, F_VISS, lane); } while (0)
#define V_USE(i) do { a0 = mfma(place(pp[(i) >> 2][(i) & 3], sel.lo), r[i], a0); a1 = mfma(place(pp[(i) >> 2][(i) & 3], sel.hi), r[i], a1); } while (0)
#pragma unroll
    for (int i = 0; i <= VSPLIT; ++i) V_ISSUE(i);
    u32x4 pp[4];
    f32x4 a0 = {0, 0, 0, 0}, a1 = {0, 0, 0, 0};
#define P_READ() _Pragma("unroll") for (int t = 0; t < 4; ++t) \
      pp[t] = *(const LAS u32x4*)(L + L_P + w * 512 + ((lane >> 3) & 1) * 256 + (((t + trot) & 3) * 32 + (lane >> 4) * 8) * 2)
    float f;
    unsigned wad = (unsigned)(uintptr_t)(L + L_OP) + w * 512 + ((lane >> 5) * 64 + (lane & 7) * 8 + ((lane >> 4) & 1) * 4) * 4;
    unsigned rad = (unsigned)(uintptr_t)(L + L_OP) + (lane >> 5) * 2048 + (lane & 31) * 16;
    int oad = WS_OB + inbox(grp) + b * 512 + lane * 16;
#define F_GET() do { lds_wait_ge(flag + F_FRDY, 1u); f = *(const LAS float*)(L + L_FW + w * 8 + (lane >> 5) * 4); \
      asm volatile("" : "+v"(f), "+v"(wad), "+v"(rad), "+v"(oad)); } while (0)
#define V_LAST(id) do { SCHED_FENCE(); F_GET(); u32x4 la[WV], lb[WV]; \
      _Pragma("unroll") for (int k = 0; k < WV; ++k) { \
        la[k] = place(pp[(16 - WV + k) >> 2][(16 - WV + k) & 3], sel.lo); lb[k] = place(pp[(16 - WV + k) >> 2][(16 - WV + k) & 3], sel.hi); \
        asm volatile("; last " #id : "+v"(la[k]), "+v"(lb[k])); } \
      SCHED_FENCE(); \
      _Pragma("unroll") for (int k = 0; k < WV; ++k) { a0 = mfma(la[k], r[16 - WV + k], a0); a1 = mfma(lb[k], r[16 - WV + k], a1); } \
      asm volatile("; done " #id : "+v"(a0), "+v"(a1)); } while (0)
    if (__builtin_expect(__builtin_amdgcn_readfirstlane(flag[F_PR + (w & 3)]) != 0u, 1)) {
      MEM_FENCE();
      P_READ();
#pragma unroll
      for (int i = VSPLIT + 1; i < 16; ++i) {
        V_ISSUE(i);
#pragma unroll
        for (int k = 0; k < 16; ++k)
          if (k >= (i - VSPLIT - 1) * VPER && k < (i - VSPLIT) * VPER && k <= i - WV) V_USE(k);
        SCHED_FENCE();
      }
#pragma unroll
      for (int k = 0; k < 16 - WV; ++k) {
        bool done = false;
#pragma unroll
        for (int i = VSPLIT + 1; i < 16; ++i) done = done || (k >= (i - VSPLIT - 1) * VPER && k < (i - VSPLIT) * VPER && k <= i - WV);
        if (!done) { V_USE(k); if (k & 1) SCHED_FENCE(); }
      }
      V_LAST(0);
    } else {
#pragma unroll
      for (int i = VSPLIT + 1; i < 16; ++i) V_ISSUE(i);
      lds_wait_ge(flag + F_PR + (w & 3), 1u);
      P_READ();
#pragma unroll
      for (int k = 0; k < 16 - WV; ++k) { V_USE(k); if (k & 1) SCHED_FENCE(); }
      V_LAST(1);
    }
    f32x4 t = (lane & 8) ? a1 : a0;
#pragma unroll
    for (int k = 0; k < 4; ++k) t[k] = (t[k] + dpp<ROR8>(t[k])) * f;
    unsigned old;
    f32x4 o0v, o1v, o2v, o3v;
    asm volatile("ds_write_b128 %[wa], %[t]\n\tds_add_rtn_u32 %[old], %[ca], %[one]\n\t"
                 "ds_read_b128 %[p0], %[ra]\n\tds_read_b128 %[p1], %[ra] offset:512\n\tds_read_b128 %[p2], %[ra] offset:1024\n\tds_read_b128 %[p3], %[ra] offset:1536\n\t"
                 "s_waitcnt lgkmcnt(0)"
                 : [old] "=&v"(old), [p0] "=&v"(o0v), [p1] "=&v"(o1v), [p2] "=&v"(o2v), [p3] "=&v"(o3v)
                 : [wa] "v"(wad), [t] "v"(t), [ca] "v"((unsigned)(uintptr_t)(L + (lane == 0 ? F_OCNT * 4 : 128 + (lane & 31) * 4))), [one] "v"(1u), [ra] "v"(rad)
                 : "memory");
    if (__builtin_amdgcn_readfirstlane(old) == 7u) {
      f32x4 ov = (o0v + o1v) + (o2v + o3v);
#pragma unroll
      for (int k = 0; k < 4; ++k) { float hi, lo = x32_a(ov[k], hi); ov[k] = lo + hi; }
      if (lane < 32) {
        const u32x4 ogr = {pk_bf16(ov[0], ov[1]), tag, pk_bf16(ov[2], ov[3]), tag};
        ST128(ogr, rws, oad, AUX_SC1);
        ST128(ogr, rws, oad + 8192, AUX_SC1);
        *(LAS u32x4*)(L + L_OM + lane * 16) = ogr;
      }
      MEM_FENCE();
      lds_inc(L, F_OMR, lane);
    }
  } else if (wave == 0) {
    __builtin_amdgcn_s_setprio(3);
    tag = __builtin_amdgcn_readfirstlane(epochv) * 1664525u + 1013904223u;
    if (lane == 0) *(volatile LAS u32x2*)flag = u32x2{1u, tag};
    lds_wait_ge(flag + F_QPC, 4u);
    __builtin_amdgcn_s_sleep(QSLEEP);
    {
      const int po = WS_QP + (cb * 4 + rb) * QPSTR + lane * 16;
      u32x4 pg[3];
      int of[3] = {po + ((rb + 1) & 3) * 1024, po + ((rb + 2) & 3) * 1024, po + ((rb + 3) & 3) * 1024};
      gatherm<3>(pg, of, tag, rws);
      const f32x2 own = *(const LAS f32x2*)(L + L_QO + lane * 8);
      const float a0 = (own[0] + __uint_as_float(pg[0][0])) + (__uint_as_float(pg[1][0]) + __uint_as_float(pg[2][0]));
      const float a1 = (own[1] + __uint_as_float(pg[0][2])) + (__uint_as_float(pg[1][2]) + __uint_as_float(pg[2][2]));
      const float n0 = dpp<XOR1>(a0), n1 = dpp<XOR1>(a1);
      const unsigned pk = (lane & 1) ? pk_bf16(n1, a1) : pk_bf16(a0, n0);
      const int src = (lane & 15) * 4 + ((lane >> 4) & 1);
      const unsigned pa = bperm(pk, src), pb = bperm(pk, src + 2);
      if (lane < 32) ST128((u32x4{pa, tag, pb, tag}), rws, WS_Q + ((cb >> 3) * 8 + rb + 4 * (lane >> 4)) * QSTR + (cb & 7) * 256 + (lane & 15) * 16, AUX_SC1);
    }
    __builtin_amdgcn_s_sleep(QSLEEP);
    {
      const int qoff = WS_Q + (kvh * 8 + b) * QSTR + lane * 16;
      u32x4 qq[2];
      int of[2] = {qoff, qoff + 1024};
      gatherm<2>(qq, of, tag, rws);
      *(LAS u32x2*)(L + L_Q + lane * 8) = u32x2{qq[0][0], qq[0][2]};
      *(LAS u32x2*)(L + L_Q + 512 + lane * 8) = u32x2{qq[1][0], qq[1][2]};
      LGKM0();
      if (lane == 0) flag[F_QRDY] = 1u;
    }
    u32x4 xg = {0, 0, 0, 0}, gg = {0, 0, 0, 0};
    if (lane < 56) {
      xg = *(const GAS u32x4*)(x + b * DM + lane * 8);
      gg = *(const GAS u32x4*)(gw + lane * 8);
    }
    float z = 0.f;
#pragma unroll
    for (int t = 0; t < 4; ++t) z += rbf(bf_lo(xg[t]) * bf_lo(gg[t])) + rbf(bf_hi(xg[t]) * bf_hi(gg[t]));
    z = rbf(wave_sum(z));
    const float g = rbf(1.f / (1.f + __expf(-z)));
    const unsigned g1u = __float_as_uint(rbf(1.f - g));
    if (lane == 0) *(volatile LAS u32x2*)(flag + F_G1V) = u32x2{1u, g1u};
    lds_wait_ge(flag + F_SCNT, 4u);
    {
      const f32x2 ml = *(const LAS f32x2*)(L + L_ST + (lane & 15) * 8);
      float M = fmaxf(ml[0], dpp<XOR2>(ml[0]));
      M = fmaxf(M, dpp<0x124>(M)); M = fmaxf(M, dpp<ROR8>(M));
      const float al = __builtin_amdgcn_exp2f((ml[0] - M) * C2);
      float Ls = al * ml[1];
      Ls += dpp<XOR2>(Ls); Ls += dpp<0x124>(Ls); Ls += dpp<ROR8>(Ls);
      if (lane < 16) *(LAS float*)(L + L_FW + lane * 4) = al * (g / Ls);
      LGKM0();
      if (lane == 0) flag[F_FRDY] = 1u;
    }
  } else if (wave != 7 && xc < 7) {
    const int j = wave - 5, t = xc * 8 + kvh;
    const unsigned lb = (unsigned)(uintptr_t)(L + L_T) + j * 14336;
    const auto r1 = RSRC(w1, DM * FF * 2);
    const int o = ((j * 224 + (lane >> 2)) * FF + t * 32 + (lane & 3) * 8) * 2;
#pragma unroll
    for (int i = 0; i < 14; ++i) {
      if (i >= WT) vmwait<WT - 1>();
      dma16(r1, o + i * 16 * FF * 2, lb + i * 1024);
    }
    {
      const auto rx = RSRC((u64)x, NB * DM * 2);
#pragma unroll
      for (int i = 0; i < 4; ++i) {
        vmwait<WT - 1>();
        dma16p(rx, (j * 4 + i) * 1024 + lane * 16, (unsigned)(uintptr_t)(L + L_XA) + (j * 4 + i) * 1024);
      }
    }
    tag = get_tag(flag);
    vmwait<0>();
    lds_inc(L, F_TCNT, lane);
    lds_wait_ge(flag + F_TCNT, 2u);
    LAS unsigned char* tb = L + L_T + ((lane >> 4) * 8 + ((lane >> 2) & 3)) * 64 + j * 32 + (lane & 3) * 8;
    f32x4 acc0 = {0, 0, 0, 0}, acc1 = {0, 0, 0, 0};
#pragma unroll
    for (int ks = 0; ks < 14; ++ks) {
      const u32x2 p0 = trd(tb + ks * 2048), p1 = trd(tb + ks * 2048 + 256);
      const u32x4 xa = *(const LAS u32x4*)(L + L_XA + (lane & 7) * (DM * 2) + ks * 64 + (lane >> 4) * 16);
      if (ks & 1) acc1 = mfma(u32x4{p0[0], p0[1], p1[0], p1[1]}, xa, acc1);
      else acc0 = mfma(u32x4{p0[0], p0[1], p1[0], p1[1]}, xa, acc0);
    }
    acc0 += acc1;
    float hv[4];
#pragma unroll
    for (int k = 0; k < 4; ++k) {
      const float v = rbf(acc0[k]);
      hv[k] = v / (1.f + __expf(-v));
    }
    if (!(lane & 8))
      ST128((u32x4{pk_bf16(hv[0], hv[1]), tag, pk_bf16(hv[2], hv[3]), tag}), rws, WS_HB + t * 1024 + j * 512 + (lane >> 4) * 128 + (lane & 7) * 16, AUX_SC1);
  }
  const int cpair = (xc ^ 1) * 32 + jj;
  if (wave == 7) {
    const int fgrp = b == 7 ? -1 : s < 3 ? kvh * 3 + s : (kvh < 4 ? 24 + kvh : -1);
    tag = get_tag(flag);
    const bool hi8 = lane & 8;
    const unsigned upper = (__builtin_amdgcn_s_getreg(20 | (3 << 11)) >> 2) & 1u;
    if (s == 0) {
      const u64 pa = ws + WS_PR + kvh * 16384 + b * 1024;
      int vote = 0;
#pragma unroll
      for (int i = 0; i < NPAIR; ++i) {
        u64 t0, t1, t2;
        unsigned d;
        asm volatile("s_memtime %0\n\ts_waitcnt lgkmcnt(0)\n\ts_load_dword %3, %4, %5\n\ts_waitcnt lgkmcnt(0)\n\ts_memtime %1\n\ts_waitcnt lgkmcnt(0)\n\t"
                     "s_load_dword %3, %4, %6\n\ts_waitcnt lgkmcnt(0)\n\ts_memtime %2\n\ts_waitcnt lgkmcnt(0)"
                     : "=&s"(t0), "=&s"(t1), "=&s"(t2), "=&s"(d) : "s"(pa), "i"(i * 128), "i"(i * 128 + 8192) : "memory");
        vote += (unsigned)(t1 - t0) > (unsigned)(t2 - t1) ? 1 : -1;
      }
      lds_wait_ge(flag + F_G1V, 1u);
      if (lane < 8) ST128((u32x4{flag[F_G1], tag, (unsigned)(upper ? -vote : vote), tag}), rws, WS_G + (kvh * 8 + b) * 128 + lane * 16, AUX_SC1);
    }
    u32x4 hb[3];
    int nearc;
    {
      const unsigned lb = (unsigned)(uintptr_t)(L + L_WO);
      const auto r2 = RSRC(w2, FF * DM * 2);
      const auto ro = RSRC(wo, NQ * DM * 2);
      const int off2 = (fgrp * 64 + (lane >> 3)) * 896 + b * 128 + (lane & 7) * 16;
      const int offo = (kvh * 512 + s * 64 + (lane >> 3)) * 896 + b * 128 + (lane & 7) * 16;
      lds_wait_ge(flag + F_SCNT, 2u);
      {
        int of[3] = {fgrp >= 0 ? WS_HB + fgrp * 2048 + lane * 16 : OOB, fgrp >= 0 ? WS_HB + fgrp * 2048 + 1024 + lane * 16 : OOB, WS_G + (kvh * 8 + (lane & 7)) * 128};
        gatherm<3>(hb, of, tag, rws);
        float vs = (float)(int)hb[2][2];
        vs += dpp<XOR1>(vs); vs += dpp<XOR2>(vs); vs += dpp<HMIRROR>(vs);
        nearc = (((__builtin_amdgcn_readfirstlane(__float_as_uint(vs)) >> 31) ^ upper) & 1u) * 8192;
        if (lane == 0) *(volatile LAS u32x2*)(flag + F_NRV) = u32x2{1u, (unsigned)nearc};
      }
      if (b == 7) return;
      lds_wait_ge(flag + F_VISS, 8u);
      if (fgrp >= 0) {
#pragma unroll
        for (int i = 0; i < 8; ++i) dma16(r2, off2 + i * 8 * 896, lb + 16384 + i * 1024);
      }
#pragma unroll
      for (int i = 0; i < 16; ++i) dma16(ro, offo + ((i >> 3) * 256 + (i & 7) * 8) * 896, lb + i * 1024);
    }
    __builtin_amdgcn_s_setprio(3);
    const bool ownb = (lane & 7) == b;
    const int ooff = WS_OB + nearc + inbox(grp) + (lane & 7) * 512 + (lane >> 3) * 16;
    u32x4 ob[4];
#pragma unroll
    for (int i = 0; i < 4; ++i) ob[i] = LD128(rws, ownb ? OOB : ooff + i * 128, AUX_SC1);
    SCHED_FENCE();
    LAS unsigned char* tb = L + L_WO + ((lane >> 4) * 8 + ((lane >> 2) & 3)) * 128 + (lane & 3) * 8;
    f32x4 acc[4] = {{0, 0, 0, 0}, {0, 0, 0, 0}, {0, 0, 0, 0}, {0, 0, 0, 0}};
    if (fgrp >= 0) {
      vmwait<20>();
      const u32x4 hv = {hb[0][0], hb[0][2], hb[1][0], hb[1][2]};
      const float g1 = __uint_as_float(hb[2][0]);
#pragma unroll
      for (int ks = 0; ks < 2; ++ks) {
        const u32x4 bf = {hi8 ? 0u : hv[2 * ks], hi8 ? 0u : hv[2 * ks + 1], hi8 ? hv[2 * ks] : 0u, hi8 ? hv[2 * ks + 1] : 0u};
#pragma unroll
        for (int ct = 0; ct < 4; ++ct) {
          const u32x2 p0 = trd(tb + 16384 + ks * 4096 + ct * 32), p1 = trd(tb + 16384 + ks * 4096 + 512 + ct * 32);
          acc[ct] = mfma(u32x4{p0[0], p0[1], p1[0], p1[1]}, bf, acc[ct]);
        }
      }
#pragma unroll
      for (int ct = 0; ct < 4; ++ct) acc[ct] *= g1;
    }
    u32x4 wf[4][4];
#pragma unroll
    for (int ks = 0; ks < 4; ++ks) {
      if (ks == 0) vmwait<16>(); else if (ks == 1) vmwait<12>(); else if (ks == 2) vmwait<8>(); else vmwait<4>();
#pragma unroll
      for (int ct = 0; ct < 4; ++ct) {
        const u32x2 p0 = trd(tb + ks * 4096 + ct * 32), p1 = trd(tb + ks * 4096 + 512 + ct * 32);
        wf[ks][ct] = u32x4{p0[0], p0[1], p1[0], p1[1]};
      }
    }
    int po = WS_PO + (nearc ^ (((xc ^ b) >> 2) & 1) * 8192) + inbox(b * 32 + (lane >> 3)) + grp * 128 + (lane & 7) * 16;
    asm volatile("" : "+v"(po));
    lds_wait_ge(flag + F_OMR, 1u);
#pragma unroll
    for (int i = 0; i < 4; ++i) {
      const u32x4 v = *(const LAS u32x4*)(L + L_OM + i * 128 + (lane >> 3) * 16);
      ob[i] = ownb ? v : ob[i];
    }
    u64 ms[4] = {~0ull, ~0ull, ~0ull, ~0ull};
    if (missing4(ob, ms, tag)) {
#pragma nounroll
      for (unsigned n = 0; n < SPIN_CAP; ++n) {
        pass4<128>(ob, ms, ooff, rws);
        if (!missing4(ob, ms, tag)) break;
      }
    }
    u32x4 bf[4];
#pragma unroll
    for (int ks = 0; ks < 4; ++ks) bf[ks] = u32x4{hi8 ? 0u : ob[ks][0], hi8 ? 0u : ob[ks][2], hi8 ? ob[ks][0] : 0u, hi8 ? ob[ks][2] : 0u};
#pragma unroll
    for (int ct = 0; ct < 4; ++ct)
#pragma unroll
      for (int ks = 0; ks < 4; ++ks) acc[ct] = mfma(wf[ks][ct], bf[ks], acc[ct]);
    u32x4 pv[4];
#pragma unroll
    for (int ct = 0; ct < 4; ++ct) {
      const float k0 = hi8 ? acc[ct][2] : acc[ct][0], k1 = hi8 ? acc[ct][3] : acc[ct][1];
      const float s0 = hi8 ? acc[ct][0] : acc[ct][2], s1 = hi8 ? acc[ct][1] : acc[ct][3];
      pv[ct] = u32x4{__float_as_uint(k0 + dpp<ROR8>(s0)), tag, __float_as_uint(k1 + dpp<ROR8>(s1)), tag};
      __builtin_amdgcn_raw_buffer_store_b128(pv[ct], rws, po, ct * 4 * 16384, AUX_SC1);
    }
    po ^= 8192;
#pragma unroll
    for (int ct = 0; ct < 4; ++ct) __builtin_amdgcn_raw_buffer_store_b128(pv[ct], rws, po, ct * 4 * 16384, AUX_SC1);
  } else if (wave == 0 && cpair < 224) {
    __builtin_amdgcn_s_setprio(3);
    lds_wait_ge(flag + F_OCNT, 8u);
    __builtin_amdgcn_s_sleep(FSLEEP);
    lds_wait_ge(flag + F_NRV, 1u);
    const int poff = WS_PO + __builtin_amdgcn_readfirstlane(flag[F_NR]) + inbox(cpair) + lane * 16;
    u32x4 pg[4] = {{0, 0, 0, 0}, {0, 0, 0, 0}, {0, 0, 0, 0}, {0, 0, 0, 0}};
    u64 ms[4] = {~0ull, ~0ull, ~0ull, ~0ull};
    GAS unsigned* op = (GAS unsigned*)(out + (lane & 7) * DM + cpair * 2);
    asm volatile("" : "+v"(op));
#pragma nounroll
    for (unsigned n = 0; n < SPIN_CAP; ++n) {
      pass4<1024>(pg, ms, poff, rws);
      if (!missing4(pg, ms, tag)) {
        float a0 = (__uint_as_float(pg[0][0]) + __uint_as_float(pg[1][0])) + (__uint_as_float(pg[2][0]) + __uint_as_float(pg[3][0]));
        float a1 = (__uint_as_float(pg[0][2]) + __uint_as_float(pg[1][2])) + (__uint_as_float(pg[2][2]) + __uint_as_float(pg[3][2]));
        a0 += dpp<ROR8>(a0); a1 += dpp<ROR8>(a1);
        float o, t;
        t = x16_a(a0, o); a0 = t + o; t = x16_a(a1, o); a1 = t + o;
        t = x32_a(a0, o); a0 = t + o; t = x32_a(a1, o); a1 = t + o;
        if (lane < 8) *op = pk_bf16(a0, a1);
        if (bid == 0 && lane == 0) __hip_atomic_store((GAS unsigned*)ARG(kb, 1), tag, __ATOMIC_RELAXED, __HIP_MEMORY_SCOPE_AGENT);
        break;
      }
    }
  }
}

extern "C" void kernel_launch(void* const* d_in, const int* in_sizes, int n_in, void* d_out, int out_size, void* d_ws, size_t ws_size,
                              hipStream_t stream) {
  fused_kernel<<<256, 1024, 0, stream>>>(d_in[0], d_in[1], d_in[2], d_in[3], d_in[4], d_in[6], d_in[7], d_in[8], d_out, d_ws);
}
```

```cpp
#include <hip/hip_runtime.h>
#include <hip/hip_bf16.h>
#include <stdint.h>

constexpr int CODE_BYTES = 19072;
constexpr int WK0 = 5, WK = 4, WV = 3, WT = 3;
constexpr int VSPLIT = 12, VPER = 3;
#define QSLEEP 10
#define SSLEEP 24
#define FSLEEP 10
constexpr int NPAIR = 6;

typedef unsigned u32x4 __attribute__((ext_vector_type(4)));
typedef unsigned u32x2 __attribute__((ext_vector_type(2)));
typedef float f32x4 __attribute__((ext_vector_type(4)));
typedef float f32x2 __attribute__((ext_vector_type(2)));
typedef short s16x4 __attribute__((ext_vector_type(4)));
typedef __bf16 bf16x2 __attribute__((ext_vector_type(2)));
typedef __bf16 bf16x8 __attribute__((ext_vector_type(8)));
typedef unsigned short u16;
typedef unsigned u32x16 __attribute__((ext_vector_type(16)));
typedef unsigned long long u64;
#define LAS __attribute__((address_space(3)))
#define GAS __attribute__((address_space(1)))

constexpr int NB = 8, DM = 448, HKV = 8, SK = 4096, DH = 256, WIN = 1024, FF = 1792, NQ = 4096;
constexpr int QPSTR = 4096 + 256, QSTR = 2048 + 128;
constexpr int WS_G = 4096;
constexpr int WS_QP = 16384;
constexpr int WS_Q = WS_QP + 256 * QPSTR;
constexpr int WS_HB = WS_Q + 64 * QSTR;
constexpr int WS_S = WS_HB + 65536;
constexpr int WS_PR = (WS_S + 524288 + 16383) & ~16383;
constexpr int WS_OB = 2 << 20;
constexpr int WS_PO = WS_OB + 16 * 16384;
constexpr int WS_END = WS_PO + 112 * 16384;
static_assert(WS_PR + 8 * 16384 <= WS_OB && WS_END == 4 << 20, "two 2 MB pages");
__device__ __forceinline__ int inbox(int k) { return (k >> 1) * 16384 + (k & 1) * 4096; }
constexpr int L_FLAG = 0, L_Q = 256, L_QO = L_Q + 1024, L_ST = L_QO + 512, L_FW = L_ST + 128, L_OM = L_FW + 64, L_SM = L_OM + 512, L_P = L_SM + 2304, L_OP = L_P + 4096, L_WO = L_OP + 4096, L_W2 = L_WO + 16384,
              L_X = L_W2 + 8192, L_WQ = L_X + 2048, L_T = L_WQ + 14336, L_XA = L_T + 28672, L_END = L_XA + 8192;
enum { F_TAGV = 0, F_TAG = 1, F_QRDY = 2, F_WQC = 3, F_QPC = 4, F_KISS = 5, F_OCNT = 6, F_TCNT = 7, F_VISS = 8, F_SCNT = 9, F_FRDY = 10, F_KCNT = 11, F_OMR = 12, F_G1V = 14, F_G1 = 15, F_PR = 16, F_NRV = 20, F_NR = 21 };
constexpr int AUX_NT = 2, AUX_SC1 = 16;
constexpr float C2 = 0.0625f * 1.4426950408889634f;
constexpr int OOB = 0x7f000000;
constexpr unsigned SPIN_CAP = 1u << 20;

#define RSRC(p, n) __builtin_amdgcn_make_buffer_rsrc((void*)(p), 0, (n), 0x00020000)
#define LD128(r, off, aux) __builtin_amdgcn_raw_buffer_load_b128((r), (off), 0, (aux))
#define ST128(v, r, off, aux) __builtin_amdgcn_raw_buffer_store_b128((v), (r), (off), 0, (aux))
#define SCHED_FENCE() __builtin_amdgcn_sched_barrier(0)
#define MEM_FENCE() asm volatile("" ::: "memory")
template <int N> __device__ __forceinline__ void vmwait() { asm volatile("s_waitcnt vmcnt(%0)" ::"n"(N) : "memory"); }
#define LGKM0() asm volatile("s_waitcnt lgkmcnt(0)" ::: "memory")

__device__ __forceinline__ f32x4 mfma(u32x4 a, u32x4 b, f32x4 c) {
  return __builtin_amdgcn_mfma_f32_16x16x32_bf16(__builtin_bit_cast(bf16x8, a), __builtin_bit_cast(bf16x8, b), c, 0, 0, 0);
}
__device__ __forceinline__ u32x2 trd(LAS unsigned char* p) {
  return __builtin_bit_cast(u32x2, __builtin_amdgcn_ds_read_tr16_b64_v4i16((LAS s16x4*)p));
}
__device__ __forceinline__ unsigned pk_bf16(float lo, float hi) {
  f32x2 v = {lo, hi};
  return __builtin_bit_cast(unsigned, __builtin_convertvector(v, bf16x2));
}
__device__ __forceinline__ float bf_lo(unsigned u) { return __uint_as_float(u << 16); }
__device__ __forceinline__ float bf_hi(unsigned u) { return __uint_as_float(u & 0xffff0000u); }
__device__ __forceinline__ float rbf(float f) { return bf_lo(pk_bf16(f, f)); }
template <int CTRL> __device__ __forceinline__ float dpp(float x) {
  return __builtin_bit_cast(float, __builtin_amdgcn_mov_dpp(__builtin_bit_cast(int, x), CTRL, 0xf, 0xf, true));
}
constexpr int XOR1 = 0xB1, XOR2 = 0x4E, HMIRROR = 0x141, ROR8 = 0x128;
__device__ __forceinline__ float x16_a(float x, float& o) {
  auto s = __builtin_amdgcn_permlane16_swap(__float_as_uint(x), __float_as_uint(x), false, false);
  o = __uint_as_float(s[1]);
  return __uint_as_float(s[0]);
}
__device__ __forceinline__ float x32_a(float x, float& o) {
  auto s = __builtin_amdgcn_permlane32_swap(__float_as_uint(x), __float_as_uint(x), false, false);
  o = __uint_as_float(s[1]);
  return __uint_as_float(s[0]);
}
__device__ __forceinline__ float wave_max(float x) {
  x = fmaxf(x, dpp<XOR1>(x)); x = fmaxf(x, dpp<XOR2>(x)); x = fmaxf(x, dpp<HMIRROR>(x)); x = fmaxf(x, dpp<ROR8>(x));
  float o, a = x16_a(x, o); x = fmaxf(a, o);
  a = x32_a(x, o); return fmaxf(a, o);
}
__device__ __forceinline__ float wave_sum(float x) {
  x += dpp<XOR1>(x); x += dpp<XOR2>(x); x += dpp<HMIRROR>(x); x += dpp<ROR8>(x);
  float o, a = x16_a(x, o); x = a + o;
  a = x32_a(x, o); return a + o;
}
__device__ __forceinline__ void lds_wait_ge(volatile LAS unsigned* f, unsigned want) {
  unsigned n, t, v;
  asm volatile("s_mov_b32 %[n], 0x800000\n"
               "1:\n\tds_read_b32 %[v], %[a]\n\ts_waitcnt lgkmcnt(0)\n\tv_readfirstlane_b32 %[t], %[v]\n\ts_cmp_ge_u32 %[t], %[w]\n\ts_cbranch_scc1 2f\n\t"
               "s_sleep 1\n\ts_sub_u32 %[n], %[n], 1\n\ts_cmp_lg_u32 %[n], 0\n\ts_cbranch_scc1 1b\n"
               "2:"
               : [n] "=&s"(n), [t] "=&s"(t), [v] "=&v"(v) : [a] "v"((unsigned)(uintptr_t)f), [w] "s"(__builtin_amdgcn_readfirstlane(want)) : "memory", "scc");
}
__device__ __forceinline__ unsigned bperm(unsigned v, int srclane) { return __builtin_amdgcn_ds_bpermute(srclane << 2, v); }
__device__ __forceinline__ void lds_inc(LAS unsigned char* L, int f, int lane) {
  if (lane == 0) asm volatile("ds_add_u32 %0, %1" ::"v"((unsigned)(uintptr_t)(L + f * 4)), "v"(1u) : "memory");
}
__device__ __forceinline__ unsigned get_tag(volatile LAS unsigned* flag) {
  lds_wait_ge(flag + 0, 1u);
  return __builtin_amdgcn_readfirstlane(flag[1]);
}
__device__ __forceinline__ void dma16(__amdgpu_buffer_rsrc_t r, int voff, unsigned ldsdst) {
  unsigned keep;
  asm volatile("s_mov_b32 %0, m0\n\ts_mov_b32 m0, %3\n\ts_nop 0\n\tbuffer_load_dwordx4 %1, %2, 0 offen nt lds\n\ts_mov_b32 m0, %0"
               : "=&s"(keep) : "v"(voff), "s"(r), "s"(ldsdst) : "memory");
}
__device__ __forceinline__ void dma16p(__amdgpu_buffer_rsrc_t r, int voff, unsigned ldsdst) {
  unsigned keep;
  asm volatile("s_mov_b32 %0, m0\n\ts_mov_b32 m0, %3\n\ts_nop 0\n\tbuffer_load_dwordx4 %1, %2, 0 offen lds\n\ts_mov_b32 m0, %0"
               : "=&s"(keep) : "v"(voff), "s"(r), "s"(ldsdst) : "memory");
}
template <int N>
__device__ __forceinline__ void gatherm(u32x4 (&a)[N], int (&off)[N], unsigned tag, __amdgpu_buffer_rsrc_t r) {
#pragma nounroll
  for (unsigned n = 0; n < SPIN_CAP; ++n) {
    MEM_FENCE();
    u32x4 v[N];
#pragma unroll
    for (int i = 0; i < N; ++i) v[i] = LD128(r, off[i], AUX_SC1);
    bool all = true;
#pragma unroll
    for (int i = 0; i < N; ++i) {
      const bool mine = off[i] != OOB, ok = v[i][1] == tag && v[i][3] == tag;
      a[i][0] = mine ? v[i][0] : a[i][0];
      a[i][2] = mine ? v[i][2] : a[i][2];
      off[i] = ok ? OOB : off[i];
      all = all && (ok || !mine);
    }
    if (__all(all)) break;
  }
}
template <int STEP>
__device__ __forceinline__ void pass4(u32x4 (&a)[4], const u64 (&m)[4], int off, __amdgpu_buffer_rsrc_t r) {
  asm volatile(
      "s_mov_b64 exec, %[m0]\n\tbuffer_load_dwordx4 %[a0], %[o], %[r], 0 offen sc1\n\t"
      "s_mov_b64 exec, %[m1]\n\tbuffer_load_dwordx4 %[a1], %[o], %[r], 0 offen offset:%c[s1] sc1\n\t"
      "s_mov_b64 exec, %[m2]\n\tbuffer_load_dwordx4 %[a2], %[o], %[r], 0 offen offset:%c[s2] sc1\n\t"
      "s_mov_b64 exec, %[m3]\n\tbuffer_load_dwordx4 %[a3], %[o], %[r], 0 offen offset:%c[s3] sc1\n\t"
      "s_mov_b64 exec, -1\n\ts_waitcnt vmcnt(0)"
      : [a0] "+v"(a[0]), [a1] "+v"(a[1]), [a2] "+v"(a[2]), [a3] "+v"(a[3])
      : [m0] "s"(m[0]), [m1] "s"(m[1]), [m2] "s"(m[2]), [m3] "s"(m[3]), [o] "v"(off), [r] "s"(r), [s1] "i"(STEP), [s2] "i"(2 * STEP), [s3] "i"(3 * STEP)
      : "memory");
}
__device__ __forceinline__ bool missing4(const u32x4 (&a)[4], u64 (&m)[4], unsigned tag) {
#pragma unroll
  for (int i = 0; i < 4; ++i) m[i] &= __builtin_amdgcn_ballot_w64(a[i][1] != tag) | __builtin_amdgcn_ballot_w64(a[i][3] != tag);
  return (m[0] | m[1] | m[2] | m[3]) != 0ull;
}
struct Sel { unsigned lo[4], hi[4]; };
__device__ __forceinline__ Sel make_sel(int lane) {
  Sel s;
  const int j = lane & 7;
#pragma unroll
  for (int t = 0; t < 4; ++t) {
    const bool on = (j >> 1) == t;
    s.lo[t] = on ? ((j & 1) ? 0x01000c0cu : 0x0c0c0100u) : 0x0c0c0c0cu;
    s.hi[t] = on ? ((j & 1) ? 0x03020c0cu : 0x0c0c0302u) : 0x0c0c0c0cu;
  }
  return s;
}
__device__ __forceinline__ u32x4 place(unsigned src, const unsigned (&sel)[4]) {
  u32x4 r;
#pragma unroll
  for (int t = 0; t < 4; ++t) r[t] = __builtin_amdgcn_perm(src, src, sel[t]);
  return r;
}

#define KA0 "s36"
#define KA1 "s37"
#define KA6 "s42"
#define KA7 "s43"
#define KB23 "s[54:55]"
#define RT "s[56:59]"
#define RT01 "s[56:57]"
#define RT0 "s56"
#define RT1 "s57"
#define RT2 "s58"
#define RT3 "s59"
__global__ void __launch_bounds__(1024, 4) fused_kernel(const void*, const void*, const void*, const void*, const void*, const void*, const void*,
                                                     const void*, void*, void*) {
  __shared__ __attribute__((aligned(16))) unsigned char smem[L_END];
  LAS unsigned char* L = (LAS unsigned char*)smem;
  volatile LAS unsigned* flag = (volatile LAS unsigned*)(L + L_FLAG);
  const int bid = blockIdx.x, tid = threadIdx.x, lane = tid & 63;
  u32x16 ka;
  u32x4 kb, rt;
  unsigned epochv = 0, wave, t0, t1, t2, v0, v1;
  asm volatile(
      "8:\n\ts_load_dwordx16 %[ka], %[kp], 0x0\n\ts_load_dwordx4 %[kb], %[kp], 0x40\n\t"
      "v_readfirstlane_b32 %[w], %[tid]\n\ts_lshr_b32 %[w], %[w], 6\n\t"
      "s_cmp_lt_u32 %[w], 8\n\ts_cbranch_scc1 1f\n\t"
      "s_add_u32 %[t0], %[bid], 1\n\ts_and_b32 %[t0], %[t0], 3\n\t"
      "s_lshl_b32 %[t2], %[w], 11\n\ts_add_u32 m0, %[t2], %[lx]\n\t"
      "s_cmp_eq_u32 %[w], 8\n\ts_cbranch_scc1 5f\n\t"
      "v_lshrrev_b32 %[v0], 3, %[lane]\n\tv_and_b32 %[v1], 7, %[lane]\n\tv_lshlrev_b32 %[v0], 13, %[v0]\n\tv_lshl_or_b32 %[v0], %[v1], 4, %[v0]\n\t"
      "s_mul_i32 %[t0], %[t0], 0xe0000\n\ts_lshl_b32 %[t1], %[w], 17\n\ts_add_u32 %[t0], %[t0], %[t1]\n\t"
      "s_lshl_b32 %[t1], %[bid], 4\n\ts_and_b32 %[t1], %[t1], 0xf80\n\ts_add_u32 %[t0], %[t0], %[t1]\n\t"
      "s_lshl_b32 %[t1], %[bid], 10\n\ts_and_b32 %[t1], %[t1], 0x1000\n\ts_add_u32 %[t0], %[t0], %[t1]\n\t"
      "s_sub_u32 %[t0], %[t0], 0x120000\n\ts_add_u32 %[t1], %[t0], 0x10000\n\t"
      "s_mov_b32 " RT2 ", 0x380000\n\ts_mov_b32 " RT3 ", 0x20000\n\t"
      "s_waitcnt lgkmcnt(0)\n\ts_mov_b32 " RT0 ", " KA6 "\n\ts_and_b32 " RT1 ", " KA7 ", 0xffff\n\t"
      "s_branch 6f\n"
      "5:\n\t"
      "v_lshrrev_b32 %[v0], 4, %[lane]\n\tv_and_b32 %[v1], 15, %[lane]\n\tv_min_u32 %[v1], 13, %[v1]\n\tv_mul_u32_u24 %[v0], 0x380, %[v0]\n\tv_lshl_add_u32 %[v0], %[v1], 4, %[v0]\n\t"
      "s_mul_i32 %[t0], %[t0], 0xe0\n\tv_add_u32 %[v0], %[t0], %[v0]\n\ts_mov_b32 %[t0], 0\n\ts_movk_i32 %[t1], 0xe00\n\t"
      "s_movk_i32 " RT2 ", 0x1c00\n\ts_mov_b32 " RT3 ", 0x20000\n\t"
      "s_waitcnt lgkmcnt(0)\n\ts_mov_b32 " RT0 ", " KA0 "\n\ts_and_b32 " RT1 ", " KA1 ", 0xffff\n"
      "6:\n\t"
      "buffer_load_dwordx4 %[v0], " RT ", %[t0] offen nt lds\n\t"
      "s_add_u32 m0, m0, 0x400\n\ts_nop 0\n\tbuffer_load_dwordx4 %[v0], " RT ", %[t1] offen nt lds\n\t"
      "s_barrier\n\ts_branch 4f\n"
      "1:\n\ts_cmp_eq_u32 %[w], 0\n\ts_cbranch_scc1 2f\n\t"
      "s_cmp_lt_u32 %[w], 5\n\ts_cbranch_scc1 4f\n\t"
      "s_cmp_eq_u32 %[w], 7\n\ts_cbranch_scc1 3f\n\t"
      "s_cmp_lt_u32 %[bid], 192\n\ts_cbranch_scc1 4f\n\t"
      "s_waitcnt lgkmcnt(0)\n\ts_barrier\n\ts_branch 4f\n"
      "3:\n\t"
      "v_lshlrev_b32 %[v0], 2, %[lane]\n\tds_write_b32 %[v0], %[e]\n\t"
      "s_getpc_b64 " RT01 "\n7:\n\ts_sub_u32 " RT0 ", " RT0 ", 7b-8b\n\ts_subb_u32 " RT1 ", " RT1 ", 0\n\ts_and_b32 " RT0 ", " RT0 ", 0xffffff00\n\ts_and_b32 " RT1 ", " RT1 ", 0xffff\n\t"
      "s_mov_b32 " RT2 ", %[cl]\n\ts_mov_b32 " RT3 ", 0x20000\n\ts_mov_b32 m0, %[lc]\n\t"
      "s_lshr_b32 %[t0], %[bid], 3\n\ts_lshl_b32 %[t0], %[t0], 10\n\tv_lshlrev_b32 %[v0], 4, %[lane]\n\tv_add_u32 %[v0], %[t0], %[v0]\n\t"
      "buffer_load_dwordx4 %[v0], " RT ", 0 offen lds\n\t"
      "s_waitcnt lgkmcnt(0)\n\ts_barrier\n\ts_branch 4f\n"
      "2:\n\ts_waitcnt lgkmcnt(0)\n\tglobal_load_dword %[e], %[e], " KB23 " sc1\n\ts_barrier\n\ts_waitcnt vmcnt(0)\n"
      "4:"
      : [ka] "={s[36:51]}"(ka), [kb] "={s[52:55]}"(kb), [rt] "={s[56:59]}"(rt), [w] "=&s"(wave), [t0] "=&s"(t0), [t1] "=&s"(t1), [t2] "=&s"(t2),
        [v0] "=&v"(v0), [v1] "=&v"(v1), [e] "+v"(epochv)
      : [kp] "{s[0:1]}"(__builtin_amdgcn_kernarg_segment_ptr()), [bid] "{s2}"(bid), [tid] "v"(tid), [lane] "v"(lane), [lx] "i"(L_X - 8 * 2048),
        [lc] "i"(L_WO), [cl] "i"(CODE_BYTES)
      : "memory", "scc");
#define WINDOW(p) (((p) + 0x3fffffull) & ~0x3fffffull)
#define ARG(v, i) ((u64)v[2 * (i)] | ((u64)v[2 * (i) + 1] << 32))
  const int xc = bid & 7, jj = bid >> 3, kvh = jj & 7, s = jj >> 3, b = (xc - 2 * s) & 7;
  const int nkw = s == 3 ? 4 : 6;
  unsigned tag = 0;

  if (__builtin_expect(wave - 1u < (unsigned)nkw, 1)) {
    const bool four = s == 3 && wave >= 3;
    const int urel = four && wave == 4 ? 16 : (wave - 1) * 6;
    const unsigned hsh = (((unsigned)bid * 8u + wave) * 2654435761u) >> 24;
    unsigned ord = (unsigned)((four ? 0x3210321032ull : 0x543210543210ull) >> (((hsh * (four ? 4u : 6u)) >> 8) * 4u));
    int klo = (lane & 7) * 512 + ((lane >> 3) & 1) * 64 + (lane >> 4) * 16 + (s * 36 + urel) * 4096;
    unsigned kvoff = (unsigned)(((b * HKV + kvh) * SK + (SK - WIN)) * DH * 2);
    unsigned brot = (wave + jj + xc) * 128u;
    u32x16 kak = ka;
    u32x4 kbk = kb;
    asm volatile("s_waitcnt lgkmcnt(0)\n\ts_barrier" : "+{s[36:51]}"(kak), "+{s[52:55]}"(kbk), "+v"(klo), "+s"(kvoff), "+s"(ord), "+s"(brot)::"memory");
    const auto rk = RSRC(ARG(kak, 1) + kvoff, WIN * DH * 2);
    u32x4 r[24];
#pragma unroll
    for (int i = 0; i < 24; ++i) {
      if (i >= WK0) vmwait<WK - 1>();
      r[i] = __builtin_amdgcn_raw_buffer_load_b128(rk, i < 16 || !four ? klo : OOB, ((ord >> (i & ~3)) & 15u) * 4096 + ((brot + i * 128) & 0x180), AUX_NT);
      SCHED_FENCE();
    }
    lds_inc(L, F_KISS, lane);
    const u64 ws = WINDOW(ARG(kbk, 1));
    const auto rws = RSRC(ws, WS_END);
    const int kw = wave - 1, nu = four ? 4 : 6, ulo = s * 36, uhi = s == 3 ? 128 : ulo + 36, u0 = ulo + urel;
    __builtin_amdgcn_s_setprio(2);
    lds_wait_ge(flag + F_QRDY, 1u);
    tag = __builtin_amdgcn_readfirstlane(flag[F_TAG]);
    u32x4 qa[4];
#pragma unroll
    for (int t = 0; t < 4; ++t) qa[t] = *(const LAS u32x4*)(L + L_Q + (lane & 1) * 512 + ((brot + t * 128) & 0x180) + ((lane >> 1) & 1) * 64 + (lane >> 4) * 16);
    float s0 = 0.f, s1 = 0.f;
#pragma unroll
    for (int u = 0; u < 6; ++u) {
      f32x4 c = {0, 0, 0, 0};
#pragma unroll
      for (int t = 0; t < 4; ++t) c = mfma(qa[t], r[4 * u + t], c);
      float c0 = (lane & 8) ? c[2] : c[0], c1 = (lane & 8) ? c[3] : c[1];
      c0 += dpp<ROR8>(c0); c1 += dpp<ROR8>(c1);
      if ((unsigned)(lane >> 3) == ((ord >> (4 * u)) & 15u) && (u < 4 || !four)) { s0 = c0; s1 = c1; }
    }
    const unsigned pk = pk_bf16(s0, s1);
    const unsigned pa = bperm(pk, (2 * lane) & 63), pb = bperm(pk, (2 * lane + 1) & 63);
    if (lane < 4 * nu) {
      ST128((u32x4{pa, tag, pb, tag}), rws, WS_S + (b * 8 + kvh) * 8192 + u0 * 64 + lane * 16, AUX_SC1);
      *(LAS u32x4*)(L + L_SM + urel * 64 + lane * 16) = u32x4{pa, tag, pb, tag};
    }
    MEM_FENCE();
    lds_inc(L, F_KCNT, lane);
    if (wave <= 4) {
      u32x4 sg[2];
      int of[2];
      bool own[2];
#pragma unroll
      for (int i = 0; i < 2; ++i) {
        const int myu = (kw + 4 * i) * 16 + (lane >> 2);
        own[i] = myu >= ulo && myu < uhi;
        of[i] = own[i] ? OOB : WS_S + (b * 8 + kvh) * 8192 + (kw + 4 * i) * 1024 + lane * 16;
      }
      __builtin_amdgcn_s_sleep(SSLEEP);
      gatherm<2>(sg, of, tag, rws);
      lds_wait_ge(flag + F_KCNT, (unsigned)nkw);
#pragma unroll
      for (int i = 0; i < 2; ++i) {
        const int c = kw + 4 * i;
        const u32x4 mv = *(const LAS u32x4*)(L + L_SM + (own[i] ? (c * 16 - ulo) * 64 + lane * 16 : 0));
        const unsigned sa = own[i] ? mv[0] : sg[i][0], sb = own[i] ? mv[2] : sg[i][2];
        const float m0 = wave_max(fmaxf(bf_lo(sa), bf_lo(sb))), m1 = wave_max(fmaxf(bf_hi(sa), bf_hi(sb)));
        const float n0 = m0 == -INFINITY ? 0.f : m0, n1 = m1 == -INFINITY ? 0.f : m1;
        const float ea = __builtin_amdgcn_exp2f((bf_lo(sa) - n0) * C2), eb = __builtin_amdgcn_exp2f((bf_lo(sb) - n0) * C2);
        const float ec = __builtin_amdgcn_exp2f((bf_hi(sa) - n1) * C2), ed = __builtin_amdgcn_exp2f((bf_hi(sb) - n1) * C2);
        *(LAS unsigned*)(L + L_P + c * 512 + lane * 4) = pk_bf16(ea, eb);
        *(LAS unsigned*)(L + L_P + c * 512 + 256 + lane * 4) = pk_bf16(ec, ed);
        const float l0 = wave_sum(ea + eb), l1 = wave_sum(ec + ed);
        if (lane == 0) *(LAS f32x4*)(L + L_ST + c * 16) = f32x4{m0, l0, m1, l1};
      }
      MEM_FENCE();
      lds_inc(L, F_PR + kw, lane);
      lds_inc(L, F_SCNT, lane);
    }
    return;
  }
  const GAS u16* x = (const GAS u16*)ARG(ka, 0);
  const u64 vc = ARG(ka, 2), wo = ARG(ka, 4), w1 = ARG(ka, 5), w2 = ARG(ka, 6), ws = WINDOW(ARG(kb, 1));
  const GAS u16* gw = (const GAS u16*)ARG(ka, 7);
  GAS u16* out = (GAS u16*)ARG(kb, 0);
  const auto rws = RSRC(ws, WS_END);
  const int cb = jj + 32 * (xc >> 2), rb = (xc + 1) & 3;
  const int grp = kvh * 4 + s;
  if (wave >= 8) {
    const unsigned kvoff = (unsigned)(((b * HKV + kvh) * SK + (SK - WIN)) * DH * 2);
    const int w = wave - 8;
    vmwait<0>();
    lds_inc(L, F_WQC, lane);
    tag = get_tag(flag);
    if (w >= 4) {
      __builtin_amdgcn_s_setprio(3);
      const int ct = w - 4;
      const int xrow = ((lane & 7) >> 1) + 4 * (lane & 1);
      LAS unsigned char* xp = L + L_X + xrow * 256 + (lane >> 4) * 16;
      LAS unsigned char* xp2 = L + L_X + xrow * 256 + 192 + (lane >> 4) * 8;
      LAS unsigned char* tb = L + L_WQ + ((lane >> 4) * 8 + ((lane >> 2) & 3)) * 128 + (lane & 3) * 8 + ct * 32;
      LAS unsigned char* tb2 = L + L_WQ + (96 + (lane >> 4) * 4 + ((lane >> 2) & 3)) * 128 + (lane & 3) * 8 + ct * 32;
      const int dest = ((lane >> 4) & 1) * 2 + (lane >> 5);
      const int qo = WS_QP + (cb * 4 + dest) * QPSTR + rb * 1024 + ct * 256 + (lane & 15) * 16;
      lds_wait_ge(flag + F_WQC, 8u);
      const u32x2 pt = trd(tb2), xb = *(const LAS u32x2*)xp2;
      f32x4 acc = mfma(u32x4{xb[0], xb[1], 0u, 0u}, u32x4{pt[0], pt[1], 0u, 0u}, f32x4{0, 0, 0, 0});
#pragma unroll
      for (int ks = 0; ks < 3; ++ks) {
        const u32x2 p0 = trd(tb + ks * 4096), p1 = trd(tb + ks * 4096 + 512);
        acc = mfma(*(const LAS u32x4*)(xp + ks * 64), u32x4{p0[0], p0[1], p1[0], p1[1]}, acc);
      }
      const float q0 = lane >= 32 ? acc[2] : acc[0], q1 = lane >= 32 ? acc[3] : acc[1];
      if (dest != rb) ST128((u32x4{__float_as_uint(q0), tag, __float_as_uint(q1), tag}), rws, qo, AUX_SC1);
      else *(LAS f32x2*)(L + L_QO + (ct * 16 + (lane & 15)) * 8) = f32x2{q0, q1};
      LGKM0();
      lds_inc(L, F_QPC, lane);
      __builtin_amdgcn_s_setprio(0);
    }
    const auto rv = RSRC(vc + kvoff, WIN * DH * 2);
    u32x4 r[16];
    const int trot = w + jj + xc;
    const int voff = (w * 128 + (lane >> 4) * 8 + ((lane >> 3) & 1)) * 512 + s * 128 + (lane & 7) * 16;
    const Sel sel = make_sel(lane);
    lds_wait_ge(flag + F_KISS, (unsigned)nkw);
#define V_ISSUE(i) do { if ((i) >= WV) vmwait<WV - 1>(); \
      r[i] = __builtin_amdgcn_raw_buffer_load_b128(rv, voff + ((i) & 3) * 1024, ((((i) >> 2) + trot) & 3) * 16384, AUX_NT); SCHED_FENCE(); \
      if ((i) == 15) lds_inc(L, F_VISS, lane); } while (0)
#define V_USE(i) do { a0 = mfma(place(pp[(i) >> 2][(i) & 3], sel.lo), r[i], a0); a1 = mfma(place(pp[(i) >> 2][(i) & 3], sel.hi), r[i], a1); } while (0)
#pragma unroll
    for (int i = 0; i <= VSPLIT; ++i) {
      if (i >= 10 && __builtin_amdgcn_readfirstlane(flag[F_PR + (w & 3)]) == 0u) vmwait<WV - 2>();
      V_ISSUE(i);
    }
    u32x4 pp[4];
    f32x4 a0 = {0, 0, 0, 0}, a1 = {0, 0, 0, 0};
#define P_READ() _Pragma("unroll") for (int t = 0; t < 4; ++t) \
      pp[t] = *(const LAS u32x4*)(L + L_P + w * 512 + ((lane >> 3) & 1) * 256 + (((t + trot) & 3) * 32 + (lane >> 4) * 8) * 2)
    float f;
    unsigned wad = (unsigned)(uintptr_t)(L + L_OP) + w * 512 + ((lane >> 5) * 64 + (lane & 7) * 8 + ((lane >> 4) & 1) * 4) * 4;
    unsigned rad = (unsigned)(uintptr_t)(L + L_OP) + (lane >> 5) * 2048 + (lane & 31) * 16;
    int oad = WS_OB + inbox(grp) + b * 512 + lane * 16;
#define F_GET() do { lds_wait_ge(flag + F_FRDY, 1u); f = *(const LAS float*)(L + L_FW + w * 8 + (lane >> 5) * 4); \
      asm volatile("" : "+v"(f), "+v"(wad), "+v"(rad), "+v"(oad)); } while (0)
#define V_LAST(id) do { SCHED_FENCE(); F_GET(); u32x4 la[WV], lb[WV]; \
      _Pragma("unroll") for (int k = 0; k < WV; ++k) { \
        la[k] = place(pp[(16 - WV + k) >> 2][(16 - WV + k) & 3], sel.lo); lb[k] = place(pp[(16 - WV + k) >> 2][(16 - WV + k) & 3], sel.hi); \
        asm volatile("; last " #id : "+v"(la[k]), "+v"(lb[k])); } \
      SCHED_FENCE(); \
      _Pragma("unroll") for (int k = 0; k < WV; ++k) { a0 = mfma(la[k], r[16 - WV + k], a0); a1 = mfma(lb[k], r[16 - WV + k], a1); } \
      asm volatile("; done " #id : "+v"(a0), "+v"(a1)); } while (0)
    if (__builtin_expect(__builtin_amdgcn_readfirstlane(flag[F_PR + (w & 3)]) != 0u, 1)) {
      MEM_FENCE();
      P_READ();
#pragma unroll
      for (int i = VSPLIT + 1; i < 16; ++i) {
        V_ISSUE(i);
#pragma unroll
        for (int k = 0; k < 16; ++k)
          if (k >= (i - VSPLIT - 1) * VPER && k < (i - VSPLIT) * VPER && k <= i - WV) V_USE(k);
        SCHED_FENCE();
      }
#pragma unroll
      for (int k = 0; k < 16 - WV; ++k) {
        bool done = false;
#pragma unroll
        for (int i = VSPLIT + 1; i < 16; ++i) done = done || (k >= (i - VSPLIT - 1) * VPER && k < (i - VSPLIT) * VPER && k <= i - WV);
        if (!done) { V_USE(k); if (k & 1) SCHED_FENCE(); }
      }
      V_LAST(0);
    } else {
#pragma unroll
      for (int i = VSPLIT + 1; i < 16; ++i) V_ISSUE(i);
      lds_wait_ge(flag + F_PR + (w & 3), 1u);
      P_READ();
#pragma unroll
      for (int k = 0; k < 16 - WV; ++k) { V_USE(k); if (k & 1) SCHED_FENCE(); }
      V_LAST(1);
    }
    f32x4 t = (lane & 8) ? a1 : a0;
#pragma unroll
    for (int k = 0; k < 4; ++k) t[k] = (t[k] + dpp<ROR8>(t[k])) * f;
    unsigned old;
    f32x4 o0v, o1v, o2v, o3v;
    asm volatile("ds_write_b128 %[wa], %[t]\n\tds_add_rtn_u32 %[old], %[ca], %[one]\n\t"
                 "ds_read_b128 %[p0], %[ra]\n\tds_read_b128 %[p1], %[ra] offset:512\n\tds_read_b128 %[p2], %[ra] offset:1024\n\tds_read_b128 %[p3], %[ra] offset:1536\n\t"
                 "s_waitcnt lgkmcnt(0)"
                 : [old] "=&v"(old), [p0] "=&v"(o0v), [p1] "=&v"(o1v), [p2] "=&v"(o2v), [p3] "=&v"(o3v)
                 : [wa] "v"(wad), [t] "v"(t), [ca] "v"((unsigned)(uintptr_t)(L + (lane == 0 ? F_OCNT * 4 : 128 + (lane & 31) * 4))), [one] "v"(1u), [ra] "v"(rad)
                 : "memory");
    if (__builtin_amdgcn_readfirstlane(old) == 7u) {
      f32x4 ov = (o0v + o1v) + (o2v + o3v);
#pragma unroll
      for (int k = 0; k < 4; ++k) { float hi, lo = x32_a(ov[k], hi); ov[k] = lo + hi; }
      if (lane < 32) {
        const u32x4 ogr = {pk_bf16(ov[0], ov[1]), tag, pk_bf16(ov[2], ov[3]), tag};
        ST128(ogr, rws, oad, AUX_SC1);
        ST128(ogr, rws, oad + 8192, AUX_SC1);
        *(LAS u32x4*)(L + L_OM + lane * 16) = ogr;
      }
      MEM_FENCE();
      lds_inc(L, F_OMR, lane);
    }
  } else if (wave == 0) {
    __builtin_amdgcn_s_setprio(3);
    tag = __builtin_amdgcn_readfirstlane(epochv) * 1664525u + 1013904223u;
    if (lane == 0) *(volatile LAS u32x2*)flag = u32x2{1u, tag};
    lds_wait_ge(flag + F_QPC, 4u);
    __builtin_amdgcn_s_sleep(QSLEEP);
    {
      const int po = WS_QP + (cb * 4 + rb) * QPSTR + lane * 16;
      u32x4 pg[3];
      int of[3] = {po + ((rb + 1) & 3) * 1024, po + ((rb + 2) & 3) * 1024, po + ((rb + 3) & 3) * 1024};
      gatherm<3>(pg, of, tag, rws);
      const f32x2 own = *(const LAS f32x2*)(L + L_QO + lane * 8);
      const float a0 = (own[0] + __uint_as_float(pg[0][0])) + (__uint_as_float(pg[1][0]) + __uint_as_float(pg[2][0]));
      const float a1 = (own[1] + __uint_as_float(pg[0][2])) + (__uint_as_float(pg[1][2]) + __uint_as_float(pg[2][2]));
      const float n0 = dpp<XOR1>(a0), n1 = dpp<XOR1>(a1);
      const unsigned pk = (lane & 1) ? pk_bf16(n1, a1) : pk_bf16(a0, n0);
      const int src = (lane & 15) * 4 + ((lane >> 4) & 1);
      const unsigned pa = bperm(pk, src), pb = bperm(pk, src + 2);
      if (lane < 32) ST128((u32x4{pa, tag, pb, tag}), rws, WS_Q + ((cb >> 3) * 8 + rb + 4 * (lane >> 4)) * QSTR + (cb & 7) * 256 + (lane & 15) * 16, AUX_SC1);
    }
    __builtin_amdgcn_s_sleep(QSLEEP);
    {
      const int qoff = WS_Q + (kvh * 8 + b) * QSTR + lane * 16;
      u32x4 qq[2];
      int of[2] = {qoff, qoff + 1024};
      gatherm<2>(qq, of, tag, rws);
      *(LAS u32x2*)(L + L_Q + lane * 8) = u32x2{qq[0][0], qq[0][2]};
      *(LAS u32x2*)(L + L_Q + 512 + lane * 8) = u32x2{qq[1][0], qq[1][2]};
      LGKM0();
      if (lane == 0) flag[F_QRDY] = 1u;
    }
    u32x4 xg = {0, 0, 0, 0}, gg = {0, 0, 0, 0};
    if (lane < 56) {
      xg = *(const GAS u32x4*)(x + b * DM + lane * 8);
      gg = *(const GAS u32x4*)(gw + lane * 8);
    }
    float z = 0.f;
#pragma unroll
    for (int t = 0; t < 4; ++t) z += rbf(bf_lo(xg[t]) * bf_lo(gg[t])) + rbf(bf_hi(xg[t]) * bf_hi(gg[t]));
    z = rbf(wave_sum(z));
    const float g = rbf(1.f / (1.f + __expf(-z)));
    const unsigned g1u = __float_as_uint(rbf(1.f - g));
    if (lane == 0) *(volatile LAS u32x2*)(flag + F_G1V) = u32x2{1u, g1u};
    lds_wait_ge(flag + F_SCNT, 4u);
    {
      const f32x2 ml = *(const LAS f32x2*)(L + L_ST + (lane & 15) * 8);
      float M = fmaxf(ml[0], dpp<XOR2>(ml[0]));
      M = fmaxf(M, dpp<0x124>(M)); M = fmaxf(M, dpp<ROR8>(M));
      const float al = __builtin_amdgcn_exp2f((ml[0] - M) * C2);
      float Ls = al * ml[1];
      Ls += dpp<XOR2>(Ls); Ls += dpp<0x124>(Ls); Ls += dpp<ROR8>(Ls);
      if (lane < 16) *(LAS float*)(L + L_FW + lane * 4) = al * (g / Ls);
      LGKM0();
      if (lane == 0) flag[F_FRDY] = 1u;
    }
  } else if (wave != 7 && xc < 7) {
    const int j = wave - 5, t = xc * 8 + kvh;
    const unsigned lb = (unsigned)(uintptr_t)(L + L_T) + j * 14336;
    const auto r1 = RSRC(w1, DM * FF * 2);
    const int o = ((j * 224 + (lane >> 2)) * FF + t * 32 + (lane & 3) * 8) * 2;
#pragma unroll
    for (int i = 0; i < 14; ++i) {
      if (i >= WT) vmwait<WT - 1>();
      dma16(r1, o + i * 16 * FF * 2, lb + i * 1024);
    }
    {
      const auto rx = RSRC((u64)x, NB * DM * 2);
#pragma unroll
      for (int i = 0; i < 4; ++i) {
        vmwait<WT - 1>();
        dma16p(rx, (j * 4 + i) * 1024 + lane * 16, (unsigned)(uintptr_t)(L + L_XA) + (j * 4 + i) * 1024);
      }
    }
    tag = get_tag(flag);
    vmwait<0>();
    lds_inc(L, F_TCNT, lane);
    lds_wait_ge(flag + F_TCNT, 2u);
    LAS unsigned char* tb = L + L_T + ((lane >> 4) * 8 + ((lane >> 2) & 3)) * 64 + j * 32 + (lane & 3) * 8;
    f32x4 acc0 = {0, 0, 0, 0}, acc1 = {0, 0, 0, 0};
#pragma unroll
    for (int ks = 0; ks < 14; ++ks) {
      const u32x2 p0 = trd(tb + ks * 2048), p1 = trd(tb + ks * 2048 + 256);
      const u32x4 xa = *(const LAS u32x4*)(L + L_XA + (lane & 7) * (DM * 2) + ks * 64 + (lane >> 4) * 16);
      if (ks & 1) acc1 = mfma(u32x4{p0[0], p0[1], p1[0], p1[1]}, xa, acc1);
      else acc0 = mfma(u32x4{p0[0], p0[1], p1[0], p1[1]}, xa, acc0);
    }
    acc0 += acc1;
    float hv[4];
#pragma unroll
    for (int k = 0; k < 4; ++k) {
      const float v = rbf(acc0[k]);
      hv[k] = v / (1.f + __expf(-v));
    }
    if (!(lane & 8))
      ST128((u32x4{pk_bf16(hv[0], hv[1]), tag, pk_bf16(hv[2], hv[3]), tag}), rws, WS_HB + t * 1024 + j * 512 + (lane >> 4) * 128 + (lane & 7) * 16, AUX_SC1);
  }
  const int cpair = (xc ^ 1) * 32 + jj;
  if (wave == 7) {
    const int fgrp = b == 7 ? -1 : s < 3 ? kvh * 3 + s : (kvh < 4 ? 24 + kvh : -1);
    tag = get_tag(flag);
    const bool hi8 = lane & 8;
    const unsigned upper = (__builtin_amdgcn_s_getreg(20 | (3 << 11)) >> 2) & 1u;
    if (s == 0) {
      const u64 pa = ws + WS_PR + kvh * 16384 + b * 1024;
      int vote = 0;
#pragma unroll
      for (int i = 0; i < NPAIR; ++i) {
        u64 t0, t1, t2;
        unsigned d;
        asm volatile("s_memtime %0\n\ts_waitcnt lgkmcnt(0)\n\ts_load_dword %3, %4, %5\n\ts_waitcnt lgkmcnt(0)\n\ts_memtime %1\n\ts_waitcnt lgkmcnt(0)\n\t"
                     "s_load_dword %3, %4, %6\n\ts_waitcnt lgkmcnt(0)\n\ts_memtime %2\n\ts_waitcnt lgkmcnt(0)"
                     : "=&s"(t0), "=&s"(t1), "=&s"(t2), "=&s"(d) : "s"(pa), "i"(i * 128), "i"(i * 128 + 8192) : "memory");
        vote += (unsigned)(t1 - t0) > (unsigned)(t2 - t1) ? 1 : -1;
      }
      lds_wait_ge(flag + F_G1V, 1u);
      if (lane < 8) ST128((u32x4{flag[F_G1], tag, (unsigned)(upper ? -vote : vote), tag}), rws, WS_G + (kvh * 8 + b) * 128 + lane * 16, AUX_SC1);
    }
    u32x4 hb[3];
    int nearc;
    {
      const unsigned lb = (unsigned)(uintptr_t)(L + L_WO);
      const auto r2 = RSRC(w2, FF * DM * 2);
      const auto ro = RSRC(wo, NQ * DM * 2);
      const int off2 = (fgrp * 64 + (lane >> 3)) * 896 + b * 128 + (lane & 7) * 16;
      const int offo = (kvh * 512 + s * 64 + (lane >> 3)) * 896 + b * 128 + (lane & 7) * 16;
      lds_wait_ge(flag + F_SCNT, 2u);
      {
        int of[3] = {fgrp >= 0 ? WS_HB + fgrp * 2048 + lane * 16 : OOB, fgrp >= 0 ? WS_HB + fgrp * 2048 + 1024 + lane * 16 : OOB, WS_G + (kvh * 8 + (lane & 7)) * 128};
        gatherm<3>(hb, of, tag, rws);
        float vs = (float)(int)hb[2][2];
        vs += dpp<XOR1>(vs); vs += dpp<XOR2>(vs); vs += dpp<HMIRROR>(vs);
        nearc = (((__builtin_amdgcn_readfirstlane(__float_as_uint(vs)) >> 31) ^ upper) & 1u) * 8192;
        if (lane == 0) *(volatile LAS u32x2*)(flag + F_NRV) = u32x2{1u, (unsigned)nearc};
      }
      if (b == 7) return;
      lds_wait_ge(flag + F_VISS, 8u);
      if (fgrp >= 0) {
#pragma unroll
        for (int i = 0; i < 8; ++i) dma16(r2, off2 + i * 8 * 896, lb + 16384 + i * 1024);
      }
#pragma unroll
      for (int i = 0; i < 16; ++i) dma16(ro, offo + ((i >> 3) * 256 + (i & 7) * 8) * 896, lb + i * 1024);
    }
    __builtin_amdgcn_s_setprio(3);
    const bool ownb = (lane & 7) == b;
    const int ooff = WS_OB + nearc + inbox(grp) + (lane & 7) * 512 + (lane >> 3) * 16;
    u32x4 ob[4];
#pragma unroll
    for (int i = 0; i < 4; ++i) ob[i] = LD128(rws, ownb ? OOB : ooff + i * 128, AUX_SC1);
    SCHED_FENCE();
    LAS unsigned char* tb = L + L_WO + ((lane >> 4) * 8 + ((lane >> 2) & 3)) * 128 + (lane & 3) * 8;
    f32x4 acc[4] = {{0, 0, 0, 0}, {0, 0, 0, 0}, {0, 0, 0, 0}, {0, 0, 0, 0}};
    if (fgrp >= 0) {
      vmwait<20>();
      const u32x4 hv = {hb[0][0], hb[0][2], hb[1][0], hb[1][2]};
      const float g1 = __uint_as_float(hb[2][0]);
#pragma unroll
      for (int ks = 0; ks < 2; ++ks) {
        const u32x4 bf = {hi8 ? 0u : hv[2 * ks], hi8 ? 0u : hv[2 * ks + 1], hi8 ? hv[2 * ks] : 0u, hi8 ? hv[2 * ks + 1] : 0u};
#pragma unroll
        for (int ct = 0; ct < 4; ++ct) {
          const u32x2 p0 = trd(tb + 16384 + ks * 4096 + ct * 32), p1 = trd(tb + 16384 + ks * 4096 + 512 + ct * 32);
          acc[ct] = mfma(u32x4{p0[0], p0[1], p1[0], p1[1]}, bf, acc[ct]);
        }
      }
#pragma unroll
      for (int ct = 0; ct < 4; ++ct) acc[ct] *= g1;
    }
    u32x4 wf[4][4];
#pragma unroll
    for (int ks = 0; ks < 4; ++ks) {
      if (ks == 0) vmwait<16>(); else if (ks == 1) vmwait<12>(); else if (ks == 2) vmwait<8>(); else vmwait<4>();
#pragma unroll
      for (int ct = 0; ct < 4; ++ct) {
        const u32x2 p0 = trd(tb + ks * 4096 + ct * 32), p1 = trd(tb + ks * 4096 + 512 + ct * 32);
        wf[ks][ct] = u32x4{p0[0], p0[1], p1[0], p1[1]};
      }
    }
    int po = WS_PO + (nearc ^ (((xc ^ b) >> 2) & 1) * 8192) + inbox(b * 32 + (lane >> 3)) + grp * 128 + (lane & 7) * 16;
    asm volatile("" : "+v"(po));
    lds_wait_ge(flag + F_OMR, 1u);
#pragma unroll
    for (int i = 0; i < 4; ++i) {
      const u32x4 v = *(const LAS u32x4*)(L + L_OM + i * 128 + (lane >> 3) * 16);
      ob[i] = ownb ? v : ob[i];
    }
    u64 ms[4] = {~0ull, ~0ull, ~0ull, ~0ull};
    if (missing4(ob, ms, tag)) {
#pragma nounroll
      for (unsigned n = 0; n < SPIN_CAP; ++n) {
        pass4<128>(ob, ms, ooff, rws);
        if (!missing4(ob, ms, tag)) break;
      }
    }
    u32x4 bf[4];
#pragma unroll
    for (int ks = 0; ks < 4; ++ks) bf[ks] = u32x4{hi8 ? 0u : ob[ks][0], hi8 ? 0u : ob[ks][2], hi8 ? ob[ks][0] : 0u, hi8 ? ob[ks][2] : 0u};
#pragma unroll
    for (int ct = 0; ct < 4; ++ct)
#pragma unroll
      for (int ks = 0; ks < 4; ++ks) acc[ct] = mfma(wf[ks][ct], bf[ks], acc[ct]);
    u32x4 pv[4];
#pragma unroll
    for (int ct = 0; ct < 4; ++ct) {
      const float k0 = hi8 ? acc[ct][2] : acc[ct][0], k1 = hi8 ? acc[ct][3] : acc[ct][1];
      const float s0 = hi8 ? acc[ct][0] : acc[ct][2], s1 = hi8 ? acc[ct][1] : acc[ct][3];
      pv[ct] = u32x4{__float_as_uint(k0 + dpp<ROR8>(s0)), tag, __float_as_uint(k1 + dpp<ROR8>(s1)), tag};
      __builtin_amdgcn_raw_buffer_store_b128(pv[ct], rws, po, ct * 4 * 16384, AUX_SC1);
    }
    po ^= 8192;
#pragma unroll
    for (int ct = 0; ct < 4; ++ct) __builtin_amdgcn_raw_buffer_store_b128(pv[ct], rws, po, ct * 4 * 16384, AUX_SC1);
  } else if (wave == 0 && cpair < 224) {
    __builtin_amdgcn_s_setprio(3);
    lds_wait_ge(flag + F_OCNT, 8u);
    __builtin_amdgcn_s_sleep(FSLEEP);
    lds_wait_ge(flag + F_NRV, 1u);
    const int poff = WS_PO + __builtin_amdgcn_readfirstlane(flag[F_NR]) + inbox(cpair) + lane * 16;
    u32x4 pg[4] = {{0, 0, 0, 0}, {0, 0, 0, 0}, {0, 0, 0, 0}, {0, 0, 0, 0}};
    u64 ms[4] = {~0ull, ~0ull, ~0ull, ~0ull};
    GAS unsigned* op = (GAS unsigned*)(out + (lane & 7) * DM + cpair * 2);
    asm volatile("" : "+v"(op));
#pragma nounroll
    for (unsigned n = 0; n < SPIN_CAP; ++n) {
      pass4<1024>(pg, ms, poff, rws);
      if (!missing4(pg, ms, tag)) {
        float a0 = (__uint_as_float(pg[0][0]) + __uint_as_float(pg[1][0])) + (__uint_as_float(pg[2][0]) + __uint_as_float(pg[3][0]));
        float a1 = (__uint_as_float(pg[0][2]) + __uint_as_float(pg[1][2])) + (__uint_as_float(pg[2][2]) + __uint_as_float(pg[3][2]));
        a0 += dpp<ROR8>(a0); a1 += dpp<ROR8>(a1);
        float o, t;
        t = x16_a(a0, o); a0 = t + o; t = x16_a(a1, o); a1 = t + o;
        t = x32_a(a0, o); a0 = t + o; t = x32_a(a1, o); a1 = t + o;
        if (lane < 8) *op = pk_bf16(a0, a1);
        if (bid == 0 && lane == 0) __hip_atomic_store((GAS unsigned*)ARG(kb, 1), tag, __ATOMIC_RELAXED, __HIP_MEMORY_SCOPE_AGENT);
        break;
      }
    }
  }
}

extern "C" void kernel_launch(void* const* d_in, const int* in_sizes, int n_in, void* d_out, int out_size, void* d_ws, size_t ws_size,
                              hipStream_t stream) {
  if (n_in != 9 || out_size != NB * DM || ws_size < (8u << 20)) return;
  fused_kernel<<<256, 1024, 0, stream>>>(d_in[0], d_in[1], d_in[2], d_in[3], d_in[4], d_in[6], d_in[7], d_in[8], d_out, d_ws);
}
```
